```python
import torch

def _softmax(x, axis=-1):
    return torch.softmax(x, dim=axis)

def _gelu(x):
    return torch.nn.functional.gelu(x, approximate='tanh')

def _silu(x):
    return torch.nn.functional.silu(x)

def _sigmoid(x):
    return torch.sigmoid(x)

def _layernorm(x, axis=-1, eps=1e-05):
    m = x.mean(dim=axis, keepdim=True)
    v = x.var(dim=axis, keepdim=True, unbiased=False)
    return (x - m) / torch.sqrt(v + eps)

def _scatter_add(tgt, idx, src, axis=0):
    return tgt.clone().index_add_(axis, idx, src)

def _segment_sum(src, seg, n):
    out = torch.zeros((n,) + src.shape[1:], dtype=src.dtype)
    return out.index_add_(0, seg, src)

def _pad(x, pw):
    flat = tuple((v for pair in reversed(pw) for v in pair))
    return torch.nn.functional.pad(x, flat)
kernel_name = 'parallel_sum__softmax_full_mha__mixer_gmlp__prompt'

def setup_inputs():

    _w = lambda a: (lambda t: t.to(torch.bfloat16) if t.is_floating_point() else t)(torch.as_tensor(a))

    def _A_setup():
        B, S, D = (1, 2048, 448)
        return {'x': _w((lambda *s: torch.randn(*s, dtype=torch.bfloat16) * 0.05)(B, S, D)), 'wqkv': _w((lambda *s: torch.randn(*s, dtype=torch.bfloat16) * 0.2)(D, 3 * D)), 'wo': _w((lambda *s: torch.randn(*s, dtype=torch.bfloat16) * 0.1)(D, D))}
    _A_in = _A_setup()

    def _B_setup():
        B, S, D, F = (1, 2048, 448, 1792)
        Fh = F // 2
        return {'x': _w((lambda *s: torch.randn(*s, dtype=torch.bfloat16) * 0.05)(B, S, D)), 'w_in': _w((lambda *s: torch.randn(*s, dtype=torch.bfloat16) * 0.4)(D, 2 * Fh)), 'w_sp': _w(0.01 * (lambda *s: torch.randn(*s, dtype=torch.bfloat16) * 0.4)(S, S)), 'b_sp': _w(1.0 + 0.01 * (lambda *s: torch.randn(*s, dtype=torch.bfloat16) * 0.2)(S)), 'w_out': _w((lambda *s: torch.randn(*s, dtype=torch.bfloat16) * 0.2)(Fh, D))}
    _B_in = _B_setup()
    return {'A_x': _A_in['x'], 'A_wqkv': _A_in['wqkv'], 'A_wo': _A_in['wo'], 'B_x': _B_in['x'], 'B_w_in': _B_in['w_in'], 'B_w_sp': _B_in['w_sp'], 'B_b_sp': _B_in['b_sp'], 'B_w_out': _B_in['w_out']}

def reference(A_x, A_wqkv, A_wo, B_x, B_w_in, B_w_sp, B_b_sp, B_w_out):

    def _A_ref(x, wqkv, wo):
        B, S, D = x.shape
        H = 16
        Dh = D // H
        qkv = (lambda x, *d: x.permute(*d))((x @ wqkv).reshape(B, S, 3, H, Dh), 2, 0, 3, 1, 4)
        q, k, v = (qkv[0], qkv[1], qkv[2])
        scale = Dh ** (-0.5)
        scores = torch.einsum('bhqd,bhkd->bhqk', q, k) * scale
        p = _softmax(scores, axis=-1)
        o = torch.einsum('bhqk,bhkd->bhqd', p, v)
        return (lambda x, *d: x.permute(*d))(o, 0, 2, 1, 3).reshape(B, S, D) @ wo

    def _B_ref(x, w_in, w_sp, b_sp, w_out):
        B, S, D = x.shape
        F = w_in.shape[-1]
        Fh = F // 2
        z = _gelu(torch.einsum('bsd,df->bsf', x, w_in))
        z1 = z[..., :Fh].clone()
        z2 = _layernorm(z[..., Fh:], axis=-1)
        g = torch.einsum('ts,bsf->btf', w_sp, z2) + b_sp[None, :, None]
        return torch.einsum('bsf,fd->bsd', z1 * g, w_out)
    x = A_x
    xn = _layernorm(x, axis=-1)
    return x + _A_ref(xn, A_wqkv, A_wo) + _B_ref(xn, B_w_in, B_w_sp, B_b_sp, B_w_out)
```

```cpp
#include <hip/hip_runtime.h>
#include <hip/hip_bf16.h>
typedef __hip_bfloat16 bf16;
constexpr int S=2048, D=448, H=16, DH=28, F=1792, FH=896;
__device__ __forceinline__ float b2f(bf16 x){ return __bfloat162float(x); }
__device__ __forceinline__ bf16 f2b(float x){ return __float2bfloat16(x); }

__global__ void k_ln(const bf16* x, bf16* y, int n, int ldx, int ldy){
  int row=blockIdx.x; const bf16* p=x+(size_t)row*ldx;
  __shared__ float red[256];
  float s=0; for(int i=threadIdx.x;i<n;i+=256) s+=b2f(p[i]);
  red[threadIdx.x]=s; __syncthreads();
  for(int o=128;o>0;o>>=1){ if(threadIdx.x<o) red[threadIdx.x]+=red[threadIdx.x+o]; __syncthreads(); }
  float m=red[0]/n; __syncthreads();
  float v=0; for(int i=threadIdx.x;i<n;i+=256){ float d=b2f(p[i])-m; v+=d*d; }
  red[threadIdx.x]=v; __syncthreads();
  for(int o=128;o>0;o>>=1){ if(threadIdx.x<o) red[threadIdx.x]+=red[threadIdx.x+o]; __syncthreads(); }
  float r=rsqrtf(red[0]/n+1e-5f);
  for(int i=threadIdx.x;i<n;i+=256) y[(size_t)row*ldy+i]=f2b((b2f(p[i])-m)*r);
}
__global__ void k_gemm(const bf16* A,int lda,const bf16* B,int ldb,bf16* C,int ldc,int M,int N,int K,int mode,const bf16* bias,const bf16* z1,int ldz){
  int n=blockIdx.x*64+threadIdx.x; int m=blockIdx.y*4+threadIdx.y;
  if(n>=N) return;
  float acc=0;
  for(int k=0;k<K;k++) acc+=b2f(A[(size_t)m*lda+k])*b2f(B[(size_t)k*ldb+n]);
  if(mode==1){ float u=0.7978845608028654f*(acc+0.044715f*acc*acc*acc); acc=0.5f*acc*(1.f+tanhf(u)); }
  if(mode==2){ acc=(acc+b2f(bias[m]))*b2f(z1[(size_t)m*ldz+n]); }
  C[(size_t)m*ldc+n]=f2b(acc);
}
__global__ void k_attn(const bf16* qkv, bf16* o, int ldo){
  int q=blockIdx.x, h=blockIdx.y; __shared__ float sc[S]; __shared__ float red[256]; __shared__ float qs[DH];
  if(threadIdx.x<DH) qs[threadIdx.x]=b2f(qkv[(size_t)q*3*D+h*DH+threadIdx.x]);
  __syncthreads();
  float mx=-1e30f;
  for(int k=threadIdx.x;k<S;k+=256){ float s=0; for(int d=0;d<DH;d++) s+=qs[d]*b2f(qkv[(size_t)k*3*D+D+h*DH+d]); s*=0.1889822365046136f; sc[k]=s; mx=fmaxf(mx,s);}
  red[threadIdx.x]=mx; __syncthreads();
  for(int o2=128;o2>0;o2>>=1){ if(threadIdx.x<o2) red[threadIdx.x]=fmaxf(red[threadIdx.x],red[threadIdx.x+o2]); __syncthreads(); }
  mx=red[0]; __syncthreads();
  float l=0; for(int k=threadIdx.x;k<S;k+=256){ float e=__expf(sc[k]-mx); sc[k]=e; l+=e; }
  red[threadIdx.x]=l; __syncthreads();
  for(int o2=128;o2>0;o2>>=1){ if(threadIdx.x<o2) red[threadIdx.x]+=red[threadIdx.x+o2]; __syncthreads(); }
  l=red[0];
  if(threadIdx.x<DH){ float a=0; for(int k=0;k<S;k++) a+=sc[k]*b2f(qkv[(size_t)k*3*D+2*D+h*DH+threadIdx.x]); o[(size_t)q*ldo+h*DH+threadIdx.x]=f2b(a/l); }
}
__global__ void k_out(const bf16* x,const bf16* oh,const bf16* wo,const bf16* wout,bf16* out){
  int n=blockIdx.x*64+threadIdx.x; int m=blockIdx.y*4+threadIdx.y;
  float a=0,b=0;
  for(int k=0;k<D;k++) a+=b2f(oh[(size_t)m*1344+k])*b2f(wo[(size_t)k*D+n]);
  for(int k=0;k<FH;k++) b+=b2f(oh[(size_t)m*1344+D+k])*b2f(wout[(size_t)k*D+n]);
  out[(size_t)m*D+n]=f2b(b2f(x[(size_t)m*D+n])+a+b);
}
extern "C" void kernel_launch(void* const* d_in, const int* in_sizes, int n_in, void* d_out, int out_size, void* d_ws, size_t ws_size, hipStream_t stream){
  const bf16* x=(const bf16*)d_in[0]; const bf16* wqkv=(const bf16*)d_in[1]; const bf16* wo=(const bf16*)d_in[2];
  const bf16* win=(const bf16*)d_in[4]; const bf16* wsp=(const bf16*)d_in[5]; const bf16* bsp=(const bf16*)d_in[6]; const bf16* wout=(const bf16*)d_in[7];
  bf16* ws=(bf16*)d_ws;
  bf16* xn=ws; bf16* qkv=xn+S*D; bf16* z=qkv+S*3*D; bf16* z2n=z+S*F; bf16* oh=z2n+S*FH;
  k_ln<<<S,256,0,stream>>>(x,xn,D,D,D);
  k_gemm<<<dim3(1344/64,S/4),dim3(64,4),0,stream>>>(xn,D,wqkv,1344,qkv,1344,S,1344,D,0,nullptr,nullptr,0);
  k_gemm<<<dim3(F/64,S/4),dim3(64,4),0,stream>>>(xn,D,win,F,z,F,S,F,D,1,nullptr,nullptr,0);
  k_ln<<<S,256,0,stream>>>(z+FH,z2n,FH,F,FH);
  k_attn<<<dim3(S,H),256,0,stream>>>(qkv,oh,1344);
  k_gemm<<<dim3(FH/64,S/4),dim3(64,4),0,stream>>>(wsp,S,z2n,FH,oh+D,1344,S,FH,S,2,bsp,z,F);
  k_out<<<dim3(D/64,S/4),dim3(64,4),0,stream>>>(x,oh,wo,wout,(bf16*)d_out);
}
```

```cpp
#include <hip/hip_runtime.h>
#include <hip/hip_bf16.h>
#include <stdint.h>

typedef short bf16x8 __attribute__((ext_vector_type(8)));
typedef short s16x4 __attribute__((ext_vector_type(4)));
typedef float f32x16 __attribute__((ext_vector_type(16)));
typedef float f32x4 __attribute__((ext_vector_type(4)));
typedef float f32x2 __attribute__((ext_vector_type(2)));
typedef unsigned u32x4 __attribute__((ext_vector_type(4)));
typedef unsigned u32x2 __attribute__((ext_vector_type(2)));
typedef __bf16 bf2 __attribute__((ext_vector_type(2)));
#define AS3 __attribute__((address_space(3)))
typedef AS3 char* lptr;
#define DI __device__ __forceinline__
#define SB() __builtin_amdgcn_sched_barrier(0)
#define PIN(x) asm volatile("" : "+v"(x))
#define MFMA32(a, b, c) __builtin_amdgcn_mfma_f32_32x32x16_bf16((a), (b), (c), 0, 0, 0)
#define MFMA16(a, b, c) __builtin_amdgcn_mfma_f32_16x16x32_bf16((a), (b), (c), 0, 0, 0)

constexpr size_t WS_QF = 0;
constexpr size_t WS_KVF = 2u << 20;
constexpr size_t WS_ZF = 6u << 20;
constexpr size_t WS_Z2N = 14u << 20;
constexpr size_t WS_AF = 18u << 20;
constexpr size_t WS_BF = 24u << 20;
constexpr size_t WS_ST = 26u << 20;
constexpr size_t WS_FLG = 27u << 20;
constexpr size_t WS_DBG = 28u << 20;

#define TS(k, slot) do {} while (0)
#define TDECL do {} while (0)
#define TMARK(i) do {} while (0)
#define TFLUSH(k) do {} while (0)

DI unsigned pk2(float a, float b) { f32x2 v = {a, b}; return __builtin_bit_cast(unsigned, __builtin_convertvector(v, bf2)); }
DI float bflo(unsigned u) { return __uint_as_float(u << 16); }
DI float bfhi(unsigned u) { return __uint_as_float(u & 0xffff0000u); }
DI float dot2(unsigned a, unsigned b, float c) { return __builtin_amdgcn_fdot2_f32_bf16(__builtin_bit_cast(bf2, a), __builtin_bit_cast(bf2, b), c, false); }
DI float halfsum(float v) { auto r = __builtin_amdgcn_permlane32_swap(__float_as_uint(v), __float_as_uint(v), false, false); return __uint_as_float(r[0]) + __uint_as_float(r[1]); }
DI float halfmax(float v) { auto r = __builtin_amdgcn_permlane32_swap(__float_as_uint(v), __float_as_uint(v), false, false); return fmaxf(__uint_as_float(r[0]), __uint_as_float(r[1])); }
DI float hihalf(float v) { auto r = __builtin_amdgcn_permlane32_swap(__float_as_uint(v), __float_as_uint(v), false, false); return __uint_as_float(r[1]); }
DI s16x4 trread(lptr p) { return __builtin_bit_cast(s16x4, __builtin_amdgcn_ds_read_tr16_b64_v4i16((AS3 s16x4*)p)); }
DI bf16x8 cat8(s16x4 a, s16x4 b) { return __builtin_shufflevector(a, b, 0, 1, 2, 3, 4, 5, 6, 7); }
DI void glds16(unsigned voff, const void* sbase, unsigned ldsbase) {
    unsigned sv;
    asm volatile("s_mov_b32 %0, m0\n\ts_mov_b32 m0, %3\n\ts_nop 0\n\tglobal_load_lds_dwordx4 %1, %2\n\ts_mov_b32 m0, %0"
                 : "=&s"(sv) : "v"(voff), "s"(sbase), "s"(ldsbase) : "memory");
}
DI float gelu_f(float v) {
    const float c1 = -2.3022081985f, c2 = -2.3022081985f * 0.044715f;
    float t = v * __builtin_fmaf(v * v, c2, c1);
    float e = __builtin_amdgcn_exp2f(t);
    return v * __builtin_amdgcn_rcpf(1.0f + e);
}

constexpr int K1_SLOT = 45056, K1_BZ = 16384, K1_BQ = 32768;
constexpr int K1_AB = 3 * K1_SLOT, K1_CS = K1_AB + 1024, K1_LDS = K1_CS + 1024;

template <int WN> DI void k1_wait_bar(int kt) {
    if (kt == 0) { if (WN == 0) asm volatile("s_waitcnt vmcnt(10) lgkmcnt(0)\n\ts_barrier" ::: "memory"); else asm volatile("s_waitcnt vmcnt(12) lgkmcnt(0)\n\ts_barrier" ::: "memory"); }
    else if (kt < 6) { if (WN == 0) asm volatile("s_waitcnt vmcnt(5) lgkmcnt(0)\n\ts_barrier" ::: "memory"); else asm volatile("s_waitcnt vmcnt(6) lgkmcnt(0)\n\ts_barrier" ::: "memory"); }
    else asm volatile("s_waitcnt vmcnt(0) lgkmcnt(0)\n\ts_barrier" ::: "memory");
}

template <int WN, bool ISV>
DI void k1_gemm(const char* x, const char* wqkv, const char* win, char* ws, char* smem, int mt, int cg, int wid) {
    constexpr int NQ = WN == 0 ? 2 : 1;
    TDECL; TMARK(0);
    const int lane = threadIdx.x & 63, r = lane & 31, h = lane >> 5, m = wid & 3;
    const lptr L = (lptr)smem;
    const unsigned lds0 = (unsigned)(uintptr_t)L;
    unsigned voA[2], voZ[2], voQ[2];
#pragma unroll
    for (int j = 0; j < 2; ++j) {
        int ia = 2 * wid + j, row = 8 * ia + (lane >> 3);
        voA[j] = (mt * 128 + row) * 896 + (((lane & 7) ^ ((row >> 1) & 7)) << 4);
        int krow = 4 * ia + (lane >> 4), c = (lane & 15) ^ ((lane >> 4) << 2);
        voZ[j] = krow * 3584 + (c >> 3) * 1792 + cg * 128 + (c & 7) * 16;
        int iq = WN == 0 ? wid : 4 + 2 * (wid - 4) + j;
        int u = iq * 64 + lane, kr = u / 12, cc = u - kr * 12;
        voQ[j] = kr * 2688 + cg * 192 + cc * 16;
    }
    const char* xa = x;
    const int iq0 = WN == 0 ? wid : 4 + 2 * (wid - 4);
    auto stage = [&](int kt, int slot) {
        unsigned sb = lds0 + slot * K1_SLOT;
        glds16(voA[0], xa + kt * 128, sb + (2 * wid) * 1024);
        glds16(voA[1], xa + kt * 128, sb + (2 * wid + 1) * 1024);
        glds16(voZ[0], win + (size_t)kt * 64 * 3584, sb + K1_BZ + (2 * wid) * 1024);
        glds16(voZ[1], win + (size_t)kt * 64 * 3584, sb + K1_BZ + (2 * wid + 1) * 1024);
        glds16(voQ[0], wqkv + (size_t)kt * 64 * 2688, sb + K1_BQ + iq0 * 1024);
        if (WN == 1) glds16(voQ[1], wqkv + (size_t)kt * 64 * 2688, sb + K1_BQ + (iq0 + 1) * 1024);
    };
    stage(0, 0); stage(1, 1); stage(2, 2);
    if (threadIdx.x < 256) *(AS3 float*)(L + K1_CS + threadIdx.x * 4) = 0.f;
    const int q = (lane & 15) >> 2, p = lane & 3, ch = (lane >> 4) & 1;
    int offA[4], offZ[4], offQ[4];
#pragma unroll
    for (int i = 0; i < 4; ++i) {
        int ks = (i + m) & 3;
        offA[i] = (m * 32 + r) * 128 + (((2 * ks + h) ^ ((r >> 1) & 7)) << 4);
        offZ[i] = K1_BZ + (16 * ks + 8 * h + q) * 256 + 32 * ch + 8 * p;
        offQ[i] = K1_BQ + (16 * ks + 8 * h + q) * 192 + 32 * ch + 8 * p + (WN == 0 ? 64 : 0);
    }
    const int zx0 = ((2 * WN) ^ q) * 64, zx1 = ((2 * WN + 1) ^ q) * 64;
    f32x16 accz[2], accq[NQ];
#pragma unroll
    for (int i = 0; i < 16; ++i) { accz[0][i] = 0.f; accz[1][i] = 0.f; accq[0][i] = 0.f; accq[NQ - 1][i] = 0.f; }
    float csz[2] = {0.f, 0.f}, csq[NQ]; csq[0] = 0.f; csq[NQ - 1] = 0.f;
    float sx = 0.f, sq = 0.f;
    const unsigned ONES = 0x3f803f80u;
#pragma unroll
    for (int kt = 0; kt < 7; ++kt) {
        k1_wait_bar<WN>(kt);
        if (kt == 0) TMARK(1);
        if (kt == 1) TMARK(2);
        if (kt == 6) TMARK(3);
        if (kt >= 1 && kt + 2 < 7) stage(kt + 2, (kt + 2) % 3);
        const lptr sb = L + (kt % 3) * K1_SLOT;
#pragma unroll
        for (int i = 0; i < 4; ++i) {
            bf16x8 a = *(const AS3 bf16x8*)(sb + offA[i]);
            bf16x8 z0 = cat8(trread(sb + offZ[i] + zx0), trread(sb + offZ[i] + zx0 + 1024));
            bf16x8 z1 = cat8(trread(sb + offZ[i] + zx1), trread(sb + offZ[i] + zx1 + 1024));
            bf16x8 wq[NQ];
#pragma unroll
            for (int t = 0; t < NQ; ++t) wq[t] = cat8(trread(sb + offQ[i] + 64 * t), trread(sb + offQ[i] + 64 * t + 768));
            accz[0] = MFMA32(z0, a, accz[0]);
            accz[1] = MFMA32(z1, a, accz[1]);
#pragma unroll
            for (int t = 0; t < NQ; ++t) accq[t] = ISV ? MFMA32(a, wq[t], accq[t]) : MFMA32(wq[t], a, accq[t]);
            if (i == 0) {
                u32x4 u0 = __builtin_bit_cast(u32x4, z0), u1 = __builtin_bit_cast(u32x4, z1);
#pragma unroll
                for (int j = 0; j < 4; ++j) { csz[0] = dot2(u0[j], ONES, csz[0]); csz[1] = dot2(u1[j], ONES, csz[1]); }
#pragma unroll
                for (int t = 0; t < NQ; ++t) { u32x4 u = __builtin_bit_cast(u32x4, wq[t]);
#pragma unroll
                    for (int j = 0; j < 4; ++j) csq[t] = dot2(u[j], ONES, csq[t]);
                    PIN(csq[t]); }
                PIN(csz[0]); PIN(csz[1]);
            }
            if (WN == 1) { u32x4 u = __builtin_bit_cast(u32x4, a);
#pragma unroll
                for (int j = 0; j < 4; ++j) { sx = dot2(u[j], ONES, sx); sq = dot2(u[j], u[j], sq); }
                PIN(sx); PIN(sq); }
        }
    }
    TMARK(4);
    AS3 float* cs = (AS3 float*)(L + K1_CS);
    AS3 f32x2* ab = (AS3 f32x2*)(L + K1_AB);
    {
        float v0 = halfsum(csz[0]), v1 = halfsum(csz[1]);
        __hip_atomic_fetch_add(cs + (2 * WN + h) * 32 + r, h ? v1 : v0, __ATOMIC_RELAXED, __HIP_MEMORY_SCOPE_WORKGROUP);
        if (WN == 0) { float w0 = halfsum(csq[0]), w1 = halfsum(csq[NQ - 1]);
            __hip_atomic_fetch_add(cs + (5 + h) * 32 + r, h ? w1 : w0, __ATOMIC_RELAXED, __HIP_MEMORY_SCOPE_WORKGROUP);
        } else { float w0 = halfsum(csq[0]);
            if (h == 0) __hip_atomic_fetch_add(cs + 4 * 32 + r, w0, __ATOMIC_RELAXED, __HIP_MEMORY_SCOPE_WORKGROUP);
            float s1 = halfsum(sx), s2 = halfsum(sq);
            float mu = s1 * (1.0f / 448.0f), var = fmaxf(s2 * (1.0f / 448.0f) - mu * mu, 0.f);
            float rs = __builtin_amdgcn_rsqf(var + 1e-5f);
            if (h == 0) ab[m * 32 + r] = f32x2{rs, -mu * rs};
        }
    }
    __syncthreads();
    TMARK(5);
    const int tt = mt * 4 + m;
    const f32x2 myab = ab[m * 32 + r];
    float zs = 0.f, zq = 0.f;
#pragma unroll
    for (int t = 0; t < 2; ++t) {
        float g[16];
#pragma unroll
        for (int gq = 0; gq < 4; ++gq) {
            f32x4 c4 = *(const AS3 f32x4*)(cs + (2 * WN + t) * 32 + 8 * gq + 4 * h);
#pragma unroll
            for (int j = 0; j < 4; ++j) { float v = __builtin_fmaf(myab[0], accz[t][4 * gq + j], myab[1] * c4[j]); g[4 * gq + j] = gelu_f(v); }
        }
        if (WN == 1) {
#pragma unroll
            for (int j = 0; j < 16; ++j) { zs += g[j]; zq = __builtin_fmaf(g[j], g[j], zq); }
        }
#pragma unroll
        for (int s = 0; s < 2; ++s) {
            u32x4 o = {pk2(g[8 * s], g[8 * s + 1]), pk2(g[8 * s + 2], g[8 * s + 3]), pk2(g[8 * s + 4], g[8 * s + 5]), pk2(g[8 * s + 6], g[8 * s + 7])};
            *(u32x4*)(ws + WS_ZF + ((size_t)tt * 112 + WN * 56 + cg * 4 + t * 2 + s) * 1024 + lane * 16) = o;
        }
    }
    if (WN == 1) { zs = halfsum(zs); zq = halfsum(zq);
        if (h == 0) *(f32x2*)(ws + WS_ST + ((size_t)cg * 2048 + tt * 32 + r) * 8) = f32x2{zs, zq}; }
    TMARK(6);
#pragma unroll
    for (int t = 0; t < NQ; ++t) {
        const int tq = WN == 0 ? 1 + t : 0, T = 3 * cg + tq;
        if (ISV) {
            const int c = (T - 28) * 32 + r, head = c / 28, d = c - head * 28;
            const float csl = cs[(4 + tq) * 32 + r];
            float v[16];
#pragma unroll
            for (int gq = 0; gq < 4; ++gq) {
                f32x4 p0 = *(const AS3 f32x4*)(ab + m * 32 + 8 * gq + 4 * h), p1 = *(const AS3 f32x4*)(ab + m * 32 + 8 * gq + 4 * h + 2);
                v[4 * gq + 0] = __builtin_fmaf(p0[0], accq[t][4 * gq + 0], p0[1] * csl);
                v[4 * gq + 1] = __builtin_fmaf(p0[2], accq[t][4 * gq + 1], p0[3] * csl);
                v[4 * gq + 2] = __builtin_fmaf(p1[0], accq[t][4 * gq + 2], p1[1] * csl);
                v[4 * gq + 3] = __builtin_fmaf(p1[2], accq[t][4 * gq + 3], p1[3] * csl);
            }
#pragma unroll
            for (int s = 0; s < 2; ++s) {
                u32x4 o = {pk2(v[8 * s], v[8 * s + 1]), pk2(v[8 * s + 2], v[8 * s + 3]), pk2(v[8 * s + 4], v[8 * s + 5]), pk2(v[8 * s + 6], v[8 * s + 7])};
                *(u32x4*)(ws + WS_KVF + ((size_t)(head * 64 + tt) * 4 + 2 + s) * 1024 + (h * 32 + d) * 16) = o;
            }
        } else {
            const bool isq = T < 14;
            const int Tp = isq ? T : T - 14;
            const float sc = isq ? 0.18898223650461362f * 1.4426950408889634f : 1.0f;
            const float a0 = myab[0] * sc, b0 = myab[1] * sc;
            char* base = isq ? ws + WS_QF + (size_t)tt * 2048 : ws + WS_KVF + (size_t)tt * 4096;
            const int hstride = isq ? 64 * 2048 : 64 * 4096;
#pragma unroll
            for (int gq = 0; gq < 4; ++gq) {
                f32x4 c4 = *(const AS3 f32x4*)(cs + (4 + tq) * 32 + 8 * gq + 4 * h);
                float v0 = __builtin_fmaf(a0, accq[t][4 * gq + 0], b0 * c4[0]), v1 = __builtin_fmaf(a0, accq[t][4 * gq + 1], b0 * c4[1]);
                float v2 = __builtin_fmaf(a0, accq[t][4 * gq + 2], b0 * c4[2]), v3 = __builtin_fmaf(a0, accq[t][4 * gq + 3], b0 * c4[3]);
                const int G = 8 * Tp + 2 * gq + h, head = G / 7, gi = G - head * 7;
                *(u32x2*)(base + (size_t)head * hstride + (gi >> 2) * 1024 + ((gi & 1) * 32 + r) * 16 + ((gi >> 1) & 1) * 8) = u32x2{pk2(v0, v1), pk2(v2, v3)};
            }
        }
    }
    TMARK(7); TFLUSH(0);
}

DI void k1_helper(const unsigned short* wo, const unsigned short* wout, char* ws, int sp) {
    const int tid = threadIdx.x, lane = tid & 63, wid = tid >> 6, gw = sp * 8 + wid;
    for (int f = gw; f < 14 * 84; f += 256) {
        const int nt = f / 84, ks = f - nt * 84, col = nt * 32 + (lane & 31), h = lane >> 5;
        const unsigned short* src = ks < 28 ? wo + (size_t)ks * 16 * 448 : wout + (size_t)(ks - 28) * 16 * 448;
        unsigned e[8];
#pragma unroll
        for (int j = 0; j < 8; ++j) e[j] = src[(8 * (j >> 2) + 4 * h + (j & 3)) * 448 + col];
        *(u32x4*)(ws + WS_BF + (size_t)f * 1024 + lane * 16) = u32x4{e[0] | (e[1] << 16), e[2] | (e[3] << 16), e[4] | (e[5] << 16), e[6] | (e[7] << 16)};
    }
    for (int b = gw * 4; b < gw * 4 + 4; ++b) {
        char* blk = ws + WS_KVF + (size_t)b * 4096;
        if (lane < 32) *(u32x2*)(blk + 1024 + (32 + lane) * 16 + 8) = u32x2{0x00003f80u, 0u};
        else if (lane < 48) { const int i = lane - 32, s = i >> 3, hh = (i >> 2) & 1, d = 28 + (i & 3); const unsigned v = (i & 3) == 0 ? 0x3f803f80u : 0u;
            *(u32x4*)(blk + (2 + s) * 1024 + (hh * 32 + d) * 16) = u32x4{v, v, v, v}; }
    }
    if (tid < 32) *(unsigned*)(ws + WS_FLG + (sp * 32 + tid) * 4) = 0u;
}

__global__ __launch_bounds__(512, 2) void k1_kernel(const char* x, const char* wqkv, const char* win, const unsigned short* wo, const unsigned short* wout, char* ws) {
    extern __shared__ __attribute__((aligned(16))) char smem[];
    TS(0, 0);
    const int xcd = blockIdx.x & 7, idx = blockIdx.x >> 3;
    const int wid = __builtin_amdgcn_readfirstlane(threadIdx.x >> 6);
    if (idx >= 28) { k1_helper(wo, wout, ws, xcd * 4 + idx - 28); TS(0, 1); return; }
    const int mt = (xcd >> 1) * 4 + idx / 7, cg = (xcd & 1) * 7 + idx % 7;
    if (wid < 4) { if (cg >= 9) k1_gemm<0, true>(x, wqkv, win, ws, smem, mt, cg, wid); else k1_gemm<0, false>(x, wqkv, win, ws, smem, mt, cg, wid); }
    else { if (cg >= 10) k1_gemm<1, true>(x, wqkv, win, ws, smem, mt, cg, wid); else k1_gemm<1, false>(x, wqkv, win, ws, smem, mt, cg, wid); }
    TS(0, 1);
}

template <int NF>
DI void z2_norm(char* ws, lptr L, int j, int tt, int f0) {
    const int lane = threadIdx.x & 63, r = lane & 31, h = lane >> 5;
    u32x4 raw[NF];
#pragma unroll
    for (int i = 0; i < NF; ++i) raw[i] = *(const u32x4*)(ws + WS_ZF + ((size_t)tt * 112 + 56 + 7 * j + f0 + i) * 1024 + lane * 16);
    float s1 = 0.f, s2 = 0.f;
#pragma unroll
    for (int i = 0; i < 7; ++i) { f32x2 v = *(const f32x2*)(ws + WS_ST + ((size_t)(2 * i + h) * 2048 + tt * 32 + r) * 8); s1 += v[0]; s2 += v[1]; }
    s1 = halfsum(s1); s2 = halfsum(s2);
    const float mu = s1 * (1.0f / 896.0f), var = fmaxf(s2 * (1.0f / 896.0f) - mu * mu, 0.f);
    const float rs = __builtin_amdgcn_rsqf(var + 1e-5f), nb = -mu * rs;
#pragma unroll
    for (int i = 0; i < NF; ++i) {
        unsigned o[4];
#pragma unroll
        for (int c = 0; c < 4; ++c) o[c] = pk2(__builtin_fmaf(bflo(raw[i][c]), rs, nb), __builtin_fmaf(bfhi(raw[i][c]), rs, nb));
        *(AS3 u32x2*)(L + i * 1024 + r * 32 + 8 * h) = u32x2{o[0], o[1]};
        *(AS3 u32x2*)(L + i * 1024 + r * 32 + 16 + 8 * h) = u32x2{o[2], o[3]};
    }
    const int kq = lane >> 4, q = (lane & 15) >> 2, p = lane & 3;
#pragma unroll
    for (int i = 0; i < NF; ++i) {
        bf16x8 v = cat8(trread(L + i * 1024 + (8 * kq + q) * 32 + 8 * p), trread(L + i * 1024 + (8 * kq + 4 + q) * 32 + 8 * p));
        *(bf16x8*)(ws + WS_Z2N + (((size_t)j * 64 + tt) * 7 + f0 + i) * 1024 + lane * 16) = v;
    }
}
__global__ __launch_bounds__(256) void k15_kernel(char* ws) {
    __shared__ __attribute__((aligned(16))) char sm[4 * 4096];
    TS(1, 0);
    const int wid = __builtin_amdgcn_readfirstlane(threadIdx.x >> 6);
    const int task = blockIdx.x * 4 + wid;
    const int j = task >> 7, tt = (task >> 1) & 63;
    if (task & 1) z2_norm<3>(ws, (lptr)sm + wid * 4096, j, tt, 4); else z2_norm<4>(ws, (lptr)sm + wid * 4096, j, tt, 0);
    TS(1, 1);
}

constexpr int K2_GP = 21504, K2_AT = 4 * K2_GP, K2_LDS = K2_AT + 4 * 12288;
#define EX2(x) __builtin_amdgcn_exp2f(x)

DI void attn_store(char* ws, const f32x16& o, int head, int tt) {
    const int lane = threadIdx.x & 63, r = lane & 31, h = lane >> 5;
    const float inv = 1.0f / hihalf(o[12]);
#pragma unroll
    for (int g = 0; g < 4; ++g) {
        const int gi = 2 * g + h, G = 7 * head + gi;
        if (gi < 7) *(u32x2*)(ws + WS_AF + ((size_t)tt * 84 + (G >> 2)) * 1024 + ((G & 1) * 32 + r) * 16 + ((G >> 1) & 1) * 8) =
            u32x2{pk2(o[4 * g] * inv, o[4 * g + 1] * inv), pk2(o[4 * g + 2] * inv, o[4 * g + 3] * inv)};
    }
}
__device__ __noinline__ void attn_safe(char* ws, int head, int qt) {
    const int lane = threadIdx.x & 63, h = lane >> 5;
    const char* qp = ws + WS_QF + ((size_t)head * 64 + qt) * 2048 + lane * 16;
    bf16x8 q0 = *(const bf16x8*)qp; u32x4 q1u = *(const u32x4*)(qp + 1024);
    if (h) { q1u[2] = 0u; q1u[3] = 0u; }
    const bf16x8 q1 = __builtin_bit_cast(bf16x8, q1u);
    f32x16 o, zero;
#pragma unroll
    for (int i = 0; i < 16; ++i) { o[i] = 0.f; zero[i] = 0.f; }
    float m = -3.0e38f;
    for (int kt = 0; kt < 64; ++kt) {
        const char* kp = ws + WS_KVF + ((size_t)head * 64 + kt) * 4096 + lane * 16;
        bf16x8 k0 = *(const bf16x8*)kp, k1 = *(const bf16x8*)(kp + 1024), v0 = *(const bf16x8*)(kp + 2048), v1 = *(const bf16x8*)(kp + 3072);
        f32x16 s = MFMA32(k0, q0, zero); s = MFMA32(k1, q1, s);
        float mx = s[0];
#pragma unroll
        for (int i = 1; i < 16; ++i) mx = fmaxf(mx, s[i]);
        mx = fmaxf(halfmax(mx), m);
        const float f = EX2(m - mx); m = mx;
        u32x4 p0, p1;
#pragma unroll
        for (int i = 0; i < 16; ++i) { o[i] *= f; s[i] = EX2(s[i] - m); }
#pragma unroll
        for (int i = 0; i < 4; ++i) { p0[i] = pk2(s[2 * i], s[2 * i + 1]); p1[i] = pk2(s[8 + 2 * i], s[9 + 2 * i]); }
        o = MFMA32(v0, __builtin_bit_cast(bf16x8, p0), o); o = MFMA32(v1, __builtin_bit_cast(bf16x8, p1), o);
    }
    attn_store(ws, o, head, qt);
}

DI void k2_attn(char* ws, lptr L, int head, int qb, int w) {
    const int lane = threadIdx.x & 63, h = lane >> 5;
    TDECL; TMARK(0);
    const char* qp = ws + WS_QF + ((size_t)head * 64 + qb * 4) * 2048 + lane * 16;
    const char* kv = ws + WS_KVF + (size_t)head * 64 * 4096 + lane * 16;
    bf16x8 q0[4]; u32x4 q1[4];
#pragma unroll
    for (int t = 0; t < 4; ++t) { q0[t] = *(const bf16x8*)(qp + t * 2048); q1[t] = *(const u32x4*)(qp + t * 2048 + 1024); }
    bf16x8 r0 = *(const bf16x8*)kv, r1 = *(const bf16x8*)(kv + 1024);
    kv += (size_t)w * 16 * 4096;
    bf16x8 ka0 = *(const bf16x8*)kv, ka1 = *(const bf16x8*)(kv + 1024), va0 = *(const bf16x8*)(kv + 2048), va1 = *(const bf16x8*)(kv + 3072);
    bf16x8 kb0, kb1, vb0, vb1;
    f32x16 zero, o[4], sA, sB;
#pragma unroll
    for (int i = 0; i < 16; ++i) { zero[i] = 0.f; o[0][i] = 0.f; o[1][i] = 0.f; o[2][i] = 0.f; o[3][i] = 0.f; }
#pragma unroll
    for (int t = 0; t < 4; ++t) {
        if (h) { q1[t][2] = 0u; q1[t][3] = 0u; }
        f32x16 s = MFMA32(r0, q0[t], zero); s = MFMA32(r1, __builtin_bit_cast(bf16x8, q1[t]), s);
        float mx = fmaxf(s[0], s[1]);
#pragma unroll
        for (int i = 2; i < 16; i += 2) mx = fmaxf(fmaxf(mx, s[i]), s[i + 1]);
        mx = halfmax(mx);
        if (h) q1[t][2] = pk2(-(mx + 32.0f), 0.f);
    }
#define Q1(t) __builtin_bit_cast(bf16x8, q1[t])
#define PB(x) __builtin_bit_cast(bf16x8, x)
#define ASTEP(A, B, O, V0, V1, K0, K1, QN) do { u32x4 p0, p1; \
    B = MFMA32(K0, q0[QN], zero); SB(); \
    A[0] = EX2(A[0]); A[1] = EX2(A[1]); A[2] = EX2(A[2]); A[3] = EX2(A[3]); PIN(A); SB(); \
    B = MFMA32(K1, Q1(QN), B); SB(); \
    A[4] = EX2(A[4]); A[5] = EX2(A[5]); p0[0] = pk2(A[0], A[1]); A[6] = EX2(A[6]); A[7] = EX2(A[7]); p0[1] = pk2(A[2], A[3]); PIN(A); SB(); \
    A[8] = EX2(A[8]); p0[2] = pk2(A[4], A[5]); A[9] = EX2(A[9]); p0[3] = pk2(A[6], A[7]); A[10] = EX2(A[10]); A[11] = EX2(A[11]); PIN(A); PIN(p0); SB(); \
    O = MFMA32(V0, PB(p0), O); SB(); \
    A[12] = EX2(A[12]); A[13] = EX2(A[13]); p1[0] = pk2(A[8], A[9]); A[14] = EX2(A[14]); A[15] = EX2(A[15]); p1[1] = pk2(A[10], A[11]); PIN(A); SB(); \
    p1[2] = pk2(A[12], A[13]); p1[3] = pk2(A[14], A[15]); PIN(p1); SB(); \
    O = MFMA32(V1, PB(p1), O); SB(); } while (0)
    TMARK(1);
    sA = MFMA32(ka0, q0[0], zero); sA = MFMA32(ka1, Q1(0), sA);
    for (int it = 0; it < 8; ++it) {
        kb0 = *(const bf16x8*)(kv + 4096); kb1 = *(const bf16x8*)(kv + 4096 + 1024); vb0 = *(const bf16x8*)(kv + 4096 + 2048); vb1 = *(const bf16x8*)(kv + 4096 + 3072); SB();
        ASTEP(sA, sB, o[0], va0, va1, ka0, ka1, 1);
        ASTEP(sB, sA, o[1], va0, va1, ka0, ka1, 2);
        ASTEP(sA, sB, o[2], va0, va1, ka0, ka1, 3);
        ASTEP(sB, sA, o[3], va0, va1, kb0, kb1, 0);
        kv += it < 7 ? 8192 : 0;
        ka0 = *(const bf16x8*)kv; ka1 = *(const bf16x8*)(kv + 1024); va0 = *(const bf16x8*)(kv + 2048); va1 = *(const bf16x8*)(kv + 3072); SB();
        ASTEP(sA, sB, o[0], vb0, vb1, kb0, kb1, 1);
        ASTEP(sB, sA, o[1], vb0, vb1, kb0, kb1, 2);
        ASTEP(sA, sB, o[2], vb0, vb1, kb0, kb1, 3);
        ASTEP(sB, sA, o[3], vb0, vb1, ka0, ka1, 0);
    }
    TMARK(2);
    const lptr P = L + K2_AT;
#pragma unroll
    for (int t = 0; t < 4; ++t) if (t != w) {
        const int si = w < t ? w : w - 1;
#pragma unroll
        for (int g = 0; g < 4; ++g) *(AS3 f32x4*)(P + (t * 3 + si) * 4096 + g * 1024 + lane * 16) = f32x4{o[t][4 * g], o[t][4 * g + 1], o[t][4 * g + 2], o[t][4 * g + 3]};
    }
    f32x16 mine;
#pragma unroll
    for (int i = 0; i < 16; ++i) mine[i] = w == 0 ? o[0][i] : w == 1 ? o[1][i] : w == 2 ? o[2][i] : o[3][i];
    TMARK(3);
    __syncthreads();
    TMARK(4);
#pragma unroll
    for (int s = 0; s < 3; ++s)
#pragma unroll
        for (int g = 0; g < 4; ++g) { f32x4 v = *(const AS3 f32x4*)(P + (w * 3 + s) * 4096 + g * 1024 + lane * 16);
#pragma unroll
            for (int c = 0; c < 4; ++c) mine[4 * g + c] += v[c]; }
    float amax = 0.f;
#pragma unroll
    for (int i = 0; i < 16; ++i) { float a = fabsf(mine[i]); amax = a > amax || a != a ? a : amax; }
    const float l = hihalf(mine[12]);
    const bool bad = !(amax < 1.0e36f) || !(l > 1.0e-30f);
    if (__builtin_expect(__any(bad), 0)) attn_safe(ws, head, qb * 4 + w);
    else attn_store(ws, mine, head, qb * 4 + w);
    TMARK(5); TFLUSH(2);
}

DI void k2_gemm(const char* wsp, const unsigned short* bsp, char* ws, lptr L, int mi, int j, int w) {
    const int lane = threadIdx.x & 63, r = lane & 15, kq = lane >> 4;
    TDECL; TMARK(0);
    const lptr W = L + w * K2_GP;
    const char* zsrc = ws + WS_Z2N + ((size_t)j * 64 + w * 16) * 7 * 1024 + lane * 16;
    const char* wsrc = wsp + ((size_t)mi * 64 + (lane >> 3)) * 4096 + w * 1024 + (lane & 7) * 16;
    const int wwo = (lane >> 3) * 128;
    const int rd0 = r * 128 + ((kq ^ (r >> 1)) << 4), rd1 = rd0 ^ 64;
    f32x4 acc[7][4];
#pragma unroll
    for (int a = 0; a < 7; ++a)
#pragma unroll
        for (int b = 0; b < 4; ++b) acc[a][b] = f32x4{0.f, 0.f, 0.f, 0.f};
    u32x4 wst[8]; bf16x8 z[2][7];
#define WLOAD(ds) _Pragma("unroll") for (int i = 0; i < 8; ++i) wst[i] = *(const u32x4*)(wsrc + (size_t)i * 8 * 4096 + (ds) * 128)
#define WPUT(buf) _Pragma("unroll") for (int i = 0; i < 8; ++i) *(AS3 u32x4*)(W + (buf) * 8192 + i * 1024 + wwo + (((lane & 7) ^ ((4 * i + (lane >> 4)) & 7)) << 4)) = wst[i]
    WLOAD(0);
#pragma unroll
    for (int s = 0; s < 2; ++s)
#pragma unroll
        for (int f = 0; f < 7; ++f) z[s][f] = *(const bf16x8*)(zsrc + (s * 7 + f) * 1024);
    SB();
    WPUT(0); SB();
    WLOAD(1); SB();
    TMARK(1);
#pragma unroll
    for (int ds = 0; ds < 8; ++ds) {
#pragma unroll
        for (int ks = 0; ks < 2; ++ks) {
            bf16x8 wf[4];
#pragma unroll
            for (int t = 0; t < 4; ++t) wf[t] = *(const AS3 bf16x8*)(W + (ds & 1) * 8192 + t * 2048 + (ks ? rd1 : rd0));
            if (ks == 1 && ds < 7) { WPUT((ds + 1) & 1); SB(); if (ds < 6) { WLOAD(ds + 2); } SB(); }
#pragma unroll
            for (int f = 0; f < 7; ++f) {
#pragma unroll
                for (int t = 0; t < 4; ++t) acc[f][t] = MFMA16(z[ks][f], wf[t], acc[f][t]);
                SB();
                if (ds < 7) z[ks][f] = *(const bf16x8*)(zsrc + ((2 * ds + 2 + ks) * 7 + f) * 1024);
                SB();
            }
        }
    }
    TMARK(2);
    const int tok = mi * 64 + w * 16 + r, tt = tok >> 5;
    const size_t uo = ((kq & 1) * 32 + (tok & 31)) * 16 + (kq >> 1) * 8;
    u32x2 z1[7];
#pragma unroll
    for (int f = 0; f < 7; ++f) z1[f] = *(const u32x2*)(ws + WS_ZF + ((size_t)tt * 112 + 7 * j + f) * 1024 + uo);
    const float bias = bflo(bsp[tok]);
#pragma unroll
    for (int t = 0; t < 4; ++t) if (t != w) {
        const int si = t < w ? t : t - 1;
#pragma unroll
        for (int f = 0; f < 7; ++f) *(AS3 f32x4*)(W + (si * 7 + f) * 1024 + lane * 16) = acc[f][t];
    }
    f32x4 mine[7];
#pragma unroll
    for (int f = 0; f < 7; ++f) mine[f] = w == 0 ? acc[f][0] : w == 1 ? acc[f][1] : w == 2 ? acc[f][2] : acc[f][3];
    TMARK(3);
    __syncthreads();
    TMARK(4);
#pragma unroll
    for (int s = 0; s < 3; ++s) {
        const int src = s < w ? s : s + 1, si = w < src ? w : w - 1;
#pragma unroll
        for (int f = 0; f < 7; ++f) mine[f] += *(const AS3 f32x4*)(L + src * K2_GP + (si * 7 + f) * 1024 + lane * 16);
    }
#pragma unroll
    for (int f = 0; f < 7; ++f) {
        u32x2 o = {pk2((mine[f][0] + bias) * bflo(z1[f][0]), (mine[f][1] + bias) * bfhi(z1[f][0])), pk2((mine[f][2] + bias) * bflo(z1[f][1]), (mine[f][3] + bias) * bfhi(z1[f][1]))};
        *(u32x2*)(ws + WS_AF + ((size_t)tt * 84 + 28 + 7 * j + f) * 1024 + uo) = o;
    }
    TMARK(5); TFLUSH(2);
}

__global__ __launch_bounds__(512, 2) void k2_kernel(const char* wsp, const unsigned short* bsp, char* ws) {
    extern __shared__ __attribute__((aligned(16))) char smem[];
    TS(2, 0);
    const int xcd = blockIdx.x & 7, idx = blockIdx.x >> 3;
    const int wid = __builtin_amdgcn_readfirstlane(threadIdx.x >> 6);
    if (wid < 4) k2_attn(ws, (lptr)smem, 2 * xcd + (idx >> 4), idx & 15, wid);
    else k2_gemm(wsp, bsp, ws, (lptr)smem, (xcd >> 1) * 8 + (idx >> 2), (xcd & 1) * 4 + (idx & 3), wid - 4);
    TS(2, 1);
}

constexpr int K3_ROW = 272, K3_TILE = 64 * K3_ROW, K3_LDS = 4 * K3_TILE;
__global__ __launch_bounds__(256) void k3_kernel(const char* x, const char* ws_c, char* out, char* ws) {
    extern __shared__ __attribute__((aligned(16))) char smem[];
    TS(3, 0);
    TDECL; TMARK(0);
    const lptr L = (lptr)smem;
    const int xcd = blockIdx.x & 7, idx = blockIdx.x >> 3;
    const int mi = xcd * 4 + idx / 7, nj = idx % 7;
    const int tid = threadIdx.x, lane = tid & 63, r = lane & 31, h = lane >> 5;
    const int w = __builtin_amdgcn_readfirstlane(tid >> 6);
    const char* ap = ws_c + WS_AF + ((size_t)mi * 2 * 84 + w * 21) * 1024 + lane * 16;
    const char* bp = ws_c + WS_BF + ((size_t)nj * 2 * 84 + w * 21) * 1024 + lane * 16;
    constexpr int DEP = 7;
    bf16x8 a[DEP][2], b[DEP][2];
#pragma unroll
    for (int s = 0; s < DEP; ++s) {
        a[s][0] = *(const bf16x8*)(ap + s * 1024); a[s][1] = *(const bf16x8*)(ap + (84 + s) * 1024);
        b[s][0] = *(const bf16x8*)(bp + s * 1024); b[s][1] = *(const bf16x8*)(bp + (84 + s) * 1024);
    }
    u32x4 xr[2];
#pragma unroll
    for (int i = 0; i < 2; ++i) xr[i] = *(const u32x4*)(x + ((size_t)mi * 64 + i * 32 + (tid >> 3)) * 896 + nj * 128 + (tid & 7) * 16);
    SB();
    f32x16 acc[2][2];
#pragma unroll
    for (int i = 0; i < 16; ++i) { acc[0][0][i] = 0.f; acc[0][1][i] = 0.f; acc[1][0][i] = 0.f; acc[1][1][i] = 0.f; }
#pragma unroll
    for (int s = 0; s < 21; ++s) {
        const int u = s % DEP;
        if (s == 1) TMARK(1);
        acc[0][0] = MFMA32(b[u][0], a[u][0], acc[0][0]); acc[0][1] = MFMA32(b[u][1], a[u][0], acc[0][1]);
        acc[1][0] = MFMA32(b[u][0], a[u][1], acc[1][0]); acc[1][1] = MFMA32(b[u][1], a[u][1], acc[1][1]);
        SB();
        if (s + DEP < 21) {
            a[u][0] = *(const bf16x8*)(ap + (s + DEP) * 1024); a[u][1] = *(const bf16x8*)(ap + (84 + s + DEP) * 1024);
            b[u][0] = *(const bf16x8*)(bp + (s + DEP) * 1024); b[u][1] = *(const bf16x8*)(bp + (84 + s + DEP) * 1024);
        }
        SB();
    }
    TMARK(2);
#pragma unroll
    for (int tm = 0; tm < 2; ++tm)
#pragma unroll
        for (int tn = 0; tn < 2; ++tn)
#pragma unroll
            for (int g = 0; g < 4; ++g)
                *(AS3 f32x4*)(L + w * K3_TILE + (tm * 32 + r) * K3_ROW + (tn * 32 + 8 * g + 4 * h) * 4) =
                    f32x4{acc[tm][tn][4 * g], acc[tm][tn][4 * g + 1], acc[tm][tn][4 * g + 2], acc[tm][tn][4 * g + 3]};
    __syncthreads();
#pragma unroll
    for (int i = 0; i < 2; ++i) {
        const int row = i * 32 + (tid >> 3), c8 = tid & 7;
        f32x4 s0 = {0.f, 0.f, 0.f, 0.f}, s1 = s0;
#pragma unroll
        for (int p = 0; p < 4; ++p) { s0 += *(const AS3 f32x4*)(L + p * K3_TILE + row * K3_ROW + c8 * 32); s1 += *(const AS3 f32x4*)(L + p * K3_TILE + row * K3_ROW + c8 * 32 + 16); }
        u32x4 o = {pk2(s0[0] + bflo(xr[i][0]), s0[1] + bfhi(xr[i][0])), pk2(s0[2] + bflo(xr[i][1]), s0[3] + bfhi(xr[i][1])),
                   pk2(s1[0] + bflo(xr[i][2]), s1[1] + bfhi(xr[i][2])), pk2(s1[2] + bflo(xr[i][3]), s1[3] + bfhi(xr[i][3]))};
        *(u32x4*)(out + ((size_t)mi * 64 + row) * 896 + nj * 128 + c8 * 16) = o;
    }
    TS(3, 1);
    TMARK(3); TFLUSH(3);
}

extern "C" void kernel_launch(void* const* d_in, const int* in_sizes, int n_in, void* d_out, int out_size, void* d_ws, size_t ws_size, hipStream_t stream) {
    const char* x = (const char*)d_in[0]; const char* wqkv = (const char*)d_in[1]; const unsigned short* wo = (const unsigned short*)d_in[2];
    const char* win = (const char*)d_in[4]; const char* wsp = (const char*)d_in[5]; const unsigned short* bsp = (const unsigned short*)d_in[6];
    const unsigned short* wout = (const unsigned short*)d_in[7];
    char* ws = (char*)d_ws;
    (void)hipFuncSetAttribute((const void*)k1_kernel, hipFuncAttributeMaxDynamicSharedMemorySize, K1_LDS);
    (void)hipFuncSetAttribute((const void*)k2_kernel, hipFuncAttributeMaxDynamicSharedMemorySize, K2_LDS);
    (void)hipFuncSetAttribute((const void*)k3_kernel, hipFuncAttributeMaxDynamicSharedMemorySize, K3_LDS);
    k1_kernel<<<256, 512, K1_LDS, stream>>>(x, wqkv, win, wo, wout, ws);
    k15_kernel<<<256, 256, 0, stream>>>(ws);
    k2_kernel<<<256, 512, K2_LDS, stream>>>(wsp, bsp, ws);
    k3_kernel<<<224, 256, K3_LDS, stream>>>(x, ws, (char*)d_out, ws);
}
```

```cpp
#include <hip/hip_runtime.h>
#include <hip/hip_bf16.h>
#include <stdint.h>

typedef short bf16x8 __attribute__((ext_vector_type(8)));
typedef short s16x4 __attribute__((ext_vector_type(4)));
typedef float f32x16 __attribute__((ext_vector_type(16)));
typedef float f32x4 __attribute__((ext_vector_type(4)));
typedef float f32x2 __attribute__((ext_vector_type(2)));
typedef unsigned u32x4 __attribute__((ext_vector_type(4)));
typedef unsigned u32x2 __attribute__((ext_vector_type(2)));
typedef __bf16 bf2 __attribute__((ext_vector_type(2)));
#define AS3 __attribute__((address_space(3)))
typedef AS3 char* lptr;
typedef __attribute__((address_space(1))) unsigned gu32;
#define DI __device__ __forceinline__
#define SB() __builtin_amdgcn_sched_barrier(0)
#define PIN(x) asm volatile("" : "+v"(x))
#define MFMA32(a, b, c) __builtin_amdgcn_mfma_f32_32x32x16_bf16((a), (b), (c), 0, 0, 0)
#define MFMA16(a, b, c) __builtin_amdgcn_mfma_f32_16x16x32_bf16((a), (b), (c), 0, 0, 0)

constexpr size_t WS_QF = 0;
constexpr size_t WS_KVF = 2u << 20;
constexpr size_t WS_ZF = 6u << 20;
constexpr size_t WS_Z2N = 14u << 20;
constexpr size_t WS_AF = 18u << 20;
constexpr size_t WS_BF = 24u << 20;
constexpr size_t WS_ST = 26u << 20;
constexpr size_t WS_EP = 27u << 20;
constexpr size_t WS_FLG = WS_EP + 8192;
constexpr size_t WS_DBG = 28u << 20;

#define TS0 do {} while (0)
#define TS1(k) do {} while (0)
#define TDECL do {} while (0)
#define TARG
#define TPASS
#define TMARK(i) do {} while (0)
#define TFLUSH(k) do {} while (0)

DI unsigned pk2(float a, float b) { f32x2 v = {a, b}; return __builtin_bit_cast(unsigned, __builtin_convertvector(v, bf2)); }
DI float bflo(unsigned u) { return __uint_as_float(u << 16); }
DI float bfhi(unsigned u) { return __uint_as_float(u & 0xffff0000u); }
DI float dot2(unsigned a, unsigned b, float c) { return __builtin_amdgcn_fdot2_f32_bf16(__builtin_bit_cast(bf2, a), __builtin_bit_cast(bf2, b), c, false); }
DI float halfsum(float v) { auto r = __builtin_amdgcn_permlane32_swap(__float_as_uint(v), __float_as_uint(v), false, false); return __uint_as_float(r[0]) + __uint_as_float(r[1]); }
DI float halfmax(float v) { auto r = __builtin_amdgcn_permlane32_swap(__float_as_uint(v), __float_as_uint(v), false, false); return fmaxf(__uint_as_float(r[0]), __uint_as_float(r[1])); }
DI float hihalf(float v) { auto r = __builtin_amdgcn_permlane32_swap(__float_as_uint(v), __float_as_uint(v), false, false); return __uint_as_float(r[1]); }
DI s16x4 trread(lptr p) { return __builtin_bit_cast(s16x4, __builtin_amdgcn_ds_read_tr16_b64_v4i16((AS3 s16x4*)p)); }
DI bf16x8 cat8(s16x4 a, s16x4 b) { return __builtin_shufflevector(a, b, 0, 1, 2, 3, 4, 5, 6, 7); }
DI void glds16(unsigned voff, const void* sbase, unsigned ldsbase) {
    unsigned sv;
    asm volatile("s_mov_b32 %0, m0\n\ts_mov_b32 m0, %3\n\ts_nop 0\n\tglobal_load_lds_dwordx4 %1, %2\n\ts_mov_b32 m0, %0"
                 : "=&s"(sv) : "v"(voff), "s"(sbase), "s"(ldsbase) : "memory");
}
typedef __amdgpu_buffer_rsrc_t rsrc_t;
DI rsrc_t mkrsrc(const void* p, unsigned bytes) { return __builtin_amdgcn_make_buffer_rsrc((void*)p, 0, bytes, 0x00020000); }
DI u32x4 bld4(rsrc_t r, unsigned voff, unsigned soff) { return __builtin_amdgcn_raw_buffer_load_b128(r, voff, soff, 0); }
DI bf16x8 bld8(rsrc_t r, unsigned voff, unsigned soff) { return __builtin_bit_cast(bf16x8, __builtin_amdgcn_raw_buffer_load_b128(r, voff, soff, 0)); }
constexpr unsigned WS_BYTES = 28u << 20;
DI float gelu_f(float v) {
    const float c1 = -2.3022081985f, c2 = -2.3022081985f * 0.044715f;
    float t = v * __builtin_fmaf(v * v, c2, c1);
    float e = __builtin_amdgcn_exp2f(t);
    return v * __builtin_amdgcn_rcpf(1.0f + e);
}

constexpr int K1_SLOT = 45056, K1_BZ = 16384, K1_BQ = 32768;
constexpr int K1_CP = 3 * K1_SLOT, K1_SP = K1_CP + 4096, K1_PRIV = K1_SP + 4096, K1_LDS = K1_PRIV + 8 * 1024;
DI f32x2 fma2(f32x2 a, f32x2 b, f32x2 c) { return __builtin_elementwise_fma(a, b, c); }
DI unsigned pk2v(f32x2 v) { return __builtin_bit_cast(unsigned, __builtin_convertvector(v, bf2)); }
#define WAITBAR(n) asm volatile("s_waitcnt vmcnt(" #n ") lgkmcnt(0)\n\ts_barrier" ::: "memory")

template <int NQ> struct K1Frag { bf16x8 a, z0, z1, wq[NQ]; };

struct K1Src { unsigned voA0, voA1, voZ, voQ, dA, dQ; };
DI void k1_piece(const K1Src& s, const char* x, const char* wqkv, const char* win, int kt, int j) {
    const unsigned so = (kt % 3) * K1_SLOT;
    if (j == 0) glds16(s.voA0, x + kt * 128, s.dA + so);
    if (j == 1) glds16(s.voA1, x + kt * 128, s.dA + so + 1024);
    if (j == 2) glds16(s.voZ, win + (size_t)kt * 64 * 3584, s.dA + so + K1_BZ);
    if (j == 3) glds16(s.voZ, win + (size_t)(kt * 64 + 4) * 3584, s.dA + so + K1_BZ + 1024);
    if (j == 4) glds16(s.voQ, wqkv + (size_t)kt * 64 * 2688, s.dQ + so);
    if (j == 5) glds16(s.voQ, wqkv + (size_t)kt * 64 * 2688 + 64, s.dQ + so + 4096);
}

template <int WN, bool ISV>
DI void k1_gemm(const K1Src& src, const char* x, const char* wqkv, const char* win, char* ws, char* smem, int mt, int cg, int wid) {
    constexpr int NQ = WN == 0 ? 2 : 1;
    TDECL; TMARK(0);
    const int lane = threadIdx.x & 63, r = lane & 31, h = lane >> 5, m = wid & 3;
    const lptr L = (lptr)smem;
    auto piece = [&](int kt, int j) { if (j < 5 || WN == 1) k1_piece(src, x, wqkv, win, kt, j); };
    const int q = (lane & 15) >> 2, p = lane & 3, ch = (lane >> 4) & 1;
    int offA[4], offZ0[4], offZ1[4], offQ[4];
#pragma unroll
    for (int i = 0; i < 4; ++i) {
        int ks = (i + m + 2 * WN) & 3;
        offA[i] = (m * 32 + r) * 128 + (((2 * ks + h) ^ ((r >> 1) & 7)) << 4);
        int oz = K1_BZ + (16 * ks + 8 * h + q) * 256 + 32 * ch + 8 * p;
        offZ0[i] = oz + ((2 * WN) ^ q) * 64; offZ1[i] = oz + ((2 * WN + 1) ^ q) * 64;
        offQ[i] = K1_BQ + (16 * ks + 8 * h + q) * 64 + 32 * ch + 8 * p + (WN == 0 ? 4096 : 0);
    }
    f32x16 accz[2], accq[NQ];
#pragma unroll
    for (int i = 0; i < 16; ++i) { accz[0][i] = 0.f; accz[1][i] = 0.f; accq[0][i] = 0.f; accq[NQ - 1][i] = 0.f; }
    f32x4 csz[2], csq[NQ];
    csz[0] = csz[1] = csq[0] = csq[NQ - 1] = f32x4{0.f, 0.f, 0.f, 0.f};
    float sx[2] = {0.f, 0.f}, sq[2] = {0.f, 0.f};
    const unsigned ONES = 0x3f803f80u;
    const unsigned selw = (lane & 15) == ((lane >> 4) & 1) ? ONES : 0u;
    const bf16x8 sel = __builtin_bit_cast(bf16x8, u32x4{selw, selw, selw, selw});
    K1Frag<NQ> fr[2];
    auto rd = [&](K1Frag<NQ>& f, int kt, int i) {
        const lptr sb = L + (kt % 3) * K1_SLOT;
        f.a = *(const AS3 bf16x8*)(sb + offA[i]);
        f.z0 = cat8(trread(sb + offZ0[i]), trread(sb + offZ0[i] + 1024));
        f.z1 = cat8(trread(sb + offZ1[i]), trread(sb + offZ1[i] + 1024));
#pragma unroll
        for (int t = 0; t < NQ; ++t) f.wq[t] = cat8(trread(sb + offQ[i] + 4096 * t), trread(sb + offQ[i] + 4096 * t + 256));
    };
    auto stat = [&](unsigned u) {
        const float lo = bflo(u), hi = bfhi(u);
        sx[0] += lo; sx[1] += hi; sq[0] = __builtin_fmaf(lo, lo, sq[0]); sq[1] = __builtin_fmaf(hi, hi, sq[1]);
        PIN(sx[0]); PIN(sx[1]); PIN(sq[0]); PIN(sq[1]); };
    AS3 float* cp = (AS3 float*)(L + K1_CP);
    AS3 f32x2* sp = (AS3 f32x2*)(L + K1_SP);
    AS3 float* pv = (AS3 float*)(L + K1_PRIV + wid * 1024);
    float c1[4], c2[4]; f32x2 st[4];
    const int tt = mt * 4 + m;
    unsigned qo[NQ][4];
#pragma unroll
    for (int t = 0; t < NQ; ++t) {
        const int T = 3 * cg + (WN == 0 ? 1 + t : 0);
        if (ISV) { const int c = (T - 28) * 32 + r, head = c / 28, d = c - head * 28;
            qo[t][0] = (unsigned)WS_KVF + ((head * 64 + tt) * 4 + 2) * 1024 + (h * 32 + d) * 16; qo[t][1] = qo[t][2] = qo[t][3] = 0;
        } else {
            const bool isq = T < 14; const int Tp = isq ? T : T - 14;
#pragma unroll
            for (int gq = 0; gq < 4; ++gq) { const int G = 8 * Tp + 2 * gq + h, head = G / 7, gi = G - head * 7;
                qo[t][gq] = (isq ? (unsigned)WS_QF + (head * 64 + tt) * 2048 : (unsigned)WS_KVF + (head * 64 + tt) * 4096) + (gi >> 2) * 1024 + ((gi & 1) * 32 + r) * 16 + ((gi >> 1) & 1) * 8; }
        }
#pragma unroll
        for (int gq = 0; gq < 4; ++gq) PIN(qo[t][gq]);
    }
    unsigned zo = (unsigned)WS_ZF + (tt * 56 + cg * 4) * 1024 + lane * 16; PIN(zo);
    unsigned epoch = 0;
    if (WN == 1) asm volatile("s_load_dword %0, %1, 0x0\n\ts_waitcnt lgkmcnt(0)" : "=s"(epoch) : "s"(ws + WS_EP + tt * 64) : "memory");
    SB();
#pragma unroll
    for (int j = 0; j < 6; ++j) piece(1, j);
    if (WN == 0) WAITBAR(5); else WAITBAR(6);
    TMARK(1);
    rd(fr[0], 0, 0);
#pragma unroll
    for (int kt = 0; kt < 7; ++kt) {
#pragma unroll
        for (int i = 0; i < 4; ++i) {
            SB();
            if (i < 3) rd(fr[(i + 1) & 1], kt, i + 1);
            else if (kt < 6) {
                if (kt < 5) { if (WN == 0) WAITBAR(5); else WAITBAR(6); } else WAITBAR(0);
                rd(fr[0], kt + 1, 0);
            } else {
                asm volatile("s_waitcnt lgkmcnt(0)\n\ts_barrier" ::: "memory");
                const int i1 = 64 * WN + lane, i2 = WN == 0 ? 160 + lane : 128 + r;
#pragma unroll
                for (int j = 0; j < 4; ++j) { c1[j] = cp[256 * j + i1]; c2[j] = cp[256 * j + i2]; }
                st[0] = sp[m * 64 + r]; st[1] = sp[m * 64 + 32 + r]; st[2] = sp[(m + 4) * 64 + r]; st[3] = sp[(m + 4) * 64 + 32 + r];
            }
            SB();
            const K1Frag<NQ>& f = fr[i & 1];
            const u32x4 ua = __builtin_bit_cast(u32x4, f.a);
            const int pk = i == 3 ? kt + 3 : kt + 2, pj = (i == 3 || (kt == 0 && i == 0)) ? 0 : 3;
            const bool pon = pk < 7 && (i == 3 || i == 0 || (i == 1 && kt == 0));
            accz[0] = MFMA32(f.z0, f.a, accz[0]);
            if (i < 2) { stat(ua[0]); if (NQ == 1) stat(ua[1]); }
            if (i == 2) csz[0] = MFMA16(f.z0, sel, csz[0]);
            if (pon) piece(pk, pj);
            SB();
            accz[1] = MFMA32(f.z1, f.a, accz[1]);
            if (i < 2) stat(ua[NQ == 1 ? 2 : 1]);
            if (i == 2) csz[1] = MFMA16(f.z1, sel, csz[1]);
            if (pon) piece(pk, pj + 1);
            SB();
            accq[0] = ISV ? MFMA32(f.a, f.wq[0], accq[0]) : MFMA32(f.wq[0], f.a, accq[0]);
            if (i < 2) stat(ua[NQ == 1 ? 3 : 2]);
            if (i == 2) csq[0] = MFMA16(f.wq[0], sel, csq[0]);
            if (pon) piece(pk, pj + 2);
            SB();
            if (NQ == 2) {
                accq[NQ - 1] = ISV ? MFMA32(f.a, f.wq[NQ - 1], accq[NQ - 1]) : MFMA32(f.wq[NQ - 1], f.a, accq[NQ - 1]);
                if (i < 2) stat(ua[3]);
                if (i == 2) csq[NQ - 1] = MFMA16(f.wq[NQ - 1], sel, csq[NQ - 1]);
            }
            if (kt == 6 && i == 1) sp[wid * 64 + lane] = f32x2{sx[0] + sx[1], sq[0] + sq[1]};
            if (kt == 6 && i == 2) {
                SB();
                if ((lane & 15) < 2) {
                    AS3 float* d = cp + m * 256 + (lane & 15) * 16 + (lane >> 4) * 4;
                    *(AS3 f32x4*)(d + 64 * WN) = csz[0]; *(AS3 f32x4*)(d + 64 * WN + 32) = csz[1];
                    *(AS3 f32x4*)(d + (WN == 0 ? 160 : 128)) = csq[0];
                    if (NQ == 2) *(AS3 f32x4*)(d + 192) = csq[NQ - 1];
                }
            }
        }
        if (kt == 0) TMARK(2);
        if (kt == 5) TMARK(3);
    }
    SB();
    TMARK(4);
    pv[lane] = (c1[0] + c1[1]) + (c1[2] + c1[3]);
    pv[64 + lane] = (c2[0] + c2[1]) + (c2[2] + c2[3]);
    const f32x2 stt = (st[0] + st[1]) + (st[2] + st[3]);
    const float mu = stt[0] * (1.0f / 448.0f), var = fmaxf(stt[1] * (1.0f / 448.0f) - mu * mu, 0.f) + 1e-5f;
    const float rs = __builtin_amdgcn_rsqf(var), sd = var * rs;
    const f32x2 nmu2 = {-mu, -mu};
    if (ISV) *(AS3 f32x2*)(pv + 128 + 2 * r) = f32x2{rs, -mu * rs};
    f32x4 cz[2][4], cq[NQ][4], pab[8]; float csl[NQ];
#pragma unroll
    for (int t = 0; t < NQ; ++t) {
        if (ISV) csl[t] = pv[64 + 32 * t + r];
        else {
#pragma unroll
            for (int gq = 0; gq < 4; ++gq) cq[t][gq] = *(const AS3 f32x4*)(pv + 64 + 32 * t + 8 * gq + 4 * h);
        }
    }
    if (ISV) {
#pragma unroll
        for (int gq = 0; gq < 4; ++gq) { pab[2 * gq] = *(const AS3 f32x4*)(pv + 128 + 2 * (8 * gq + 4 * h)); pab[2 * gq + 1] = *(const AS3 f32x4*)(pv + 128 + 2 * (8 * gq + 4 * h) + 4); }
    }
#pragma unroll
    for (int t = 0; t < 2; ++t)
#pragma unroll
        for (int gq = 0; gq < 4; ++gq) cz[t][gq] = *(const AS3 f32x4*)(pv + 32 * t + 8 * gq + 4 * h);
    SB();
    TMARK(5);
    auto qkv_out = [&]() {
#pragma unroll
        for (int t = 0; t < NQ; ++t) {
            if (ISV) {
                unsigned o[8];
#pragma unroll
                for (int gq = 0; gq < 4; ++gq) {
                    const f32x4 p0 = pab[2 * gq], p1 = pab[2 * gq + 1];
                    o[2 * gq] = pk2(__builtin_fmaf(p0[0], accq[t][4 * gq + 0], p0[1] * csl[t]), __builtin_fmaf(p0[2], accq[t][4 * gq + 1], p0[3] * csl[t]));
                    o[2 * gq + 1] = pk2(__builtin_fmaf(p1[0], accq[t][4 * gq + 2], p1[1] * csl[t]), __builtin_fmaf(p1[2], accq[t][4 * gq + 3], p1[3] * csl[t]));
                }
#pragma unroll
                for (int s = 0; s < 2; ++s) *(u32x4*)(ws + qo[t][0] + s * 1024) = u32x4{o[4 * s], o[4 * s + 1], o[4 * s + 2], o[4 * s + 3]};
            } else {
                const int T = 3 * cg + (WN == 0 ? 1 + t : 0);
                const float a0 = rs * (T < 14 ? 0.18898223650461362f * 1.4426950408889634f : 1.0f);
                const f32x2 a2 = {a0, a0};
#pragma unroll
                for (int gq = 0; gq < 4; ++gq) {
                    const f32x4 c4 = cq[t][gq];
                    f32x2 v0 = a2 * fma2(nmu2, f32x2{c4[0], c4[1]}, f32x2{accq[t][4 * gq], accq[t][4 * gq + 1]});
                    f32x2 v1 = a2 * fma2(nmu2, f32x2{c4[2], c4[3]}, f32x2{accq[t][4 * gq + 2], accq[t][4 * gq + 3]});
                    *(u32x2*)(ws + qo[t][gq]) = u32x2{pk2v(v0), pk2v(v1)};
                }
            }
        }
        SB();
    };
    if (WN == 0) qkv_out(); else __builtin_amdgcn_s_setprio(2);
    const float A1 = -2.3022081985f * rs, A3 = (-2.3022081985f * 0.044715f) * rs * rs * rs;
    const f32x2 A1v = {A1, A1}, A3v = {A3, A3}, sdv = {sd, sd};
    f32x2 gz[2][8], zs = {0.f, 0.f}, zq = {0.f, 0.f};
#pragma unroll
    for (int t = 0; t < 2; ++t) {
        f32x2 u[8], e[8];
#pragma unroll
        for (int j = 0; j < 8; ++j) u[j] = fma2(nmu2, f32x2{cz[t][j >> 1][2 * (j & 1)], cz[t][j >> 1][2 * (j & 1) + 1]}, f32x2{accz[t][2 * j], accz[t][2 * j + 1]});
        SB();
#pragma unroll
        for (int j = 0; j < 8; ++j) e[j] = u[j] * u[j];
        SB();
#pragma unroll
        for (int j = 0; j < 8; ++j) e[j] = fma2(e[j], A3v, A1v);
        SB();
#pragma unroll
        for (int j = 0; j < 8; ++j) e[j] = e[j] * u[j];
        SB();
#pragma unroll
        for (int j = 0; j < 8; ++j) e[j] = f32x2{__builtin_amdgcn_exp2f(e[j][0]), __builtin_amdgcn_exp2f(e[j][1])};
        SB();
#pragma unroll
        for (int j = 0; j < 8; ++j) e[j] = fma2(e[j], sdv, sdv);
        SB();
#pragma unroll
        for (int j = 0; j < 8; ++j) e[j] = f32x2{__builtin_amdgcn_rcpf(e[j][0]), __builtin_amdgcn_rcpf(e[j][1])};
        SB();
#pragma unroll
        for (int j = 0; j < 8; ++j) gz[t][j] = e[j] * u[j];
        SB();
        if (WN == 0) {
            *(u32x4*)(ws + zo + t * 2048) = u32x4{pk2v(gz[t][0]), pk2v(gz[t][1]), pk2v(gz[t][2]), pk2v(gz[t][3])};
            *(u32x4*)(ws + zo + t * 2048 + 1024) = u32x4{pk2v(gz[t][4]), pk2v(gz[t][5]), pk2v(gz[t][6]), pk2v(gz[t][7])};
        } else {
            f32x2 a = gz[t][0] + gz[t][1], b = gz[t][0] * gz[t][0], c = gz[t][1] * gz[t][1];
#pragma unroll
            for (int j = 2; j < 8; j += 2) { zs += gz[t][j]; a += gz[t][j + 1]; b = fma2(gz[t][j], gz[t][j], b); c = fma2(gz[t][j + 1], gz[t][j + 1], c); }
            zs += a; zq += b + c;
        }
        SB();
    }
    TMARK(6);
    if (WN == 1) {
        const unsigned tagA = epoch * 0x9E3779B1u + 0x7F4A7C15u, tagB = epoch * 0x85EBCA6Bu + 0x1B873593u;
        const float ps = halfsum(zs[0] + zs[1]), pq = halfsum(zq[0] + zq[1]);
        const rsrc_t rg = mkrsrc(ws + WS_ST + (size_t)tt * 14 * 512, 14 * 512);
        if (h == 0) __builtin_amdgcn_raw_buffer_store_b128(u32x4{__float_as_uint(ps), tagA, __float_as_uint(pq), tagB}, rg, cg * 512 + r * 16, 0, 16);
        __builtin_amdgcn_s_setprio(0);
        qkv_out();
        float t1, t2;
        for (int spin = 0; spin < 100000; ++spin) {
            bool ok = true; t1 = 0.f; t2 = 0.f;
            u32x4 gr[7];
#pragma unroll
            for (int i = 0; i < 7; ++i) gr[i] = __builtin_amdgcn_raw_buffer_load_b128(rg, lane * 16 + i * 1024, 0, 16);
#pragma unroll
            for (int i = 0; i < 7; ++i) { ok &= gr[i][1] == tagA && gr[i][3] == tagB; t1 += __uint_as_float(gr[i][0]); t2 += __uint_as_float(gr[i][2]); }
            if (__all(ok)) break;
            asm volatile("" ::: "memory");
        }
        if (cg == 0 && lane == 0) __hip_atomic_store((gu32*)(ws + WS_EP + tt * 64), epoch + 1u, __ATOMIC_RELAXED, __HIP_MEMORY_SCOPE_AGENT);
        t1 = halfsum(t1); t2 = halfsum(t2);
        const float zm = t1 * (1.0f / 896.0f), zv = fmaxf(t2 * (1.0f / 896.0f) - zm * zm, 0.f);
        const float zr = __builtin_amdgcn_rsqf(zv + 1e-5f);
        const f32x2 zr2 = {zr, zr}, zb2 = {-zm * zr, -zm * zr};
        const lptr T = L + wid * 4096;
#pragma unroll
        for (int t = 0; t < 2; ++t)
#pragma unroll
            for (int gq = 0; gq < 4; ++gq)
                *(AS3 u32x2*)(T + (2 * t + (gq >> 1)) * 1024 + r * 32 + (gq & 1) * 16 + 8 * h) = u32x2{pk2v(fma2(gz[t][2 * gq], zr2, zb2)), pk2v(fma2(gz[t][2 * gq + 1], zr2, zb2))};
        const int kq = lane >> 4;
#pragma unroll
        for (int i = 0; i < 4; ++i) {
            const bf16x8 v = cat8(trread(T + i * 1024 + (8 * kq + q) * 32 + 8 * p), trread(T + i * 1024 + (8 * kq + 4 + q) * 32 + 8 * p));
            const int F = 4 * cg + i, j = F / 7, f = F - 7 * j;
            *(bf16x8*)(ws + (unsigned)WS_Z2N + (unsigned)(((j * 64 + tt) * 7 + f) * 1024) + (unsigned)(lane * 16)) = v;
        }
    }
    TMARK(7); TFLUSH(0);
}

DI void k1_helper(const unsigned short* wo, const unsigned short* wout, const char* wsp, char* ws, int sp) {
    const int tid = threadIdx.x, lane = tid & 63, wid = __builtin_amdgcn_readfirstlane(tid >> 6), gw = sp * 8 + wid;
    if (tid < 24) *(unsigned*)(ws + WS_FLG + sp * 512 + tid * 4) = 0u;
    {
        unsigned e[5][8];
        const int h = lane >> 5;
#pragma unroll
        for (int i = 0; i < 5; ++i) {
            const int f = min(gw + 256 * i, 14 * 88 - 1), nt = f / 88, ks = f - nt * 88, col = nt * 32 + (lane & 31);
            const unsigned short* src = ks < 32 ? wo + (size_t)((ks >> 1) * 28 + (ks & 1) * 16) * 448 : wout + (size_t)(ks - 32) * 16 * 448;
            const int hi = (ks < 32 && (ks & 1)) ? 0 : 4 * h;
#pragma unroll
            for (int j = 0; j < 8; ++j) e[i][j] = src[(8 * (j >> 2) + (j < 4 ? 4 * h : hi) + (j & 3)) * 448 + col];
        }
#pragma unroll
        for (int i = 0; i < 5; ++i) {
            const int f = min(gw + 256 * i, 14 * 88 - 1), ks = f % 88;
            const bool pad = ks < 32 && (ks & 1) && h;
            *(u32x4*)(ws + WS_BF + (size_t)f * 1024 + lane * 16) = u32x4{e[i][0] | (e[i][1] << 16), e[i][2] | (e[i][3] << 16), pad ? 0u : e[i][4] | (e[i][5] << 16), pad ? 0u : e[i][6] | (e[i][7] << 16)};
        }
    }
    for (int b = gw * 4; b < gw * 4 + 4; ++b) {
        char* blk = ws + WS_KVF + (size_t)b * 4096;
        if (lane < 32) *(u32x2*)(blk + 1024 + (32 + lane) * 16 + 8) = u32x2{0x00003f80u, 0u};
        else if (lane < 48) { const int i = lane - 32, s = i >> 3, hh = (i >> 2) & 1, d = 28 + (i & 3); const unsigned v = (i & 3) == 0 ? 0x3f803f80u : 0u;
            *(u32x4*)(blk + (2 + s) * 1024 + (hh * 32 + d) * 16) = u32x4{v, v, v, v}; }
    }
    for (int b = 0; b < 2; ++b) {
        u32x4 t[16];
#pragma unroll
        for (int i = 0; i < 16; ++i) t[i] = *(const u32x4*)(wsp + (size_t)((b * 16 + i) * 256 + gw) * 1024 + lane * 16);
#pragma unroll
        for (int i = 0; i < 16; ++i) asm volatile("" :: "v"(t[i]));
    }
}

__global__ __launch_bounds__(512, 2) void k1_kernel(const char* x, const char* wqkv, const char* win, const unsigned short* wo, const unsigned short* wout, const char* wsp, char* ws) {
    extern __shared__ __attribute__((aligned(16))) char smem[];
    TS0;
    const int bid = __builtin_amdgcn_readfirstlane(blockIdx.x), xcd = bid & 7, idx = bid >> 3;
    const int wid = __builtin_amdgcn_readfirstlane(threadIdx.x >> 6);
    if (idx >= 28) { k1_helper(wo, wout, wsp, ws, xcd * 4 + idx - 28); TS1(0); return; }
    const int mt = (xcd >> 1) * 4 + idx / 7, cg = (xcd & 1) * 7 + idx % 7;
    K1Src src;
    {
        const int lane = threadIdx.x & 63;
        const unsigned lds0 = (unsigned)(uintptr_t)(lptr)smem;
        const unsigned rowo = (mt * 128 + 16 * wid + (lane >> 3)) * 896;
        src.voA0 = rowo + (((lane & 7) ^ (lane >> 4)) << 4);
        src.voA1 = rowo + 8 * 896 + (((lane & 7) ^ (4 + (lane >> 4))) << 4);
        const int zc = (lane & 15) ^ ((lane >> 4) << 2);
        src.voZ = (8 * wid + (lane >> 4)) * 3584 + (zc >> 3) * 1792 + cg * 128 + (zc & 7) * 16;
        const int seg = wid < 4 ? ((cg & 1) ? 0 : 2) : (cg & 1);
        src.voQ = (16 * (wid & 3) + (lane >> 2)) * 2688 + cg * 192 + seg * 64 + (lane & 3) * 16;
        src.dA = lds0 + wid * 2048; src.dQ = lds0 + K1_BQ + seg * 4096 + (wid & 3) * 1024;
#pragma unroll
        for (int j = 0; j < 5; ++j) k1_piece(src, x, wqkv, win, 0, j);
        SB();
    }
    if (wid < 4) { if (cg >= 9) k1_gemm<0, true>(src, x, wqkv, win, ws, smem, mt, cg, wid); else k1_gemm<0, false>(src, x, wqkv, win, ws, smem, mt, cg, wid); }
    else { k1_piece(src, x, wqkv, win, 0, 5); SB();
        if (cg >= 10) k1_gemm<1, true>(src, x, wqkv, win, ws, smem, mt, cg, wid); else k1_gemm<1, false>(src, x, wqkv, win, ws, smem, mt, cg, wid); }
    TS1(0);
}

#define RG 1
#define RA 1
constexpr int K2_GP = 24576, K2_AT = 4 * K2_GP, K2_CNT = K2_AT + 4 * 12288, K2_LDS = K2_CNT + 16;
#define EX2(x) __builtin_amdgcn_exp2f(x)
DI void role_sync(AS3 unsigned* cnt) {
    asm volatile("s_waitcnt lgkmcnt(0)" ::: "memory");
    if ((threadIdx.x & 63) == 0) __hip_atomic_fetch_add(cnt, 1u, __ATOMIC_RELAXED, __HIP_MEMORY_SCOPE_WORKGROUP);
    while (__hip_atomic_load(cnt, __ATOMIC_RELAXED, __HIP_MEMORY_SCOPE_WORKGROUP) < 4u) __builtin_amdgcn_s_sleep(1);
    asm volatile("" ::: "memory");
}

DI void attn_store(char* ws, const f32x16& o, int head, int tt) {
    const int lane = threadIdx.x & 63;
    const float inv = 1.0f / hihalf(o[12]);
    const rsrc_t rw = mkrsrc(ws, WS_BYTES);
#pragma unroll
    for (int s = 0; s < 2; ++s)
        __builtin_amdgcn_raw_buffer_store_b128(u32x4{pk2(o[8 * s] * inv, o[8 * s + 1] * inv), pk2(o[8 * s + 2] * inv, o[8 * s + 3] * inv), pk2(o[8 * s + 4] * inv, o[8 * s + 5] * inv), pk2(o[8 * s + 6] * inv, o[8 * s + 7] * inv)},
                                               rw, (unsigned)WS_AF + (tt * 88 + 2 * head) * 1024 + lane * 16 + s * 1024, 0, 16);
}
DI void role_publish(AS3 unsigned* cnt, gu32* flag, int copies) {
    asm volatile("s_waitcnt vmcnt(0)" ::: "memory");
    if ((threadIdx.x & 63) == 0) {
        if (__hip_atomic_fetch_add(cnt, 1u, __ATOMIC_RELAXED, __HIP_MEMORY_SCOPE_WORKGROUP) == 3u) {
            __hip_atomic_store(flag, 1u, __ATOMIC_RELAXED, __HIP_MEMORY_SCOPE_AGENT);
            if (copies > 1) __hip_atomic_store(flag + 128, 1u, __ATOMIC_RELAXED, __HIP_MEMORY_SCOPE_AGENT);
        }
    }
}
__device__ __noinline__ void attn_safe(char* ws, int head, int qt) {
    const int lane = threadIdx.x & 63, h = lane >> 5;
    const char* qp = ws + WS_QF + ((size_t)head * 64 + qt) * 2048 + lane * 16;
    bf16x8 q0 = *(const bf16x8*)qp; u32x4 q1u = *(const u32x4*)(qp + 1024);
    if (h) { q1u[2] = 0u; q1u[3] = 0u; }
    const bf16x8 q1 = __builtin_bit_cast(bf16x8, q1u);
    f32x16 o, zero;
#pragma unroll
    for (int i = 0; i < 16; ++i) { o[i] = 0.f; zero[i] = 0.f; }
    float m = -3.0e38f;
    for (int kt = 0; kt < 64; ++kt) {
        const char* kp = ws + WS_KVF + ((size_t)head * 64 + kt) * 4096 + lane * 16;
        bf16x8 k0 = *(const bf16x8*)kp, k1 = *(const bf16x8*)(kp + 1024), v0 = *(const bf16x8*)(kp + 2048), v1 = *(const bf16x8*)(kp + 3072);
        f32x16 s = MFMA32(k0, q0, zero); s = MFMA32(k1, q1, s);
        float mx = s[0];
#pragma unroll
        for (int i = 1; i < 16; ++i) mx = fmaxf(mx, s[i]);
        mx = fmaxf(halfmax(mx), m);
        const float f = EX2(m - mx); m = mx;
        u32x4 p0, p1;
#pragma unroll
        for (int i = 0; i < 16; ++i) { o[i] *= f; s[i] = EX2(s[i] - m); }
#pragma unroll
        for (int i = 0; i < 4; ++i) { p0[i] = pk2(s[2 * i], s[2 * i + 1]); p1[i] = pk2(s[8 + 2 * i], s[9 + 2 * i]); }
        o = MFMA32(v0, __builtin_bit_cast(bf16x8, p0), o); o = MFMA32(v1, __builtin_bit_cast(bf16x8, p1), o);
    }
    attn_store(ws, o, head, qt);
}

DI void k2_attn(char* ws, lptr L, int head, int qb, int w TARG) {
    const int lane = threadIdx.x & 63, h = lane >> 5;
    const rsrc_t rw = mkrsrc(ws, WS_BYTES);
    unsigned kv = (unsigned)WS_KVF + head * 64 * 4096;
    const unsigned lo = lane * 16;
    bf16x8 q0[4]; u32x4 q1[4];
#pragma unroll
    for (int t = 0; t < 4; ++t) { const unsigned qp = (unsigned)WS_QF + (head * 64 + qb * 4 + ((w + t) & 3)) * 2048;
        q0[t] = bld8(rw, lo, qp); q1[t] = bld4(rw, lo + 1024, qp); }
    bf16x8 r0 = bld8(rw, lo, kv), r1 = bld8(rw, lo + 1024, kv);
    kv += w * 16 * 4096;
    const unsigned rho = (qb & (RA - 1)) * 4096; unsigned tq = 0;
#define KVO(off) (kv + ((tq + (off)) ^ rho))
    bf16x8 ka0 = bld8(rw, lo, KVO(0)), ka1 = bld8(rw, lo + 1024, KVO(0)), va0 = bld8(rw, lo + 2048, KVO(0)), va1 = bld8(rw, lo + 3072, KVO(0));
    bf16x8 kb0 = bld8(rw, lo, KVO(4096)), kb1 = bld8(rw, lo + 1024, KVO(4096)), vb0, vb1;
    f32x16 zero, o[4], sA, sB;
    u32x4 p0, p1 = {0u, 0u, 0u, 0u};
#pragma unroll
    for (int i = 0; i < 16; ++i) { zero[i] = 0.f; o[0][i] = 0.f; o[1][i] = 0.f; o[2][i] = 0.f; o[3][i] = 0.f; }
#pragma unroll
    for (int i = 0; i < 8; ++i) vb1[i] = 0;
#pragma unroll
    for (int t = 0; t < 4; ++t) {
        if (h) { q1[t][2] = 0u; q1[t][3] = 0u; }
        f32x16 s = MFMA32(r0, q0[t], zero); s = MFMA32(r1, __builtin_bit_cast(bf16x8, q1[t]), s);
        float mx = fmaxf(s[0], s[1]);
#pragma unroll
        for (int i = 2; i < 16; i += 2) mx = fmaxf(fmaxf(mx, s[i]), s[i + 1]);
        mx = halfmax(mx);
        if (h) q1[t][2] = pk2(-(mx + 32.0f), 0.f);
    }
#define Q1(t) __builtin_bit_cast(bf16x8, q1[t])
#define PB(x) __builtin_bit_cast(bf16x8, x)
#define ASTEP(A, B, O, V0, OP, VP, K0, K1, QN) do { \
    B = MFMA32(K0, q0[QN], zero); SB(); \
    A[0] = EX2(A[0]); A[1] = EX2(A[1]); A[2] = EX2(A[2]); A[3] = EX2(A[3]); PIN(A); SB(); \
    OP = MFMA32(VP, PB(p1), OP); SB(); \
    p0[0] = pk2(A[0], A[1]); p0[1] = pk2(A[2], A[3]); SB(); A[4] = EX2(A[4]); A[5] = EX2(A[5]); A[6] = EX2(A[6]); A[7] = EX2(A[7]); PIN(A); PIN(p0); SB(); \
    B = MFMA32(K1, Q1(QN), B); SB(); \
    p0[2] = pk2(A[4], A[5]); p0[3] = pk2(A[6], A[7]); SB(); A[8] = EX2(A[8]); A[9] = EX2(A[9]); A[10] = EX2(A[10]); A[11] = EX2(A[11]); PIN(A); PIN(p0); SB(); \
    O = MFMA32(V0, PB(p0), O); SB(); \
    A[12] = EX2(A[12]); A[13] = EX2(A[13]); A[14] = EX2(A[14]); A[15] = EX2(A[15]); SB(); \
    p1[0] = pk2(A[8], A[9]); p1[1] = pk2(A[10], A[11]); SB(); p1[2] = pk2(A[12], A[13]); SB(); p1[3] = pk2(A[14], A[15]); PIN(p1); SB(); } while (0)
#define LDK(K0, K1, off) do { K0 = bld8(rw, lo, KVO(off)); K1 = bld8(rw, lo + 1024, KVO(off)); SB(); } while (0)
#define LDV(V0, V1, off) do { V0 = bld8(rw, lo + 2048, KVO(off)); V1 = bld8(rw, lo + 3072, KVO(off)); SB(); } while (0)
#define APAIR(MORE) do { \
    ASTEP(sA, sB, o[0], va0, o[3], vb1, ka0, ka1, 1); LDV(vb0, vb1, 4096); \
    ASTEP(sB, sA, o[1], va0, o[0], va1, ka0, ka1, 2); \
    ASTEP(sA, sB, o[2], va0, o[1], va1, ka0, ka1, 3); if (MORE) LDK(ka0, ka1, 8192); \
    ASTEP(sB, sA, o[3], va0, o[2], va1, kb0, kb1, 0); \
    ASTEP(sA, sB, o[0], vb0, o[3], va1, kb0, kb1, 1); if (MORE) LDV(va0, va1, 8192); \
    ASTEP(sB, sA, o[1], vb0, o[0], vb1, kb0, kb1, 2); \
    ASTEP(sA, sB, o[2], vb0, o[1], vb1, kb0, kb1, 3); if (MORE) LDK(kb0, kb1, 12288); \
    ASTEP(sB, sA, o[3], vb0, o[2], vb1, ka0, ka1, 0); tq += 8192; } while (0)
    TMARK(1);
    sA = MFMA32(ka0, q0[0], zero); sA = MFMA32(ka1, Q1(0), sA);
    for (int it = 0; it < 7; ++it) APAIR(true);
    APAIR(false);
    o[3] = MFMA32(vb1, PB(p1), o[3]);
    TMARK(2);
    const lptr P = L + K2_AT;
#pragma unroll
    for (int t = 1; t < 4; ++t)
#pragma unroll
        for (int g = 0; g < 4; ++g) *(AS3 f32x4*)(P + (w * 3 + t - 1) * 4096 + g * 1024 + lane * 16) = f32x4{o[t][4 * g], o[t][4 * g + 1], o[t][4 * g + 2], o[t][4 * g + 3]};
    role_sync((AS3 unsigned*)(L + K2_CNT));
#pragma unroll
    for (int t = 1; t < 4; ++t)
#pragma unroll
        for (int g = 0; g < 4; ++g) { f32x4 v = *(const AS3 f32x4*)(P + (((w - t) & 3) * 3 + t - 1) * 4096 + g * 1024 + lane * 16);
#pragma unroll
            for (int c = 0; c < 4; ++c) o[0][4 * g + c] += v[c]; }
    float chk = 0.f;
#pragma unroll
    for (int i = 0; i < 16; ++i) chk = __builtin_fmaf(o[0][i], 0.f, chk);
    const float l = hihalf(o[0][12]);
    const bool bad = !(chk == 0.f) || !(l > 1.0e-30f) || !(l < 1.0e30f);
    if (__builtin_expect(__any(bad), 0)) attn_safe(ws, head, qb * 4 + w);
    else attn_store(ws, o[0], head, qb * 4 + w);
    TMARK(3);
    role_publish((AS3 unsigned*)(L + K2_CNT + 8), (gu32*)(ws + WS_FLG) + qb * 256 + head, 2);
    TMARK(4);
}

constexpr int g_issued_before(int s, int f) {
    int n = 45;
    for (int t = 0; t <= s; ++t) {
        if (t == 13) n += 8;
        for (int g = 0; g < 7; ++g) {
            if (t == s && g == f) return n;
            if (t + 3 < 16) ++n;
            if ((t & 1) && (t >> 1) + 3 < 8) n += g == 6 ? 2 : 1;
        }
    }
    return n;
}
constexpr int g_pos_z(int s, int f) {
    if (s == 0) return 8 + f + 1; if (s == 1) return 23 + f + 1; if (s == 2) return 30 + f + 1;
    return g_issued_before(s - 3, f) + 1;
}
constexpr int g_pos_w(int ds) {
    if (ds == 0) return 8; if (ds == 1) return 23; if (ds == 2) return 45;
    return g_issued_before(2 * (ds - 3) + 2, 0) - (2 * (ds - 3) + 2 == 13 ? 8 : 0);
}
template <int N> DI void vm_wait() { asm volatile("s_waitcnt vmcnt(%0)" :: "i"(N) : "memory"); }
template <int N> DI void vm_wait_for(bf16x8& v) { asm volatile("s_waitcnt vmcnt(%1)" : "+v"(v) : "i"(N)); }
template <int OFF> DI void abld(bf16x8& d, unsigned voff, const u32x4& rs, unsigned soff) {
    asm volatile("buffer_load_dwordx4 %0, %1, %2, %3 offen offset:%4" : "=v"(d) : "v"(voff), "s"(rs), "s"(soff), "i"(OFF));
}
template <int OFF> DI void abld_rw(bf16x8& d, unsigned voff, const u32x4& rs, unsigned soff) {
    asm volatile("buffer_load_dwordx4 %0, %1, %2, %3 offen offset:%4" : "+v"(d) : "v"(voff), "s"(rs), "s"(soff), "i"(OFF));
}
DI u32x4 rawrsrc(const void* p, unsigned bytes) { const unsigned long long a = (unsigned long long)p; return u32x4{(unsigned)a, (unsigned)(a >> 32) & 0xffffu, bytes, 0x00020000u}; }

template <int S> struct GStep {
    template <class C> static DI void run(C& c) {
        constexpr int ds = S >> 1, ks = S & 1;
        if constexpr (S + 1 < 16) {
            constexpr int nds = (S + 1) >> 1;
            if constexpr (ks == 1) vm_wait<g_issued_before(S, 0) - (S == 13 ? 8 : 0) - g_pos_w(nds)>();
#pragma unroll
            for (int t = 0; t < 4; ++t) c.wf[(S + 1) & 1][t] = *(const AS3 bf16x8*)(c.W + (nds % 3) * 8192 + t * 2048 + (ks ? c.rd0 : c.rd1));
        }
        if constexpr (S == 13) c.epi_loads();
        SB();
        group<0>(c); group<1>(c); group<2>(c); group<3>(c); group<4>(c); group<5>(c); group<6>(c);
        if constexpr (S + 1 < 16) GStep<S + 1>::run(c);
    }
    template <int F, class C> static DI void group(C& c) {
        constexpr int ds = S >> 1, ks = S & 1;
        vm_wait_for<g_issued_before(S, F) - g_pos_z(S, F)>(c.z[S % 3][F]);
#pragma unroll
        for (int t = 0; t < 4; ++t) c.acc[F][t] = MFMA16(c.z[S % 3][F], c.wf[S & 1][t], c.acc[F][t]);
        SB();
        if constexpr (S + 3 < 16) { constexpr int u = ((S + 3) & 1) * 7 + F; abld_rw<(u & 3) * 1024>(c.z[S % 3][F], c.lo, c.rw, c.zsrc + (((S + 3) >> 1) ^ c.rho) * 14336 + (u >> 2) * 4096); }
        if constexpr (ks == 1 && ds + 3 < 8) { c.wpiece(ds + 3, F); if constexpr (F == 6) c.wpiece(ds + 3, 7); }
        if constexpr (S == 15) {
#pragma unroll
            for (int t = 1; t < 4; ++t) if (F > 0) *(AS3 f32x4*)(c.W + ((t - 1) * 7 + F - 1) * 1024 + c.lo) = c.acc[F > 0 ? F - 1 : 0][t];
        }
        SB();
    }
};
struct GCtx {
    lptr W; unsigned lo, zsrc, wv0, wv1, wdst, zo, bo; int rd0, rd1, w, rho; const char* wsp; u32x4 rw, rb;
    f32x4 acc[7][4]; bf16x8 z[3][7], wf[2][4]; u32x2 z1[7]; unsigned braw;
    DI void wpiece(int ds, int i) { glds16((i & 1) ? wv1 : wv0, wsp + ((8 * i + 16 * w) & 63) * 4096 + (ds ^ rho) * 128, wdst + (ds % 3) * 8192 + i * 1024); }
    DI void epi_loads() {
        asm volatile("buffer_load_dwordx2 %0, %4, %5, 0 offen\n\tbuffer_load_dwordx2 %1, %4, %5, 0 offen offset:1024\n\tbuffer_load_dwordx2 %2, %4, %5, 0 offen offset:2048\n\tbuffer_load_dwordx2 %3, %4, %5, 0 offen offset:3072"
                     : "=&v"(z1[0]), "=&v"(z1[1]), "=&v"(z1[2]), "=&v"(z1[3]) : "v"(zo), "s"(rw));
        asm volatile("buffer_load_dwordx2 %0, %4, %5, %6 offen\n\tbuffer_load_dwordx2 %1, %4, %5, %6 offen offset:1024\n\tbuffer_load_dwordx2 %2, %4, %5, %6 offen offset:2048\n\tbuffer_load_ushort %3, %7, %8, 0 offen"
                     : "=&v"(z1[4]), "=&v"(z1[5]), "=&v"(z1[6]), "=&v"(braw) : "v"(zo), "s"(rw), "s"(4096u), "v"(bo), "s"(rb));
    }
};

DI void k2_gemm(const char* wsp, const unsigned short* bsp, char* ws, lptr L, int mi, int j, int w TARG) {
    const int lane = threadIdx.x & 63, r = lane & 15, kq = lane >> 4;
    GCtx c;
    c.W = L + w * K2_GP; c.w = w; c.rho = (mi + j) & (RG - 1); c.wsp = wsp; c.lo = lane * 16;
    c.rw = rawrsrc(ws, WS_BYTES); c.rb = rawrsrc(bsp, 4096);
    c.zsrc = (unsigned)WS_Z2N + (j * 64 + w * 16) * 7 * 1024;
    const unsigned wvo = (mi * 64 + (lane >> 3)) * 4096 + w * 1024;
    c.wv0 = wvo + (((lane & 7) ^ (lane >> 4)) << 4); c.wv1 = wvo + (((lane & 7) ^ (4 + (lane >> 4))) << 4);
    c.wdst = (unsigned)(uintptr_t)c.W;
#pragma unroll
    for (int i = 0; i < 8; ++i) c.wpiece(0, i);
#define ZP1(s, f) abld<((((s) & 1) * 7 + (f)) & 3) * 1024>(c.z[s][f], c.lo, c.rw, c.zsrc + (((s) >> 1) ^ c.rho) * 14336 + ((((s) & 1) * 7 + (f)) >> 2) * 4096)
#define ZPRO(s) ZP1(s, 0); ZP1(s, 1); ZP1(s, 2); ZP1(s, 3); ZP1(s, 4); ZP1(s, 5); ZP1(s, 6)
    ZPRO(0);
#pragma unroll
    for (int i = 0; i < 8; ++i) c.wpiece(1, i);
    ZPRO(1); ZPRO(2);
#pragma unroll
    for (int i = 0; i < 8; ++i) c.wpiece(2, i);
    c.rd0 = r * 128 + ((kq ^ (r >> 1)) << 4); c.rd1 = c.rd0 ^ 64;
    const int tok = mi * 64 + w * 16 + r, tt = mi * 2 + (w >> 1);
    const unsigned uo = ((kq & 1) * 32 + (tok & 31)) * 16 + (kq >> 1) * 8;
    c.zo = (unsigned)WS_ZF + (tt * 56 + 7 * j) * 1024 + uo; c.bo = tok * 2;
#pragma unroll
    for (int a = 0; a < 7; ++a)
#pragma unroll
        for (int b = 0; b < 4; ++b) c.acc[a][b] = f32x4{0.f, 0.f, 0.f, 0.f};
    vm_wait<45 - 8>();
    TMARK(1);
#pragma unroll
    for (int t = 0; t < 4; ++t) c.wf[0][t] = *(const AS3 bf16x8*)(c.W + t * 2048 + c.rd0);
    SB();
    GStep<0>::run(c);
    TMARK(2);
#pragma unroll
    for (int t = 1; t < 4; ++t) *(AS3 f32x4*)(c.W + ((t - 1) * 7 + 6) * 1024 + c.lo) = c.acc[6][t];
    role_sync((AS3 unsigned*)(L + K2_CNT + 4));
    asm volatile("s_waitcnt vmcnt(0)" : "+v"(c.z1[0]), "+v"(c.z1[1]), "+v"(c.z1[2]), "+v"(c.z1[3]), "+v"(c.z1[4]), "+v"(c.z1[5]), "+v"(c.z1[6]), "+v"(c.braw));
    const float bias = bflo(c.braw);
    u32x2 o[8];
#pragma unroll
    for (int f = 0; f < 7; ++f) {
        f32x4 g = c.acc[f][0];
#pragma unroll
        for (int t = 1; t < 4; ++t) g += *(const AS3 f32x4*)(L + ((w - t) & 3) * K2_GP + ((t - 1) * 7 + f) * 1024 + c.lo);
        o[f] = u32x2{pk2((g[0] + bias) * bflo(c.z1[f][0]), (g[1] + bias) * bfhi(c.z1[f][0])), pk2((g[2] + bias) * bflo(c.z1[f][1]), (g[3] + bias) * bfhi(c.z1[f][1]))};
    }
    o[7] = o[6];
    const rsrc_t rws = mkrsrc(ws, WS_BYTES);
    const unsigned ho = (unsigned)WS_AF + (tt * 88 + 32 + 7 * j) * 1024 + ((kq & 1) * 32 + (tok & 31)) * 16 + (kq >> 1) * 1024;
#pragma unroll
    for (int f = 0; f < 8; f += 2) {
        const auto a = __builtin_amdgcn_permlane32_swap(o[f][0], o[f + 1][0], false, false), b = __builtin_amdgcn_permlane32_swap(o[f][1], o[f + 1][1], false, false);
        if (f < 6 || lane < 32) __builtin_amdgcn_raw_buffer_store_b128(u32x4{a[0], b[0], a[1], b[1]}, rws, ho + f * 1024, 0, 16);
    }
    TMARK(3);
    role_publish((AS3 unsigned*)(L + K2_CNT + 12), (gu32*)(ws + WS_FLG) + mi * 128 + 16 + j, 1);
    TMARK(4);
}

#define PSLEEP 4
constexpr int P3_ROW = 272, P3_TILE = 64 * P3_ROW;
DI void k2_out(const char* x, char* out, char* ws, lptr L, int mi, int nj, int w TARG) {
    const int tid = threadIdx.x, lane = tid & 63, r = lane & 31, h = lane >> 5;
    const rsrc_t rw = mkrsrc(ws, WS_BYTES);
    const unsigned lo = lane * 16, bo = (unsigned)WS_BF + (nj * 2 * 88 + w * 11) * 1024, ao = (unsigned)WS_AF + (mi * 2 * 88 + w * 11) * 1024;
    bf16x8 a[11][2], b[11][2];
    while (__hip_atomic_load((AS3 unsigned*)(L + K2_CNT + 8), __ATOMIC_RELAXED, __HIP_MEMORY_SCOPE_WORKGROUP) + __hip_atomic_load((AS3 unsigned*)(L + K2_CNT + 12), __ATOMIC_RELAXED, __HIP_MEMORY_SCOPE_WORKGROUP) < 8u) __builtin_amdgcn_s_sleep(2);
    asm volatile("" ::: "memory");
#pragma unroll
    for (int s = 0; s < 11; ++s) { b[s][0] = bld8(rw, lo + (s & 3) * 1024, bo + (s >> 2) * 4096); b[s][1] = bld8(rw, lo + (s & 3) * 1024, bo + 88 * 1024 + (s >> 2) * 4096); }
    const u32x4 xr = *(const u32x4*)(x + (unsigned)((mi * 64 + (tid >> 3)) * 896 + nj * 128 + (tid & 7) * 16));
    SB();
    if (w == 0) {
        gu32* fp = (gu32*)(ws + WS_FLG) + mi * 128 + (lane < 24 ? lane : 0);
        for (int spin = 0; spin < 200000; ++spin) {
            const unsigned v = __hip_atomic_load(fp, __ATOMIC_RELAXED, __HIP_MEMORY_SCOPE_AGENT);
            if (__all(v != 0u)) break;
            __builtin_amdgcn_s_sleep(PSLEEP);
        }
    }
    asm volatile("s_waitcnt lgkmcnt(0)\n\ts_barrier" ::: "memory");
    TMARK(5);
#pragma unroll
    for (int s = 0; s < 11; ++s) {
        a[s][0] = __builtin_bit_cast(bf16x8, __builtin_amdgcn_raw_buffer_load_b128(rw, lo + (s & 3) * 1024, ao + (s >> 2) * 4096, 16));
        a[s][1] = __builtin_bit_cast(bf16x8, __builtin_amdgcn_raw_buffer_load_b128(rw, lo + (s & 3) * 1024, ao + 88 * 1024 + (s >> 2) * 4096, 16));
    }
    SB();
    f32x16 acc[2][2];
#pragma unroll
    for (int i = 0; i < 16; ++i) { acc[0][0][i] = 0.f; acc[0][1][i] = 0.f; acc[1][0][i] = 0.f; acc[1][1][i] = 0.f; }
#pragma unroll
    for (int s = 0; s < 11; ++s) {
        acc[0][0] = MFMA32(b[s][0], a[s][0], acc[0][0]); acc[0][1] = MFMA32(b[s][1], a[s][0], acc[0][1]);
        acc[1][0] = MFMA32(b[s][0], a[s][1], acc[1][0]); acc[1][1] = MFMA32(b[s][1], a[s][1], acc[1][1]);
    }
    TMARK(6);
#pragma unroll
    for (int tm = 0; tm < 2; ++tm)
#pragma unroll
        for (int tn = 0; tn < 2; ++tn)
#pragma unroll
            for (int g = 0; g < 4; ++g)
                *(AS3 f32x4*)(L + w * P3_TILE + (tm * 32 + r) * P3_ROW + (tn * 32 + 8 * g + 4 * h) * 4) =
                    f32x4{acc[tm][tn][4 * g], acc[tm][tn][4 * g + 1], acc[tm][tn][4 * g + 2], acc[tm][tn][4 * g + 3]};
    __syncthreads();
    const int row = tid >> 3, c8 = tid & 7;
    f32x4 s0 = {0.f, 0.f, 0.f, 0.f}, s1 = s0;
#pragma unroll
    for (int p = 0; p < 8; ++p) { s0 += *(const AS3 f32x4*)(L + p * P3_TILE + row * P3_ROW + c8 * 32); s1 += *(const AS3 f32x4*)(L + p * P3_TILE + row * P3_ROW + c8 * 32 + 16); }
    const u32x4 o = {pk2(s0[0] + bflo(xr[0]), s0[1] + bfhi(xr[0])), pk2(s0[2] + bflo(xr[1]), s0[3] + bfhi(xr[1])),
                     pk2(s1[0] + bflo(xr[2]), s1[1] + bfhi(xr[2])), pk2(s1[2] + bflo(xr[3]), s1[3] + bfhi(xr[3]))};
    *(u32x4*)(out + (unsigned)((mi * 64 + row) * 896 + nj * 128 + c8 * 16)) = o;
    TMARK(7);
}

__global__ __launch_bounds__(512, 2) void k2_kernel(const char* wsp, const unsigned short* bsp, const char* x, char* out, char* ws) {
    extern __shared__ __attribute__((aligned(16))) char smem[];
    TS0; TDECL; TMARK(0);
    const int bid = __builtin_amdgcn_readfirstlane(blockIdx.x), xcd = bid & 7, idx = bid >> 3;
    const int wid = __builtin_amdgcn_readfirstlane(threadIdx.x >> 6);
    if (threadIdx.x < 4) *(AS3 unsigned*)((lptr)smem + K2_CNT + threadIdx.x * 4) = 0u;
    __syncthreads();
    if (wid < 4) k2_attn(ws, (lptr)smem, 2 * xcd + (idx >> 4), idx & 15, wid TPASS);
    else k2_gemm(wsp, bsp, ws, (lptr)smem, (xcd >> 1) * 8 + (idx >> 2), (xcd & 1) * 4 + (idx & 3), wid - 4 TPASS);
    if (idx < 28) k2_out(x, out, ws, (lptr)smem, xcd * 4 + idx / 7, idx % 7, wid TPASS);
    TFLUSH(2);
    TS1(2);
}

extern "C" void kernel_launch(void* const* d_in, const int* in_sizes, int n_in, void* d_out, int out_size, void* d_ws, size_t ws_size, hipStream_t stream) {
    const char* x = (const char*)d_in[0]; const char* wqkv = (const char*)d_in[1]; const unsigned short* wo = (const unsigned short*)d_in[2];
    const char* win = (const char*)d_in[4]; const char* wsp = (const char*)d_in[5]; const unsigned short* bsp = (const unsigned short*)d_in[6];
    const unsigned short* wout = (const unsigned short*)d_in[7];
    char* ws = (char*)d_ws;
    (void)hipFuncSetAttribute((const void*)k1_kernel, hipFuncAttributeMaxDynamicSharedMemorySize, K1_LDS);
    (void)hipFuncSetAttribute((const void*)k2_kernel, hipFuncAttributeMaxDynamicSharedMemorySize, K2_LDS);
    k1_kernel<<<256, 512, K1_LDS, stream>>>(x, wqkv, win, wo, wout, wsp, ws);
    k2_kernel<<<256, 512, K2_LDS, stream>>>(wsp, bsp, x, (char*)d_out, ws);
}
```

```cpp
#include <hip/hip_runtime.h>
#include <hip/hip_bf16.h>
#include <stdint.h>

typedef short bf16x8 __attribute__((ext_vector_type(8)));
typedef short s16x4 __attribute__((ext_vector_type(4)));
typedef float f32x16 __attribute__((ext_vector_type(16)));
typedef float f32x4 __attribute__((ext_vector_type(4)));
typedef float f32x2 __attribute__((ext_vector_type(2)));
typedef unsigned u32x4 __attribute__((ext_vector_type(4)));
typedef unsigned u32x2 __attribute__((ext_vector_type(2)));
typedef __bf16 bf2 __attribute__((ext_vector_type(2)));
typedef unsigned long long u64;
#define AS3 __attribute__((address_space(3)))
typedef AS3 char* lptr;
typedef __attribute__((address_space(1))) unsigned gu32;
typedef __attribute__((address_space(1))) u64 gu64;
typedef __amdgpu_buffer_rsrc_t rsrc_t;
#define DI __device__ __forceinline__
#define SB() __builtin_amdgcn_sched_barrier(0)
#define PIN(x) asm volatile("" : "+v"(x))
#define MFMA32(a, b, c) __builtin_amdgcn_mfma_f32_32x32x16_bf16((a), (b), (c), 0, 0, 0)
#define MFMA16(a, b, c) __builtin_amdgcn_mfma_f32_16x16x32_bf16((a), (b), (c), 0, 0, 0)
#define EX2(x) __builtin_amdgcn_exp2f(x)
#define RLX_AGENT __ATOMIC_RELAXED, __HIP_MEMORY_SCOPE_AGENT
#define RLX_WG __ATOMIC_RELAXED, __HIP_MEMORY_SCOPE_WORKGROUP

constexpr unsigned WS_QF = 0;
constexpr unsigned WS_KVF = 2u << 20;
constexpr unsigned WS_ZF = 6u << 20;
constexpr unsigned WS_Z2N = 10u << 20;
constexpr unsigned WS_AF = 14u << 20;
constexpr unsigned WS_BF = 20u << 20;
constexpr unsigned WS_ST = WS_BF + (3u << 19);
constexpr unsigned WS_EP = 22u << 20;
constexpr unsigned WS_F1 = WS_EP + 4096;
constexpr unsigned WS_F2 = WS_F1 + 49152;
constexpr unsigned WS_FH = WS_F2 + 16384;
constexpr unsigned WS_BYTES = 23u << 20;
constexpr size_t WS_DBG = 28u << 20;

#define TDECL do {} while (0)
#define TARG
#define TPASS
#define TMARK(i) do {} while (0)
#define TFLUSH() do {} while (0)

DI unsigned pk2(float a, float b) { f32x2 v = {a, b}; return __builtin_bit_cast(unsigned, __builtin_convertvector(v, bf2)); }
DI unsigned pk2v(f32x2 v) { return __builtin_bit_cast(unsigned, __builtin_convertvector(v, bf2)); }
DI float bflo(unsigned u) { return __uint_as_float(u << 16); }
DI float bfhi(unsigned u) { return __uint_as_float(u & 0xffff0000u); }
DI f32x2 fma2(f32x2 a, f32x2 b, f32x2 c) { return __builtin_elementwise_fma(a, b, c); }
DI float halfsum(float v) { auto r = __builtin_amdgcn_permlane32_swap(__float_as_uint(v), __float_as_uint(v), false, false); return __uint_as_float(r[0]) + __uint_as_float(r[1]); }
DI float halfmax(float v) { auto r = __builtin_amdgcn_permlane32_swap(__float_as_uint(v), __float_as_uint(v), false, false); return fmaxf(__uint_as_float(r[0]), __uint_as_float(r[1])); }
DI float hihalf(float v) { auto r = __builtin_amdgcn_permlane32_swap(__float_as_uint(v), __float_as_uint(v), false, false); return __uint_as_float(r[1]); }
DI s16x4 trread(lptr p) { return __builtin_bit_cast(s16x4, __builtin_amdgcn_ds_read_tr16_b64_v4i16((AS3 s16x4*)p)); }
DI bf16x8 cat8(s16x4 a, s16x4 b) { return __builtin_shufflevector(a, b, 0, 1, 2, 3, 4, 5, 6, 7); }
DI void glds16(unsigned voff, const void* sbase, unsigned ldsbase) {
    unsigned sv;
    asm volatile("s_mov_b32 %0, m0\n\ts_mov_b32 m0, %3\n\ts_nop 0\n\tglobal_load_lds_dwordx4 %1, %2\n\ts_mov_b32 m0, %0"
                 : "=&s"(sv) : "v"(voff), "s"(sbase), "s"(ldsbase) : "memory");
}
DI rsrc_t mkrsrc(const void* p, unsigned bytes) { return __builtin_amdgcn_make_buffer_rsrc((void*)p, 0, bytes, 0x00020000); }
DI u32x4 ld4(rsrc_t r, unsigned voff, unsigned soff) { return __builtin_amdgcn_raw_buffer_load_b128(r, voff, soff, 16); }
DI bf16x8 ld8(rsrc_t r, unsigned voff, unsigned soff) { return __builtin_bit_cast(bf16x8, __builtin_amdgcn_raw_buffer_load_b128(r, voff, soff, 16)); }
DI void st4(rsrc_t r, unsigned voff, u32x4 v) { __builtin_amdgcn_raw_buffer_store_b128(v, r, voff, 0, 16); }
DI void st2(rsrc_t r, unsigned voff, u32x2 v) { __builtin_amdgcn_raw_buffer_store_b64(v, r, voff, 0, 16); }

constexpr int P1_SLOT = 45056, P1_B = 16384;
constexpr int P1_CP = 3 * P1_SLOT, P1_SP = P1_CP + 4096, P1_PRIV = P1_SP + 4096;
constexpr int P2_GP = 24576, P2_AT = 4 * P2_GP;
constexpr int L_CNT = P1_PRIV + 8192, LDS_BYTES = L_CNT + 64;
enum { C_SYNC_A, C_SYNC_B, C_PUB2_A, C_PUB2_B, C_PUB1_A, C_PUB1_B, C_RDY_A, C_RDY_B, C_PUBH, C_PUB1_Z };
DI AS3 unsigned* lcnt(lptr L, int i) { return (AS3 unsigned*)(L + L_CNT) + i; }
constexpr int SPIN_CAP = 100000;

DI void role_sync(AS3 unsigned* cnt) {
    asm volatile("s_waitcnt lgkmcnt(0)" ::: "memory");
    if ((threadIdx.x & 63) == 0) __hip_atomic_fetch_add(cnt, 1u, RLX_WG);
    while (__hip_atomic_load(cnt, RLX_WG) < 4u) __builtin_amdgcn_s_sleep(1);
    asm volatile("" ::: "memory");
}
DI void publish(AS3 unsigned* cnt, unsigned n, gu64* flag, int copies, int stride, u64 tag) {
    asm volatile("s_waitcnt vmcnt(0)" ::: "memory");
    const int lane = threadIdx.x & 63;
    unsigned old = 0;
    if (lane == 0) old = __hip_atomic_fetch_add(cnt, 1u, RLX_WG);
    if (__builtin_amdgcn_readfirstlane(old) == n - 1 && lane < copies) __hip_atomic_store(flag + lane * stride, tag, RLX_AGENT);
}
DI void wait_ready(AS3 unsigned* w) { while (__hip_atomic_load(w, RLX_WG) == 0u) __builtin_amdgcn_s_sleep(1); asm volatile("" ::: "memory"); }
DI void poll2(gu64* a, gu64* b, u64 tag) {
    for (int spin = 0; spin < SPIN_CAP; ++spin) {
        const u64 va = __hip_atomic_load(a, RLX_AGENT), vb = __hip_atomic_load(b, RLX_AGENT);
        if (__all(va == tag && vb == tag)) break;
        __builtin_amdgcn_s_sleep(2);
    }
    asm volatile("" ::: "memory");
}

#define WAITBAR(n) asm volatile("s_waitcnt vmcnt(" #n ") lgkmcnt(0)\n\ts_barrier" ::: "memory")
struct P1Src { unsigned voA0, voA1, voZ, voQ, dA, dZ, dQ; };
DI void p1_piece(const P1Src& s, const char* x, const char* wqkv, const char* win, int kt, int j) {
    const unsigned so = (kt % 3) * P1_SLOT;
    if (j == 0) glds16(s.voA0, x + kt * 128, s.dA + so);
    if (j == 1) glds16(s.voA1, x + kt * 128, s.dA + so + 1024);
    if (j == 2) glds16(s.voZ, win + (size_t)kt * 64 * 3584, s.dZ + so);
    if (j == 3) glds16(s.voZ, win + (size_t)kt * 64 * 3584 + 64, s.dZ + so + 4096);
    if (j == 4) glds16(s.voQ, wqkv + (size_t)kt * 64 * 2688, s.dQ + so);
    if (j == 5) glds16(s.voQ, wqkv + (size_t)kt * 64 * 2688 + 64, s.dQ + so + 4096);
}
template <int NT> struct P1Frag { bf16x8 a, w[NT]; };
struct GW { unsigned wv0, wv1, wdst; int w; const char* wsp;
    DI void piece(int ds, int i) const { glds16((i & 1) ? wv1 : wv0, wsp + ((8 * i + 16 * w) & 63) * 4096 + ds * 128, wdst + (ds % 3) * 8192 + i * 1024); } };

DI void gelu16(f32x2 (&g)[8], const f32x16& acc, const f32x4 (&cs)[4], f32x2 nmu2, f32x2 A1v, f32x2 A3v, f32x2 sdv) {
    f32x2 u[8], e[8];
#pragma unroll
    for (int j = 0; j < 8; ++j) u[j] = fma2(nmu2, f32x2{cs[j >> 1][2 * (j & 1)], cs[j >> 1][2 * (j & 1) + 1]}, f32x2{acc[2 * j], acc[2 * j + 1]});
    SB();
#pragma unroll
    for (int j = 0; j < 8; ++j) e[j] = u[j] * u[j];
    SB();
#pragma unroll
    for (int j = 0; j < 8; ++j) e[j] = fma2(e[j], A3v, A1v);
    SB();
#pragma unroll
    for (int j = 0; j < 8; ++j) e[j] = e[j] * u[j];
    SB();
#pragma unroll
    for (int j = 0; j < 8; ++j) e[j] = f32x2{EX2(e[j][0]), EX2(e[j][1])};
    SB();
#pragma unroll
    for (int j = 0; j < 8; ++j) e[j] = fma2(e[j], sdv, sdv);
    SB();
#pragma unroll
    for (int j = 0; j < 8; ++j) e[j] = f32x2{__builtin_amdgcn_rcpf(e[j][0]), __builtin_amdgcn_rcpf(e[j][1])};
    SB();
#pragma unroll
    for (int j = 0; j < 8; ++j) g[j] = e[j] * u[j];
    SB();
}

template <int ROLE, int NV>
DI void phase1(const P1Src& src, const GW& gw, const char* x, const char* wqkv, const char* win, char* ws, lptr L, int mt, int cg, int wid, u64 tag TARG) {
    constexpr int NT = ROLE ? 4 : 3, TB = ROLE ? 0 : 4;
    const int lane = threadIdx.x & 63, r = lane & 31, h = lane >> 5, m = wid & 3;
    auto piece = [&](int kt, int j) { if (j < 5 || ROLE == 0) p1_piece(src, x, wqkv, win, kt, j); };
    const int q = (lane & 15) >> 2, p = lane & 3, ch = (lane >> 4) & 1;
    int offA[4], offW[4];
#pragma unroll
    for (int i = 0; i < 4; ++i) {
        const int ks = (i + m + 2 * ROLE) & 3;
        offA[i] = (m * 32 + r) * 128 + (((2 * ks + h) ^ ((r >> 1) & 7)) << 4);
        offW[i] = P1_B + TB * 4096 + (16 * ks + 8 * h + q) * 64 + 32 * ch + 8 * p;
    }
    f32x16 acc[NT]; f32x4 cs[NT];
#pragma unroll
    for (int t = 0; t < NT; ++t) { cs[t] = f32x4{0.f, 0.f, 0.f, 0.f};
#pragma unroll
        for (int i = 0; i < 16; ++i) acc[t][i] = 0.f; }
    float sx[2] = {0.f, 0.f}, sq[2] = {0.f, 0.f};
    const unsigned selw = (lane & 15) == ((lane >> 4) & 1) ? 0x3f803f80u : 0u;
    const bf16x8 sel = __builtin_bit_cast(bf16x8, u32x4{selw, selw, selw, selw});
    P1Frag<NT> fr[2];
    auto rd = [&](P1Frag<NT>& f, int kt, int i) {
        const lptr sb = L + (kt % 3) * P1_SLOT;
        f.a = *(const AS3 bf16x8*)(sb + offA[i]);
#pragma unroll
        for (int t = 0; t < NT; ++t) f.w[t] = cat8(trread(sb + offW[i] + 4096 * t), trread(sb + offW[i] + 4096 * t + 256));
    };
    auto stat = [&](unsigned u) { const float lo = bflo(u), hi = bfhi(u);
        sx[0] += lo; sx[1] += hi; sq[0] = __builtin_fmaf(lo, lo, sq[0]); sq[1] = __builtin_fmaf(hi, hi, sq[1]);
        PIN(sx[0]); PIN(sx[1]); PIN(sq[0]); PIN(sq[1]); };
    AS3 float* cp = (AS3 float*)(L + P1_CP);
    AS3 f32x2* sp = (AS3 f32x2*)(L + P1_SP);
    AS3 float* pv = (AS3 float*)(L + P1_PRIV + wid * 1024);
    f32x2 cpart[4], st[4];
    const int tt = mt * 4 + m;
    unsigned qo[3][4];
    if (ROLE == 0) {
#pragma unroll
        for (int t = 0; t < 3; ++t) {
            const int T = 3 * cg + t;
            if (t >= 3 - NV) { const int c = (T - 28) * 32 + r, head = c / 28, d = c - head * 28;
                qo[t][0] = WS_KVF + ((head * 64 + tt) * 4 + 2) * 1024 + (h * 32 + d) * 16; qo[t][1] = d; PIN(qo[t][0]); PIN(qo[t][1]);
            } else {
                const bool isq = T < 14; const int Tp = isq ? T : T - 14;
#pragma unroll
                for (int e = 0; e < 2; ++e) { const int U = 4 * Tp + 2 * e + h, hp = U / 7, u7 = U - 7 * hp, head = 2 * hp + (u7 > 3), uu = u7 & 3;
                    qo[t][e] = (isq ? WS_QF + (head * 64 + tt) * 2048 : WS_KVF + (head * 64 + tt) * 4096) + uu * 512 + r * 16; PIN(qo[t][e]); }
            }
        }
    }
    SB();
#pragma unroll
    for (int j = 0; j < 6; ++j) piece(1, j);
    if (ROLE == 0) WAITBAR(6); else WAITBAR(5);
    TMARK(1);
    rd(fr[0], 0, 0);
#pragma unroll
    for (int kt = 0; kt < 7; ++kt) {
#pragma unroll
        for (int i = 0; i < 4; ++i) {
            SB();
            if (i < 3) rd(fr[(i + 1) & 1], kt, i + 1);
            else if (kt < 6) {
                if (kt < 5) { if (ROLE == 0) WAITBAR(6); else WAITBAR(5); } else WAITBAR(0);
                rd(fr[0], kt + 1, 0);
            } else {
                asm volatile("s_waitcnt lgkmcnt(0)\n\ts_barrier" ::: "memory");
#pragma unroll
                for (int j = 0; j < 4; ++j) cpart[j] = *(const AS3 f32x2*)(cp + 256 * j + TB * 32 + 2 * lane);
                st[0] = sp[m * 64 + r]; st[1] = sp[m * 64 + 32 + r]; st[2] = sp[(m + 4) * 64 + r]; st[3] = sp[(m + 4) * 64 + 32 + r];
            }
            SB();
            const P1Frag<NT>& f = fr[i & 1];
            const u32x4 ua = __builtin_bit_cast(u32x4, f.a);
            const int pk = i == 3 ? kt + 3 : kt + 2, pj = (i == 3 || (kt == 0 && i == 0)) ? 0 : 3;
            const bool pon = pk < 7 && (i == 3 || i == 0 || (i == 1 && kt == 0));
#pragma unroll
            for (int t = 0; t < NT; ++t) {
                acc[t] = (ROLE == 0 && t >= 3 - NV) ? MFMA32(f.a, f.w[t], acc[t]) : MFMA32(f.w[t], f.a, acc[t]);
                if (i < 2) { stat(ua[t]); if (NT == 3 && t == 0) stat(ua[3]); }
                if (i == 2) cs[t] = MFMA16(f.w[t], sel, cs[t]);
                if (pon && t < 3) piece(pk, pj + t);
                SB();
            }
            if (kt == 6 && i == 1) sp[wid * 64 + lane] = f32x2{sx[0] + sx[1], sq[0] + sq[1]};
            if (kt == 6 && i == 2 && (lane & 15) < 2) {
#pragma unroll
                for (int t = 0; t < NT; ++t) *(AS3 f32x4*)(cp + m * 256 + (TB + t) * 32 + (lane & 15) * 16 + (lane >> 4) * 4) = cs[t];
            }
        }
    }
    SB();
    TMARK(2);
    *(AS3 f32x2*)(pv + 2 * lane) = (cpart[0] + cpart[1]) + (cpart[2] + cpart[3]);
    const f32x2 stt = (st[0] + st[1]) + (st[2] + st[3]);
    const float mu = stt[0] * (1.0f / 448.0f), var = fmaxf(stt[1] * (1.0f / 448.0f) - mu * mu, 0.f) + 1e-5f;
    const float rs = __builtin_amdgcn_rsqf(var);
    const f32x2 nmu2 = {-mu, -mu};
    const rsrc_t rw = mkrsrc(ws, WS_BYTES);
    if (ROLE == 0) {
        if (NV) *(AS3 f32x2*)(pv + 128 + 2 * r) = f32x2{rs, -mu * rs};
        f32x4 cq[3][4], pab[8]; float csl[3];
#pragma unroll
        for (int t = 0; t < 3; ++t) {
            if (t >= 3 - NV) csl[t] = pv[32 * t + r];
            else {
#pragma unroll
                for (int gq = 0; gq < 4; ++gq) cq[t][gq] = *(const AS3 f32x4*)(pv + 32 * t + 8 * gq + 4 * h);
            }
        }
        if (NV) {
#pragma unroll
            for (int gq = 0; gq < 4; ++gq) { pab[2 * gq] = *(const AS3 f32x4*)(pv + 128 + 2 * (8 * gq + 4 * h)); pab[2 * gq + 1] = *(const AS3 f32x4*)(pv + 128 + 2 * (8 * gq + 4 * h) + 4); }
        }
        SB();
#pragma unroll
        for (int t = 0; t < 3; ++t) {
            if (t >= 3 - NV) {
                unsigned o[8];
#pragma unroll
                for (int gq = 0; gq < 4; ++gq) {
                    const f32x4 p0 = pab[2 * gq], p1 = pab[2 * gq + 1];
                    o[2 * gq] = pk2(__builtin_fmaf(p0[0], acc[t][4 * gq + 0], p0[1] * csl[t]), __builtin_fmaf(p0[2], acc[t][4 * gq + 1], p0[3] * csl[t]));
                    o[2 * gq + 1] = pk2(__builtin_fmaf(p1[0], acc[t][4 * gq + 2], p1[1] * csl[t]), __builtin_fmaf(p1[2], acc[t][4 * gq + 3], p1[3] * csl[t]));
                }
                st4(rw, qo[t][0], u32x4{o[0], o[1], o[2], o[3]}); st4(rw, qo[t][0] + 1024, u32x4{o[4], o[5], o[6], o[7]});
                if (qo[t][1] >= 24u) {
                    const unsigned v = qo[t][1] == 24u ? 0x3f803f80u : 0u;
                    st4(rw, qo[t][0] + 64, u32x4{v, v, v, v}); st4(rw, qo[t][0] + 1024 + 64, u32x4{v, v, v, v});
                }
            } else {
                const int T = 3 * cg + t;
                const float a0 = rs * (T < 14 ? 0.18898223650461362f * 1.4426950408889634f : 1.0f);
                const f32x2 a2 = {a0, a0};
                unsigned o[8];
#pragma unroll
                for (int gq = 0; gq < 4; ++gq) {
                    const f32x4 c4 = cq[t][gq];
                    o[2 * gq] = pk2v(a2 * fma2(nmu2, f32x2{c4[0], c4[1]}, f32x2{acc[t][4 * gq], acc[t][4 * gq + 1]}));
                    o[2 * gq + 1] = pk2v(a2 * fma2(nmu2, f32x2{c4[2], c4[3]}, f32x2{acc[t][4 * gq + 2], acc[t][4 * gq + 3]}));
                }
#pragma unroll
                for (int e = 0; e < 2; ++e) {
                    const auto a = __builtin_amdgcn_permlane32_swap(o[4 * e], o[4 * e + 2], false, false), b = __builtin_amdgcn_permlane32_swap(o[4 * e + 1], o[4 * e + 3], false, false);
                    const bool split = (qo[t][e] & 0x600u) == 0x600u;
                    st4(rw, qo[t][e], u32x4{a[0], b[0], split ? 0x00003f80u : a[1], split ? 0u : b[1]});
                    if (split) st4(rw, qo[t][e] + (T < 14 ? 64 * 2048 : 64 * 4096), u32x4{a[1], b[1], 0x00003f80u, 0u});
                }
            }
        }
        TMARK(3);
        publish(lcnt(L, C_PUB1_A), 4, (gu64*)(ws + WS_F1) + cg * 16 + mt, 8, 224, tag);
    } else {
        f32x4 cz[4][4];
#pragma unroll
        for (int t = 0; t < 4; ++t)
#pragma unroll
            for (int gq = 0; gq < 4; ++gq) cz[t][gq] = *(const AS3 f32x4*)(pv + 32 * t + 8 * gq + 4 * h);
        const float A1 = -2.3022081985f * rs, A3 = (-2.3022081985f * 0.044715f) * rs * rs * rs, sd = var * rs;
        const f32x2 A1v = {A1, A1}, A3v = {A3, A3}, sdv = {sd, sd};
        SB();
        f32x2 gz[2][8], zs = {0.f, 0.f}, zq = {0.f, 0.f};
#pragma unroll
        for (int t = 0; t < 2; ++t) {
            gelu16(gz[t], acc[2 + t], cz[2 + t], nmu2, A1v, A3v, sdv);
            f32x2 a = gz[t][0] + gz[t][1], b = gz[t][0] * gz[t][0], c = gz[t][1] * gz[t][1];
#pragma unroll
            for (int j = 2; j < 8; j += 2) { zs += gz[t][j]; a += gz[t][j + 1]; b = fma2(gz[t][j], gz[t][j], b); c = fma2(gz[t][j + 1], gz[t][j + 1], c); }
            zs += a; zq += b + c;
            SB();
        }
        const unsigned tagA = (unsigned)tag, tagB = (unsigned)(tag >> 32);
        const float ps = halfsum(zs[0] + zs[1]), pq = halfsum(zq[0] + zq[1]);
        const unsigned go = WS_ST + tt * 14 * 512;
        if (h == 0) st4(rw, go + cg * 512 + r * 16, u32x4{__float_as_uint(ps), tagA, __float_as_uint(pq), tagB});
        SB();
        u32x4 z1p[4];
#pragma unroll
        for (int t = 0; t < 2; ++t) {
            f32x2 g1[8];
            gelu16(g1, acc[t], cz[t], nmu2, A1v, A3v, sdv);
            z1p[2 * t] = u32x4{pk2v(g1[0]), pk2v(g1[1]), pk2v(g1[2]), pk2v(g1[3])}; z1p[2 * t + 1] = u32x4{pk2v(g1[4]), pk2v(g1[5]), pk2v(g1[6]), pk2v(g1[7])};
            PIN(z1p[2 * t]); PIN(z1p[2 * t + 1]);
            SB();
        }
        TMARK(3);
        float t1, t2;
        for (int spin = 0; spin < SPIN_CAP; ++spin) {
            bool ok = true; t1 = 0.f; t2 = 0.f;
            u32x4 gr[7];
#pragma unroll
            for (int i = 0; i < 7; ++i) gr[i] = ld4(rw, lane * 16 + (i & 3) * 1024, go + (i >> 2) * 4096);
#pragma unroll
            for (int i = 0; i < 7; ++i) { ok &= gr[i][1] == tagA && gr[i][3] == tagB; t1 += __uint_as_float(gr[i][0]); t2 += __uint_as_float(gr[i][2]); }
            if (__all(ok)) break;
            asm volatile("" ::: "memory");
        }
        t1 = halfsum(t1); t2 = halfsum(t2);
        const float zm = t1 * (1.0f / 896.0f), zv = fmaxf(t2 * (1.0f / 896.0f) - zm * zm, 0.f);
        const float zr = __builtin_amdgcn_rsqf(zv + 1e-5f);
        const f32x2 zr2 = {zr, zr}, zb2 = {-zm * zr, -zm * zr};
        const lptr T = L + P2_AT + m * 4096;
#pragma unroll
        for (int t = 0; t < 2; ++t)
#pragma unroll
            for (int gq = 0; gq < 4; ++gq)
                *(AS3 u32x2*)(T + (2 * t + (gq >> 1)) * 1024 + r * 32 + (gq & 1) * 16 + 8 * h) = u32x2{pk2v(fma2(gz[t][2 * gq], zr2, zb2)), pk2v(fma2(gz[t][2 * gq + 1], zr2, zb2))};
        const int kq = lane >> 4;
#pragma unroll
        for (int i = 0; i < 4; ++i) {
            const bf16x8 v = cat8(trread(T + i * 1024 + (8 * kq + q) * 32 + 8 * p), trread(T + i * 1024 + (8 * kq + 4 + q) * 32 + 8 * p));
            const int F = 4 * cg + i, j = F / 7, f = F - 7 * j;
            st4(rw, WS_Z2N + ((j * 64 + tt) * 7 + f) * 1024 + lane * 16, __builtin_bit_cast(u32x4, v));
        }
        publish(lcnt(L, C_PUB1_B), 4, (gu64*)(ws + WS_F1) + 2048 + cg * 16 + mt, 8, 224, tag);
        TMARK(13);
#pragma unroll
        for (int i = 0; i < 4; ++i) st4(rw, WS_ZF + (tt * 56 + cg * 4 + i) * 1024 + lane * 16, z1p[i]);
#pragma unroll
        for (int ds = 0; ds < 3; ++ds)
#pragma unroll
            for (int i = 0; i < 8; ++i) gw.piece(ds, i);
        publish(lcnt(L, C_PUB1_Z), 4, (gu64*)(ws + WS_F1) + 4096 + cg * 16 + mt, 1, 0, tag);
    }
    TMARK(4);
}

DI void helper(const unsigned short* wo, const unsigned short* wout, const char* wsp, char* ws, lptr L, int sp, int wid, u64 tag) {
    const int lane = threadIdx.x & 63, gw = sp * 8 + wid, h = lane >> 5;
    const rsrc_t rw = mkrsrc(ws, WS_BYTES);
    unsigned e[5][8];
#pragma unroll
    for (int i = 0; i < 5; ++i) {
        const int f = min(gw + 256 * i, 14 * 88 - 1), nt = f / 88, ks = f - nt * 88, col = nt * 32 + (lane & 31);
        const unsigned short* src = ks < 32 ? wo + (size_t)((ks >> 1) * 28 + (ks & 1) * 16) * 448 : wout + (size_t)(ks - 32) * 16 * 448;
        const int hi = (ks < 32 && (ks & 1)) ? 0 : 4 * h;
#pragma unroll
        for (int j = 0; j < 8; ++j) e[i][j] = src[(8 * (j >> 2) + (j < 4 ? 4 * h : hi) + (j & 3)) * 448 + col];
    }
#pragma unroll
    for (int i = 0; i < 5; ++i) {
        const int f = min(gw + 256 * i, 14 * 88 - 1), ks = f % 88;
        const bool pad = ks < 32 && (ks & 1) && h;
        st4(rw, WS_BF + f * 1024 + lane * 16, u32x4{e[i][0] | (e[i][1] << 16), e[i][2] | (e[i][3] << 16), pad ? 0u : e[i][4] | (e[i][5] << 16), pad ? 0u : e[i][6] | (e[i][7] << 16)});
    }
    publish(lcnt(L, C_PUBH), 8, (gu64*)(ws + WS_FH) + sp, 8, 32, tag);
    for (int b = 0; b < 2; ++b) {
        u32x4 t[16];
#pragma unroll
        for (int i = 0; i < 16; ++i) t[i] = *(const u32x4*)(wsp + (size_t)((b * 16 + i) * 256 + gw) * 1024 + lane * 16);
#pragma unroll
        for (int i = 0; i < 16; ++i) asm volatile("" :: "v"(t[i]));
    }
}

DI void attn_store(char* ws, const f32x16& o, int head, int tt) {
    const int lane = threadIdx.x & 63;
    const float inv = 1.0f / hihalf(o[12]);
    const rsrc_t rw = mkrsrc(ws, WS_BYTES);
#pragma unroll
    for (int s = 0; s < 2; ++s)
        st4(rw, WS_AF + (tt * 88 + 2 * head + s) * 1024 + lane * 16,
            u32x4{pk2(o[8 * s] * inv, o[8 * s + 1] * inv), pk2(o[8 * s + 2] * inv, o[8 * s + 3] * inv), pk2(o[8 * s + 4] * inv, o[8 * s + 5] * inv), pk2(o[8 * s + 6] * inv, o[8 * s + 7] * inv)});
}
DI void attn_safe(char* ws, f32x16& o, int head, int qt) {
    const int lane = threadIdx.x & 63, h = lane >> 5;
    const rsrc_t rw = mkrsrc(ws, WS_BYTES);
    const unsigned lo = lane * 16, qp = WS_QF + (head * 64 + qt) * 2048;
    const bf16x8 q0 = ld8(rw, lo, qp); u32x4 q1u = ld4(rw, lo + 1024, qp);
    if (h) { q1u[2] = 0u; q1u[3] = 0u; }
    const bf16x8 q1 = __builtin_bit_cast(bf16x8, q1u);
    f32x16 zero;
#pragma unroll
    for (int i = 0; i < 16; ++i) { o[i] = 0.f; zero[i] = 0.f; }
    float m = -3.0e38f;
#pragma unroll 1
    for (int kt = 0; kt < 64; ++kt) {
        const unsigned kp = WS_KVF + (head * 64 + kt) * 4096;
        const bf16x8 k0 = ld8(rw, lo, kp), k1 = ld8(rw, lo + 1024, kp), v0 = ld8(rw, lo + 2048, kp), v1 = ld8(rw, lo + 3072, kp);
        f32x16 s = MFMA32(k0, q0, zero); s = MFMA32(k1, q1, s);
        float mx = s[0];
#pragma unroll
        for (int i = 1; i < 16; ++i) mx = fmaxf(mx, s[i]);
        mx = fmaxf(halfmax(mx), m);
        const float f = EX2(m - mx); m = mx;
        u32x4 p0, p1;
#pragma unroll
        for (int i = 0; i < 16; ++i) { o[i] *= f; s[i] = EX2(s[i] - m); }
#pragma unroll
        for (int i = 0; i < 4; ++i) { p0[i] = pk2(s[2 * i], s[2 * i + 1]); p1[i] = pk2(s[8 + 2 * i], s[9 + 2 * i]); }
        o = MFMA32(v0, __builtin_bit_cast(bf16x8, p0), o); o = MFMA32(v1, __builtin_bit_cast(bf16x8, p1), o);
    }
}

DI void attention(char* ws, lptr L, int head, int qb, int w, int rep, bool shared, u64 tag TARG) {
    const int lane = threadIdx.x & 63, h = lane >> 5;
    if (w == 0) {
        const int c = lane >> 4, cgk = (448 * (1 + (c >> 1)) + 28 * head + 27 * (c & 1)) / 96, cgq = (28 * head + 27 * (lane & 1)) / 96;
        gu64* f = (gu64*)(ws + WS_F1) + rep * 224;
        poll2(f + cgk * 16 + (lane & 15), f + cgq * 16 + qb, tag);
        if (lane == 0) __hip_atomic_store(lcnt(L, C_RDY_A), 1u, RLX_WG);
    } else wait_ready(lcnt(L, C_RDY_A));
    TMARK(5);
    const rsrc_t rw = mkrsrc(ws, WS_BYTES);
    unsigned kv = WS_KVF + head * 64 * 4096;
    const unsigned lo = lane * 16;
    bf16x8 q0[4]; u32x4 q1[4];
    bf16x8 r0 = ld8(rw, lo, kv), r1 = ld8(rw, lo + 1024, kv);
#pragma unroll
    for (int t = 0; t < 4; ++t) { const unsigned qp = WS_QF + (head * 64 + qb * 4 + ((w + t) & 3)) * 2048;
        q0[t] = ld8(rw, lo, qp); q1[t] = ld4(rw, lo + 1024, qp); }
    kv += w * 16 * 4096;
    bf16x8 ka0 = ld8(rw, lo, kv), ka1 = ld8(rw, lo + 1024, kv), va0 = ld8(rw, lo + 2048, kv), va1 = ld8(rw, lo + 3072, kv);
    bf16x8 kb0 = ld8(rw, lo, kv + 4096), kb1 = ld8(rw, lo + 1024, kv + 4096), vb0, vb1;
    f32x16 zero, o[4], sA, sB;
    u32x4 p0, p1 = {0u, 0u, 0u, 0u};
#pragma unroll
    for (int i = 0; i < 16; ++i) zero[i] = 0.f;
#pragma unroll
    for (int i = 0; i < 8; ++i) vb1[i] = 0;
#pragma unroll
    for (int t = 0; t < 4; ++t) {
        q1[t][2] = h ? 0u : q1[t][2]; q1[t][3] = h ? 0u : q1[t][3];
        o[t] = MFMA32(r0, q0[t], zero); o[t] = MFMA32(r1, __builtin_bit_cast(bf16x8, q1[t]), o[t]);
    }
#pragma unroll
    for (int t = 0; t < 4; ++t) {
        float mx = fmaxf(o[t][0], o[t][1]);
#pragma unroll
        for (int i = 2; i < 16; i += 2) mx = fmaxf(fmaxf(mx, o[t][i]), o[t][i + 1]);
        mx = halfmax(mx);
        q1[t][2] = h ? pk2(-(mx + 32.0f), 0.f) : q1[t][2];
#pragma unroll
        for (int i = 0; i < 16; ++i) o[t][i] = 0.f;
    }
#define Q1(t) __builtin_bit_cast(bf16x8, q1[t])
#define PB(x) __builtin_bit_cast(bf16x8, x)
#define ASTEP(A, B, O, V0, OP, VP, K0, K1, QN) do { \
    B = MFMA32(K0, q0[QN], zero); SB(); \
    A[0] = EX2(A[0]); A[1] = EX2(A[1]); A[2] = EX2(A[2]); A[3] = EX2(A[3]); PIN(A); SB(); \
    OP = MFMA32(VP, PB(p1), OP); SB(); \
    p0[0] = pk2(A[0], A[1]); p0[1] = pk2(A[2], A[3]); SB(); A[4] = EX2(A[4]); A[5] = EX2(A[5]); A[6] = EX2(A[6]); A[7] = EX2(A[7]); PIN(A); PIN(p0); SB(); \
    B = MFMA32(K1, Q1(QN), B); SB(); \
    p0[2] = pk2(A[4], A[5]); p0[3] = pk2(A[6], A[7]); SB(); A[8] = EX2(A[8]); A[9] = EX2(A[9]); A[10] = EX2(A[10]); A[11] = EX2(A[11]); PIN(A); PIN(p0); SB(); \
    O = MFMA32(V0, PB(p0), O); SB(); \
    A[12] = EX2(A[12]); A[13] = EX2(A[13]); A[14] = EX2(A[14]); A[15] = EX2(A[15]); SB(); \
    p1[0] = pk2(A[8], A[9]); p1[1] = pk2(A[10], A[11]); SB(); p1[2] = pk2(A[12], A[13]); SB(); p1[3] = pk2(A[14], A[15]); PIN(p1); SB(); } while (0)
#define LDK(K0, K1, off) do { K0 = ld8(rw, lo, kv + (off)); K1 = ld8(rw, lo + 1024, kv + (off)); SB(); } while (0)
#define LDV(V0, V1, off) do { V0 = ld8(rw, lo + 2048, kv + (off)); V1 = ld8(rw, lo + 3072, kv + (off)); SB(); } while (0)
#define APAIR(MORE) do { \
    ASTEP(sA, sB, o[0], va0, o[3], vb1, ka0, ka1, 1); LDV(vb0, vb1, 4096); \
    ASTEP(sB, sA, o[1], va0, o[0], va1, ka0, ka1, 2); \
    ASTEP(sA, sB, o[2], va0, o[1], va1, ka0, ka1, 3); if (MORE) LDK(ka0, ka1, 8192); \
    ASTEP(sB, sA, o[3], va0, o[2], va1, kb0, kb1, 0); \
    ASTEP(sA, sB, o[0], vb0, o[3], va1, kb0, kb1, 1); if (MORE) LDV(va0, va1, 8192); \
    ASTEP(sB, sA, o[1], vb0, o[0], vb1, kb0, kb1, 2); \
    ASTEP(sA, sB, o[2], vb0, o[1], vb1, kb0, kb1, 3); if (MORE) LDK(kb0, kb1, 12288); \
    ASTEP(sB, sA, o[3], vb0, o[2], vb1, ka0, ka1, 0); kv += 8192; } while (0)
    TMARK(6);
    sA = MFMA32(ka0, q0[0], zero); sA = MFMA32(ka1, Q1(0), sA);
    for (int it = 0; it < 7; ++it) APAIR(true);
    APAIR(false);
    o[3] = MFMA32(vb1, PB(p1), o[3]);
    TMARK(7);
    if (shared) { while (__hip_atomic_load(lcnt(L, C_PUB1_B), RLX_WG) < 4u) __builtin_amdgcn_s_sleep(1); asm volatile("" ::: "memory"); }
    const lptr P = L + P2_AT;
#pragma unroll
    for (int t = 1; t < 4; ++t)
#pragma unroll
        for (int g = 0; g < 4; ++g) *(AS3 f32x4*)(P + (w * 3 + t - 1) * 4096 + g * 1024 + lo) = f32x4{o[t][4 * g], o[t][4 * g + 1], o[t][4 * g + 2], o[t][4 * g + 3]};
    role_sync(lcnt(L, C_SYNC_A));
#pragma unroll
    for (int t = 1; t < 4; ++t)
#pragma unroll
        for (int g = 0; g < 4; ++g) { f32x4 v = *(const AS3 f32x4*)(P + (((w - t) & 3) * 3 + t - 1) * 4096 + g * 1024 + lo);
#pragma unroll
            for (int c = 0; c < 4; ++c) o[0][4 * g + c] += v[c]; }
    float chk = 0.f;
#pragma unroll
    for (int i = 0; i < 16; ++i) chk = __builtin_fmaf(o[0][i], 0.f, chk);
    const float l = hihalf(o[0][12]);
    const bool bad = !(chk == 0.f) || !(l > 1.0e-30f) || !(l < 1.0e30f);
    if (__builtin_expect(__any(bad), 0)) attn_safe(ws, o[0], head, qb * 4 + w);
    attn_store(ws, o[0], head, qb * 4 + w);
    TMARK(8);
    publish(lcnt(L, C_PUB2_A), 4, (gu64*)(ws + WS_F2) + qb * 128 + head, 2, 64, tag);
    TMARK(9);
}

constexpr int g_issued_before(int s, int f) {
    int n = 21;
    for (int t = 0; t <= s; ++t) {
        for (int g = 0; g < 7; ++g) {
            if (t == s && g == f) return n;
            if (t + 3 < 16) ++n;
            if ((t & 1) && (t >> 1) + 3 < 8) n += g == 6 ? 2 : 1;
        }
    }
    return n;
}
constexpr int g_pos_z(int s, int f) { return s < 3 ? 7 * s + f + 1 : g_issued_before(s - 3, f) + 1; }
constexpr int g_pos_w(int ds) { return g_issued_before(2 * (ds - 3) + 2, 0); }
template <int N> DI void vm_wait() { asm volatile("s_waitcnt vmcnt(%0)" :: "i"(N) : "memory"); }
template <int N> DI void vm_wait_for(bf16x8& v) { asm volatile("s_waitcnt vmcnt(%1)" : "+v"(v) : "i"(N)); }
template <int OFF> DI void abld(bf16x8& d, unsigned voff, const u32x4& rs, unsigned soff) {
    asm volatile("buffer_load_dwordx4 %0, %1, %2, %3 offen offset:%4 sc1" : "=v"(d) : "v"(voff), "s"(rs), "s"(soff), "i"(OFF));
}
template <int OFF> DI void abld_rw(bf16x8& d, unsigned voff, const u32x4& rs, unsigned soff) {
    asm volatile("buffer_load_dwordx4 %0, %1, %2, %3 offen offset:%4 sc1" : "+v"(d) : "v"(voff), "s"(rs), "s"(soff), "i"(OFF));
}
DI u32x4 rawrsrc(const void* p, unsigned bytes) { const u64 a = (u64)p; return u32x4{(unsigned)a, (unsigned)(a >> 32) & 0xffffu, bytes, 0x00020000u}; }

template <int S> struct GStep {
    template <class C> static DI void run(C& c) {
        constexpr int ks = S & 1;
        if constexpr (S + 1 < 16) {
            constexpr int nds = (S + 1) >> 1;
            if constexpr (ks == 1 && nds >= 3) vm_wait<g_issued_before(S, 0) - g_pos_w(nds)>();
#pragma unroll
            for (int t = 0; t < 4; ++t) c.wf[(S + 1) & 1][t] = *(const AS3 bf16x8*)(c.W + (nds % 3) * 8192 + t * 2048 + (ks ? c.rd0 : c.rd1));
        }
        SB();
        group<0>(c); group<1>(c); group<2>(c); group<3>(c); group<4>(c); group<5>(c); group<6>(c);
        if constexpr (S + 1 < 16 && S != 12) GStep<S + 1>::run(c);
    }
    template <int F, class C> static DI void group(C& c) {
        constexpr int ds = S >> 1, ks = S & 1;
        vm_wait_for<g_issued_before(S, F) - g_pos_z(S, F)>(c.z[S % 3][F]);
#pragma unroll
        for (int t = 0; t < 4; ++t) c.acc[F][t] = MFMA16(c.z[S % 3][F], c.wf[S & 1][t], c.acc[F][t]);
        SB();
        if constexpr (S + 3 < 16) { constexpr int u = (S + 3) * 7 + F; abld_rw<(u & 3) * 1024>(c.z[S % 3][F], c.lo, c.rw, c.zsrc + (u >> 2) * 4096); }
        if constexpr (ks == 1 && ds + 3 < 8) { c.gw.piece(ds + 3, F); if constexpr (F == 6) c.gw.piece(ds + 3, 7); }
        if constexpr (S == 15 && F > 0) {
#pragma unroll
            for (int t = 1; t < 4; ++t) *(AS3 f32x4*)(c.W + ((t - 1) * 7 + F - 1) * 1024 + c.lo) = c.acc[F > 0 ? F - 1 : 0][t];
        }
        SB();
    }
};
struct GCtx {
    lptr W; unsigned lo, zsrc, zo, bo; int rd0, rd1; GW gw; u32x4 rw, rb;
    f32x4 acc[7][4]; bf16x8 z[3][7], wf[2][4]; u32x2 z1[7]; unsigned braw;
    DI void epi_loads() {
        asm volatile("buffer_load_dwordx2 %0, %4, %5, 0 offen sc1\n\tbuffer_load_dwordx2 %1, %4, %5, 0 offen offset:1024 sc1\n\tbuffer_load_dwordx2 %2, %4, %5, 0 offen offset:2048 sc1\n\tbuffer_load_dwordx2 %3, %4, %5, 0 offen offset:3072 sc1"
                     : "=&v"(z1[0]), "=&v"(z1[1]), "=&v"(z1[2]), "=&v"(z1[3]) : "v"(zo), "s"(rw));
        asm volatile("buffer_load_dwordx2 %0, %4, %5, %6 offen sc1\n\tbuffer_load_dwordx2 %1, %4, %5, %6 offen offset:1024 sc1\n\tbuffer_load_dwordx2 %2, %4, %5, %6 offen offset:2048 sc1\n\tbuffer_load_ushort %3, %7, %8, 0 offen"
                     : "=&v"(z1[4]), "=&v"(z1[5]), "=&v"(z1[6]), "=&v"(braw) : "v"(zo), "s"(rw), "s"(4096u), "v"(bo), "s"(rb));
    }
};

DI void spatial(const GW& gw, const unsigned short* bsp, char* ws, lptr L, int mi, int j, int w, int rep, u64 tag TARG) {
    const int lane = threadIdx.x & 63, r = lane & 15, kq = lane >> 4;
    if (w == 0) {
        gu64* f = (gu64*)(ws + WS_F1) + 2048 + rep * 224 + min((7 * j) / 4 + kq, (7 * j + 6) / 4) * 16 + r;
        poll2(f, f, tag);
        if (lane == 0) __hip_atomic_store(lcnt(L, C_RDY_B), 1u, RLX_WG);
    } else wait_ready(lcnt(L, C_RDY_B));
    TMARK(5);
    GCtx c;
    c.W = L + w * P2_GP; c.gw = gw; c.lo = lane * 16;
    c.rw = rawrsrc(ws, WS_BYTES); c.rb = rawrsrc(bsp, 4096);
    c.zsrc = WS_Z2N + (j * 64 + w * 16) * 7 * 1024;
    vm_wait<0>();
#define ZP1(s, f) abld<(((s) * 7 + (f)) & 3) * 1024>(c.z[s][f], c.lo, c.rw, c.zsrc + (((s) * 7 + (f)) >> 2) * 4096)
#define ZPRO(s) ZP1(s, 0); ZP1(s, 1); ZP1(s, 2); ZP1(s, 3); ZP1(s, 4); ZP1(s, 5); ZP1(s, 6)
    ZPRO(0); ZPRO(1); ZPRO(2);
    c.rd0 = r * 128 + ((kq ^ (r >> 1)) << 4); c.rd1 = c.rd0 ^ 64;
    const int tok = mi * 64 + w * 16 + r, tt = mi * 2 + (w >> 1);
    c.zo = WS_ZF + (tt * 56 + 7 * j) * 1024 + ((kq & 1) * 32 + (tok & 31)) * 16 + (kq >> 1) * 8; c.bo = tok * 2;
#pragma unroll
    for (int a = 0; a < 7; ++a)
#pragma unroll
        for (int b = 0; b < 4; ++b) c.acc[a][b] = f32x4{0.f, 0.f, 0.f, 0.f};
#pragma unroll
    for (int t = 0; t < 4; ++t) c.wf[0][t] = *(const AS3 bf16x8*)(c.W + t * 2048 + c.rd0);
    SB();
    TMARK(6);
    GStep<0>::run(c);
    {
        gu64* f = (gu64*)(ws + WS_F1) + 4096 + min((7 * j) / 4 + kq, (7 * j + 6) / 4) * 16 + (mi >> 1);
        poll2(f, f, tag);
        c.epi_loads();
        SB();
    }
    GStep<13>::run(c);
    TMARK(7);
#pragma unroll
    for (int t = 1; t < 4; ++t) *(AS3 f32x4*)(c.W + ((t - 1) * 7 + 6) * 1024 + c.lo) = c.acc[6][t];
    role_sync(lcnt(L, C_SYNC_B));
    asm volatile("s_waitcnt vmcnt(0)" : "+v"(c.z1[0]), "+v"(c.z1[1]), "+v"(c.z1[2]), "+v"(c.z1[3]), "+v"(c.z1[4]), "+v"(c.z1[5]), "+v"(c.z1[6]), "+v"(c.braw));
    const float bias = bflo(c.braw);
    u32x2 o[8];
#pragma unroll
    for (int f = 0; f < 7; ++f) {
        f32x4 g = c.acc[f][0];
#pragma unroll
        for (int t = 1; t < 4; ++t) g += *(const AS3 f32x4*)(L + ((w - t) & 3) * P2_GP + ((t - 1) * 7 + f) * 1024 + c.lo);
        o[f] = u32x2{pk2((g[0] + bias) * bflo(c.z1[f][0]), (g[1] + bias) * bfhi(c.z1[f][0])), pk2((g[2] + bias) * bflo(c.z1[f][1]), (g[3] + bias) * bfhi(c.z1[f][1]))};
    }
    o[7] = o[6];
    const rsrc_t rws = mkrsrc(ws, WS_BYTES);
    const unsigned ho = WS_AF + (tt * 88 + 32 + 7 * j) * 1024 + ((kq & 1) * 32 + (tok & 31)) * 16 + (kq >> 1) * 1024;
#pragma unroll
    for (int f = 0; f < 8; f += 2) {
        const auto a = __builtin_amdgcn_permlane32_swap(o[f][0], o[f + 1][0], false, false), b = __builtin_amdgcn_permlane32_swap(o[f][1], o[f + 1][1], false, false);
        if (f < 6 || lane < 32) st4(rws, ho + f * 1024, u32x4{a[0], b[0], a[1], b[1]});
    }
    TMARK(8);
    publish(lcnt(L, C_PUB2_B), 4, (gu64*)(ws + WS_F2) + mi * 64 + 16 + j, 1, 0, tag);
    TMARK(9);
}

constexpr int P3_ROW = 272, P3_TILE = 64 * P3_ROW;
DI void outproj(const char* x, char* out, char* ws, lptr L, int mi, int nj, int w, int rep, u64 tag TARG) {
    const int tid = threadIdx.x, lane = tid & 63, r = lane & 31, h = lane >> 5;
    const rsrc_t rw = mkrsrc(ws, WS_BYTES);
    const unsigned lo = lane * 16, bo = WS_BF + (nj * 2 * 88 + w * 11) * 1024, ao = WS_AF + (mi * 2 * 88 + w * 11) * 1024;
    bf16x8 a[11][2], b[11][2];
    { gu64* f = (gu64*)(ws + WS_FH) + rep * 32 + r; poll2(f, f, tag); }
    while (__hip_atomic_load(lcnt(L, C_PUB2_A), RLX_WG) + __hip_atomic_load(lcnt(L, C_PUB2_B), RLX_WG) < 8u) __builtin_amdgcn_s_sleep(2);
    asm volatile("" ::: "memory");
#pragma unroll
    for (int s = 0; s < 11; ++s) { b[s][0] = ld8(rw, lo + (s & 3) * 1024, bo + (s >> 2) * 4096); b[s][1] = ld8(rw, lo + (s & 3) * 1024, bo + 88 * 1024 + (s >> 2) * 4096); }
    const u32x4 xr = *(const u32x4*)(x + (unsigned)((mi * 64 + (tid >> 3)) * 896 + nj * 128 + (tid & 7) * 16));
    SB();
    if (w == 0) { gu64* f = (gu64*)(ws + WS_F2) + mi * 64 + (lane < 24 ? lane : 0); poll2(f, f, tag); }
    asm volatile("s_waitcnt lgkmcnt(0)\n\ts_barrier" ::: "memory");
    TMARK(10);
#pragma unroll
    for (int s = 0; s < 11; ++s) { a[s][0] = ld8(rw, lo + (s & 3) * 1024, ao + (s >> 2) * 4096); a[s][1] = ld8(rw, lo + (s & 3) * 1024, ao + 88 * 1024 + (s >> 2) * 4096); }
    SB();
    f32x16 acc[2][2];
#pragma unroll
    for (int i = 0; i < 16; ++i) { acc[0][0][i] = 0.f; acc[0][1][i] = 0.f; acc[1][0][i] = 0.f; acc[1][1][i] = 0.f; }
#pragma unroll
    for (int s = 0; s < 11; ++s) {
        acc[0][0] = MFMA32(b[s][0], a[s][0], acc[0][0]); acc[0][1] = MFMA32(b[s][1], a[s][0], acc[0][1]);
        acc[1][0] = MFMA32(b[s][0], a[s][1], acc[1][0]); acc[1][1] = MFMA32(b[s][1], a[s][1], acc[1][1]);
    }
    TMARK(11);
#pragma unroll
    for (int tm = 0; tm < 2; ++tm)
#pragma unroll
        for (int tn = 0; tn < 2; ++tn)
#pragma unroll
            for (int g = 0; g < 4; ++g)
                *(AS3 f32x4*)(L + w * P3_TILE + (tm * 32 + r) * P3_ROW + (tn * 32 + 8 * g + 4 * h) * 4) =
                    f32x4{acc[tm][tn][4 * g], acc[tm][tn][4 * g + 1], acc[tm][tn][4 * g + 2], acc[tm][tn][4 * g + 3]};
    __syncthreads();
    const int row = tid >> 3, c8 = tid & 7;
    f32x4 s0 = {0.f, 0.f, 0.f, 0.f}, s1 = s0;
#pragma unroll
    for (int p = 0; p < 8; ++p) { s0 += *(const AS3 f32x4*)(L + p * P3_TILE + row * P3_ROW + c8 * 32); s1 += *(const AS3 f32x4*)(L + p * P3_TILE + row * P3_ROW + c8 * 32 + 16); }
    const u32x4 o = {pk2(s0[0] + bflo(xr[0]), s0[1] + bfhi(xr[0])), pk2(s0[2] + bflo(xr[1]), s0[3] + bfhi(xr[1])),
                     pk2(s1[0] + bflo(xr[2]), s1[1] + bfhi(xr[2])), pk2(s1[2] + bflo(xr[3]), s1[3] + bfhi(xr[3]))};
    *(u32x4*)(out + (unsigned)((mi * 64 + row) * 896 + nj * 128 + c8 * 16)) = o;
    TMARK(12);
}

__global__ __launch_bounds__(512, 2) void fused_kernel(const char* x, const char* wqkv, const char* win, const unsigned short* wo, const unsigned short* wout,
                                                       const char* wsp, const unsigned short* bsp, char* out, char* ws) {
    extern __shared__ __attribute__((aligned(16))) char smem[];
    TDECL; TMARK(0);
    const lptr L = (lptr)smem;
    const int bid = blockIdx.x, xcd = bid & 7, idx = bid >> 3, lane = threadIdx.x & 63;
    const int wid = __builtin_amdgcn_readfirstlane(threadIdx.x >> 6);
    const unsigned lds0 = (unsigned)(uintptr_t)L;
    const int i7 = (idx * 37) >> 8, m7 = idx - 7 * i7;
    const int mt = (xcd >> 1) * 4 + i7, cg = (xcd & 1) * 7 + m7;
    P1Src src;
    if (idx < 28) {
        const unsigned rowo = (mt * 128 + 16 * wid + (lane >> 3)) * 896;
        src.voA0 = rowo + (((lane & 7) ^ (lane >> 4)) << 4);
        src.voA1 = rowo + 8 * 896 + (((lane & 7) ^ (4 + (lane >> 4))) << 4);
        const int krow = 16 * (wid & 3) + (lane >> 2), zh = wid >> 2;
        src.voZ = krow * 3584 + zh * 1792 + cg * 128 + (lane & 3) * 16;
        const int seg = wid >= 4 ? ((cg & 1) ? 0 : 2) : (cg & 1);
        src.voQ = krow * 2688 + cg * 192 + seg * 64 + (lane & 3) * 16;
        src.dA = lds0 + wid * 2048; src.dZ = lds0 + P1_B + zh * 8192 + (wid & 3) * 1024; src.dQ = lds0 + P1_B + (4 + seg) * 4096 + (wid & 3) * 1024;
#pragma unroll
        for (int j = 0; j < 5; ++j) p1_piece(src, x, wqkv, win, 0, j);
        if (wid < 4) p1_piece(src, x, wqkv, win, 0, 5);
        SB();
    }
    unsigned epoch;
    asm volatile("s_load_dword %0, %1, 0x0\n\ts_waitcnt lgkmcnt(0)" : "=s"(epoch) : "s"(ws + WS_EP) : "memory");
    const u64 tag = (u64)(epoch * 0x9E3779B1u + 0x7F4A7C15u) | ((u64)(epoch * 0x85EBCA6Bu + 0x1B873593u) << 32);
    if (threadIdx.x < 16) *lcnt(L, threadIdx.x) = 0u;
    const int mi = (xcd >> 1) * 8 + (idx >> 2), j = (xcd & 1) * 4 + (idx & 3);
    GW gw;
    { const unsigned wvo = (mi * 64 + (lane >> 3)) * 4096 + (wid & 3) * 1024;
      gw.wv0 = wvo + (((lane & 7) ^ (lane >> 4)) << 4); gw.wv1 = wvo + (((lane & 7) ^ (4 + (lane >> 4))) << 4);
      gw.wdst = lds0 + (wid & 3) * P2_GP; gw.w = wid & 3; gw.wsp = wsp; }
    if (idx < 28) {
        if (wid >= 4) phase1<1, 0>(src, gw, x, wqkv, win, ws, L, mt, cg, wid, tag TPASS);
        else if (cg < 9) phase1<0, 0>(src, gw, x, wqkv, win, ws, L, mt, cg, wid, tag TPASS);
        else if (cg == 9) phase1<0, 2>(src, gw, x, wqkv, win, ws, L, mt, cg, wid, tag TPASS);
        else phase1<0, 3>(src, gw, x, wqkv, win, ws, L, mt, cg, wid, tag TPASS);
    } else {
        __syncthreads();
        helper(wo, wout, wsp, ws, L, xcd * 4 + idx - 28, wid, tag);
        if (wid >= 4) {
#pragma unroll
            for (int ds = 0; ds < 3; ++ds)
#pragma unroll
                for (int i = 0; i < 8; ++i) gw.piece(ds, i);
        }
    }
    if (wid < 4) attention(ws, L, 2 * xcd + (idx >> 4), idx & 15, wid, xcd, idx < 28, tag TPASS);
    else spatial(gw, bsp, ws, L, mi, j, wid - 4, xcd, tag TPASS);
    if (idx < 28) outproj(x, out, ws, L, xcd * 4 + i7, m7, wid, xcd, tag TPASS);
    else if (bid == 255 && wid == 0) {
        gu64* f = (gu64*)(ws + WS_F2) + (lane >> 4) * 128 + (lane & 15);
        poll2(f, f + 512, tag); poll2(f + 1024, f + 1536, tag);
        if (lane == 0) __hip_atomic_store((gu32*)(ws + WS_EP), epoch + 1u, RLX_AGENT);
    }
    TFLUSH();
}

extern "C" void kernel_launch(void* const* d_in, const int* in_sizes, int n_in, void* d_out, int out_size, void* d_ws, size_t ws_size, hipStream_t stream) {
    (void)hipFuncSetAttribute((const void*)fused_kernel, hipFuncAttributeMaxDynamicSharedMemorySize, LDS_BYTES);
    fused_kernel<<<256, 512, LDS_BYTES, stream>>>((const char*)d_in[0], (const char*)d_in[1], (const char*)d_in[4], (const unsigned short*)d_in[2], (const unsigned short*)d_in[7],
                                                  (const char*)d_in[5], (const unsigned short*)d_in[6], (char*)d_out, (char*)d_ws);
}
```

```cpp
#include <hip/hip_runtime.h>
#include <hip/hip_bf16.h>
#include <stdint.h>
#include <type_traits>

typedef short bf16x8 __attribute__((ext_vector_type(8)));
typedef short s16x4 __attribute__((ext_vector_type(4)));
typedef float f32x16 __attribute__((ext_vector_type(16)));
typedef float f32x4 __attribute__((ext_vector_type(4)));
typedef float f32x2 __attribute__((ext_vector_type(2)));
typedef unsigned u32x4 __attribute__((ext_vector_type(4)));
typedef unsigned u32x2 __attribute__((ext_vector_type(2)));
typedef __bf16 bf2 __attribute__((ext_vector_type(2)));
typedef unsigned long long u64;
#define AS3 __attribute__((address_space(3)))
typedef AS3 char* lptr;
typedef __attribute__((address_space(1))) unsigned gu32;
typedef __attribute__((address_space(1))) u64 gu64;
typedef __amdgpu_buffer_rsrc_t rsrc_t;
#define DI __device__ __forceinline__
#define SB() __builtin_amdgcn_sched_barrier(0)
#define PIN(x) asm volatile("" : "+v"(x))
#define MFMA32(a, b, c) __builtin_amdgcn_mfma_f32_32x32x16_bf16((a), (b), (c), 0, 0, 0)
#define MFMA16(a, b, c) __builtin_amdgcn_mfma_f32_16x16x32_bf16((a), (b), (c), 0, 0, 0)
#define EX2(x) __builtin_amdgcn_exp2f(x)
#define RLX_AGENT __ATOMIC_RELAXED, __HIP_MEMORY_SCOPE_AGENT
#define RLX_WG __ATOMIC_RELAXED, __HIP_MEMORY_SCOPE_WORKGROUP

constexpr unsigned WS_QF = 0;
constexpr unsigned WS_KVF = 2u << 20;
constexpr unsigned WS_ZF = 6u << 20;
constexpr unsigned WS_Z2N = 10u << 20;
constexpr unsigned WS_AF = 14u << 20;
constexpr unsigned WS_BF = 20u << 20;
constexpr unsigned WS_ST = WS_BF + (3u << 19);
constexpr unsigned WS_EP = 22u << 20;
constexpr unsigned WS_F1 = WS_EP + 4096;
constexpr unsigned WS_F2 = WS_F1 + 49152;
constexpr unsigned WS_FH = WS_F2 + 16384;
constexpr unsigned WS_BYTES = 23u << 20;
constexpr size_t WS_DBG = 28u << 20;

#define TDECL do {} while (0)
#define TARG
#define TPASS
#define TMARK(i) do {} while (0)
#define TFLUSH() do {} while (0)

DI int otid() { int t = threadIdx.x; asm volatile("" : "+v"(t)); return t; }
DI unsigned pk2(float a, float b) { f32x2 v = {a, b}; return __builtin_bit_cast(unsigned, __builtin_convertvector(v, bf2)); }
DI unsigned pk2v(f32x2 v) { return __builtin_bit_cast(unsigned, __builtin_convertvector(v, bf2)); }
DI float bflo(unsigned u) { return __uint_as_float(u << 16); }
DI float bfhi(unsigned u) { return __uint_as_float(u & 0xffff0000u); }
DI f32x2 fma2(f32x2 a, f32x2 b, f32x2 c) { return __builtin_elementwise_fma(a, b, c); }
DI float halfsum(float v) { auto r = __builtin_amdgcn_permlane32_swap(__float_as_uint(v), __float_as_uint(v), false, false); return __uint_as_float(r[0]) + __uint_as_float(r[1]); }
DI float halfmax(float v) { auto r = __builtin_amdgcn_permlane32_swap(__float_as_uint(v), __float_as_uint(v), false, false); return fmaxf(__uint_as_float(r[0]), __uint_as_float(r[1])); }
DI float hihalf(float v) { auto r = __builtin_amdgcn_permlane32_swap(__float_as_uint(v), __float_as_uint(v), false, false); return __uint_as_float(r[1]); }
DI s16x4 trread(lptr p) { return __builtin_bit_cast(s16x4, __builtin_amdgcn_ds_read_tr16_b64_v4i16((AS3 s16x4*)p)); }
DI bf16x8 cat8(s16x4 a, s16x4 b) { return __builtin_shufflevector(a, b, 0, 1, 2, 3, 4, 5, 6, 7); }
template <int GO, int LO> DI void dma16(unsigned voff, const u32x4& rs, unsigned soff, unsigned lds) {
    unsigned tmp;
    asm volatile("s_add_u32 m0, %4, %6\n\ts_add_u32 %0, %3, %5\n\tbuffer_load_dwordx4 %1, %2, %0 offen lds"
                 : "=&s"(tmp) : "v"(voff), "s"(rs), "s"(soff), "s"(lds), "i"(GO), "i"(LO) : "memory", "scc");
}
DI u32x4 rawrsrc(const void* p, unsigned bytes) { const u64 a = (u64)p; return u32x4{(unsigned)a, (unsigned)(a >> 32) & 0xffffu, bytes, 0x00020000u}; }
template <int N, class F> DI void unroll(F f) { if constexpr (N > 0) { unroll<N - 1>(f); f(std::integral_constant<int, N - 1>{}); } }
DI rsrc_t mkrsrc(const void* p, unsigned bytes) { return __builtin_amdgcn_make_buffer_rsrc((void*)p, 0, bytes, 0x00020000); }
DI u32x4 ld4(rsrc_t r, unsigned voff, unsigned soff) { return __builtin_amdgcn_raw_buffer_load_b128(r, voff, soff, 16); }
DI bf16x8 ld8(rsrc_t r, unsigned voff, unsigned soff) { return __builtin_bit_cast(bf16x8, __builtin_amdgcn_raw_buffer_load_b128(r, voff, soff, 16)); }
DI void st4(rsrc_t r, unsigned voff, u32x4 v) { __builtin_amdgcn_raw_buffer_store_b128(v, r, voff, 0, 16); }
DI void st2(rsrc_t r, unsigned voff, u32x2 v) { __builtin_amdgcn_raw_buffer_store_b64(v, r, voff, 0, 16); }

constexpr int P1_SLOT = 40960, P1_WZ = 16384, P1_WQ = 24576, P1_WZ1 = P1_SLOT + 8192;
constexpr int P2_GP = 24576, P2_AT = 4 * P2_GP;
constexpr int P1_CP = P2_AT + 4 * 12288, P1_SP = P1_CP + 4096, P1_PRIV = P1_SP + 4096;
constexpr int L_CNT = P1_PRIV + 8 * 512, LDS_BYTES = L_CNT + 128;
constexpr int L_TRZ = 3 * P1_SLOT, L_TRV = L_TRZ + 16384;
static_assert(LDS_BYTES <= 163840 && L_TRV + 8192 <= P1_CP, "LDS map");
enum { C_SYNC_A, C_SYNC_B, C_PUB2_A, C_PUB2_B, C_PUB1_A, C_PUB1_B, C_RDY_A, C_RDY_B, C_PUBH, C_PUB1_Z, C_CS_B, C_Z1 };
DI AS3 unsigned* lcnt(lptr L, int i) { return (AS3 unsigned*)(L + L_CNT) + i; }
DI void larrive(AS3 unsigned* c) { if ((otid() & 63) == 0) __hip_atomic_fetch_add(c, 1u, RLX_WG); }
DI void lwait4(AS3 unsigned* c, unsigned n = 4u) { while (__hip_atomic_load(c, RLX_WG) < n) __builtin_amdgcn_s_sleep(0); asm volatile("" ::: "memory"); }
#define P3SL 4
#define GRS 14
#define PUBT 0
#define NO_PF
#define GS0 12
#define GS1 6
#define GPRIO 0
constexpr int SPIN_CAP = 100000;

DI void role_sync(AS3 unsigned* cnt) {
    asm volatile("s_waitcnt lgkmcnt(0)" ::: "memory");
    if ((otid() & 63) == 0) __hip_atomic_fetch_add(cnt, 1u, RLX_WG);
    while (__hip_atomic_load(cnt, RLX_WG) < 4u) __builtin_amdgcn_s_sleep(1);
    asm volatile("" ::: "memory");
}
DI void publish(AS3 unsigned* cnt, unsigned n, gu64* flag, int copies, int stride, u64 tag) {
    asm volatile("s_waitcnt vmcnt(0)" ::: "memory");
    const int lane = otid() & 63;
    unsigned old = 0;
    if (lane == 0) old = __hip_atomic_fetch_add(cnt, 1u, RLX_WG);
    if (__builtin_amdgcn_readfirstlane(old) == n - 1 && lane < copies) __hip_atomic_store(flag + lane * stride, tag, RLX_AGENT);
}
DI void wait_ready(AS3 unsigned* w) { while (__hip_atomic_load(w, RLX_WG) == 0u) __builtin_amdgcn_s_sleep(1); asm volatile("" ::: "memory"); }
DI void poll2(gu64* a, gu64* b, u64 tag) {
    for (int spin = 0; spin < SPIN_CAP; ++spin) {
        const u64 va = __hip_atomic_load(a, RLX_AGENT), vb = __hip_atomic_load(b, RLX_AGENT);
        if (__all(va == tag && vb == tag)) break;
        __builtin_amdgcn_s_sleep(2);
    }
    asm volatile("" ::: "memory");
}

template <int N> DI void vm_wait() { asm volatile("s_waitcnt vmcnt(%0)" :: "i"(N) : "memory"); }
template <int N> DI void waitbar() { asm volatile("s_waitcnt vmcnt(%0) lgkmcnt(0)\n\ts_barrier" :: "i"(N) : "memory"); }
DI void lbar() { asm volatile("s_waitcnt lgkmcnt(0)\n\ts_barrier" ::: "memory"); }
struct P1Src { unsigned voA0, voA1, voZ, voQ, ldsx, ldsw; u32x4 rx, rz, rq; };
template <int KT, int J> DI void p1_piece(const P1Src& s) {
    constexpr int so = (KT % 3) * P1_SLOT;
    if constexpr (KT < 7) {
        if (J == 0) dma16<KT * 128, so>(s.voA0, s.rx, 0, s.ldsx);
        if (J == 1) dma16<KT * 128, so + 1024>(s.voA1, s.rx, 0, s.ldsx);
        if (J == 2) dma16<KT * 64 * 3584 + 1792, so + P1_WZ>(s.voZ, s.rz, 0, s.ldsw);
        if (J == 3) dma16<KT * 64 * 2688, so + P1_WQ>(s.voQ, s.rq, 0, s.ldsw);
        if (J == 4) dma16<KT * 64 * 2688 + 128, so + P1_WQ + 8192>(s.voQ, s.rq, 0, s.ldsw);
    } else if constexpr ((KT - 7) * 4 + J < 7 && J < 4) {
        constexpr int k = (KT - 7) * 4 + J;
        dma16<k * 64 * 3584, P1_WZ1 + k * 8192>(s.voZ, s.rz, 0, s.ldsw);
    }
}
template <int KT> DI void p1_stage(const P1Src& s) { p1_piece<KT, 0>(s); p1_piece<KT, 1>(s); p1_piece<KT, 2>(s); p1_piece<KT, 3>(s); p1_piece<KT, 4>(s); }
struct GW { unsigned wv0, wv1, wdst, rowg[4]; u32x4 rs;
    template <int DS, int I> DI void piece() const { dma16<(I & 1) * 8 * 4096 + DS * 128, (DS % 3) * 8192 + I * 1024>((I & 1) ? wv1 : wv0, rs, rowg[I >> 1], wdst); }
    template <int DS> DI void tile() const { piece<DS, 0>(); piece<DS, 1>(); piece<DS, 2>(); piece<DS, 3>(); piece<DS, 4>(); piece<DS, 5>(); piece<DS, 6>(); piece<DS, 7>(); } };

DI GW make_gw(const char* wsp, lptr L, int mi, int w) {
    GW g; const int lane = otid() & 63;
    const unsigned wvo = (mi * 64 + (lane >> 3)) * 4096 + w * 1024;
    g.wv0 = wvo + (((lane & 7) ^ (lane >> 4)) << 4); g.wv1 = wvo + (((lane & 7) ^ (4 + (lane >> 4))) << 4);
    g.wdst = (unsigned)(uintptr_t)L + w * P2_GP; g.rs = rawrsrc(wsp, 2048 * 4096);
#pragma unroll
    for (int k = 0; k < 4; ++k) g.rowg[k] = ((k + w) & 3) * 16 * 4096;
    return g;
}
DI void gelu16(f32x2 (&g)[8], const f32x16& acc, const f32x4 (&cs)[4], f32x2 nmu2, f32x2 A1v, f32x2 A3v, f32x2 sdv) {
    f32x2 u[8], e[8];
#pragma unroll
    for (int j = 0; j < 8; ++j) u[j] = fma2(nmu2, f32x2{cs[j >> 1][2 * (j & 1)], cs[j >> 1][2 * (j & 1) + 1]}, f32x2{acc[2 * j], acc[2 * j + 1]});
    SB();
#pragma unroll
    for (int j = 0; j < 8; ++j) e[j] = u[j] * u[j];
    SB();
#pragma unroll
    for (int j = 0; j < 8; ++j) e[j] = fma2(e[j], A3v, A1v);
    SB();
#pragma unroll
    for (int j = 0; j < 8; ++j) e[j] = e[j] * u[j];
    SB();
#pragma unroll
    for (int j = 0; j < 8; ++j) e[j] = f32x2{EX2(e[j][0]), EX2(e[j][1])};
    SB();
#pragma unroll
    for (int j = 0; j < 8; ++j) e[j] = fma2(e[j], sdv, sdv);
    SB();
#pragma unroll
    for (int j = 0; j < 8; ++j) e[j] = f32x2{__builtin_amdgcn_rcpf(e[j][0]), __builtin_amdgcn_rcpf(e[j][1])};
    SB();
#pragma unroll
    for (int j = 0; j < 8; ++j) g[j] = e[j] * u[j];
    SB();
}

struct P1Lane { int lane, r, h, m, q, p, offA[4], offW[4][3]; bf16x8 sel; AS3 float* cp; AS3 float* pv; };
DI void p1_lane(P1Lane& c, lptr L, int wid, int t0) {
    c.lane = otid() & 63; c.r = c.lane & 31; c.h = c.lane >> 5; c.m = wid & 3; c.q = (c.lane & 15) >> 2; c.p = c.lane & 3;
#pragma unroll
    for (int i = 0; i < 4; ++i) {
        const int ks = (i + wid + (wid >> 2) * 2) & 3;
        c.offA[i] = (c.m * 32 + c.r) * 128 + (((2 * ks + c.h) ^ ((c.r >> 1) & 7)) << 4);
#pragma unroll
        for (int t = 0; t < 3; ++t) c.offW[i][t] = ((t0 + t) >> 1) * 8192 + (16 * ks + 8 * c.h + c.q) * 128 + ((((t0 + t) ^ (c.q >> 1)) & 1) << 6) + 32 * ((c.lane >> 4) & 1) + 8 * c.p;
    }
    const unsigned selw = (c.lane & 15) == ((c.lane >> 4) & 1) ? 0x3f803f80u : 0u;
    c.sel = __builtin_bit_cast(bf16x8, u32x4{selw, selw, selw, selw});
    c.cp = (AS3 float*)(L + P1_CP);
    c.pv = (AS3 float*)(L + P1_PRIV + wid * 512);
}
DI bf16x8 wfrag(const P1Lane& c, lptr base, int t, int i) { return cat8(trread(base + c.offW[i][t]), trread(base + c.offW[i][t] + 512)); }
template <int NT> DI void cs_put(const P1Lane& c, int tb, const f32x4 (&cs)[NT]) {
    if ((c.lane & 15) < 2) {
#pragma unroll
        for (int t = 0; t < NT; ++t) *(AS3 f32x4*)(c.cp + c.m * 256 + (tb + t) * 32 + (c.lane & 15) * 16 + (c.lane >> 4) * 4) = cs[t];
    }
}
template <int ROLE, int NT, int NX>
DI void p1_stream(const P1Src& src, const P1Lane& c, lptr L, int wid, bf16x8 (&xf)[NX], f32x16 (&acc)[NT], float& mu, float& var TARG) {
    constexpr int WB = ROLE ? P1_WZ : P1_WQ, TB = ROLE ? 0 : 4;
    f32x4 cs[NT]; bf16x8 w[2][NT];
#pragma unroll
    for (int t = 0; t < NT; ++t) { cs[t] = f32x4{0.f, 0.f, 0.f, 0.f};
#pragma unroll
        for (int i = 0; i < 16; ++i) acc[t][i] = 0.f; }
    float sx[2] = {0.f, 0.f}, sq[2] = {0.f, 0.f};
    auto stat = [&](unsigned u) { const float lo = bflo(u), hi = bfhi(u);
        sx[0] += lo; sx[1] += hi; sq[0] = __builtin_fmaf(lo, lo, sq[0]); sq[1] = __builtin_fmaf(hi, hi, sq[1]);
        PIN(sx[0]); PIN(sx[1]); PIN(sq[0]); PIN(sq[1]); };
    AS3 f32x2* sp = (AS3 f32x2*)(L + P1_SP);
    f32x2 cpart[4], st[4];
    SB();
    p1_stage<1>(src);
    waitbar<5>();
    TMARK(1);
    xf[0] = *(const AS3 bf16x8*)(L + c.offA[0]);
#pragma unroll
    for (int t = 0; t < NT; ++t) w[0][t] = wfrag(c, L + WB, t, 0);
    unroll<28>([&](auto S) { constexpr int s = S, kt = s >> 2, i = s & 3, n = s + 1, nb = ((n >> 2) % 3) * P1_SLOT;
        SB();
        if constexpr (i == 3) { if constexpr (kt < 5) waitbar<5>(); else if constexpr (kt == 5) waitbar<4>(); else lbar(); }
        if constexpr (n < 28) xf[n % NX] = *(const AS3 bf16x8*)(L + nb + c.offA[n & 3]);
        else {
#pragma unroll
            for (int j = 0; j < 4; ++j) cpart[j] = *(const AS3 f32x2*)(c.cp + 256 * j + TB * 32 + 2 * c.lane);
            st[0] = sp[c.m * 64 + c.r]; st[1] = sp[c.m * 64 + 32 + c.r]; st[2] = sp[(c.m + 4) * 64 + c.r]; st[3] = sp[(c.m + 4) * 64 + 32 + c.r];
        }
        const u32x4 ua = __builtin_bit_cast(u32x4, xf[s % NX]);
        constexpr int pk = i == 3 ? kt + 3 : kt + 2;
        constexpr bool pon = pk < 9 && (i != 2 || kt == 0);
        constexpr int pj = kt == 0 && i < 3 ? 2 * i : 2 * ((i + 1) & 3);
        unroll<NT>([&](auto T) { constexpr int t = T;
            SB();
            acc[t] = MFMA32(w[s & 1][t], xf[s % NX], acc[t]);
            if constexpr (i == 2) cs[t] = MFMA16(w[s & 1][t], c.sel, cs[t]);
            SB();
            if constexpr (n < 28) w[n & 1][t] = wfrag(c, L + nb + WB, t, n & 3);
            if constexpr (i < 2) { if constexpr (NT == 2) { stat(ua[2 * t]); stat(ua[2 * t + 1]); } else { stat(ua[t]); if constexpr (t == 2) stat(ua[3]); } }
            if constexpr (pon && t < 2 && pj + t < 5) p1_piece<pon ? pk : 0, pj + t>(src);
        });
        if constexpr (s == 25) sp[wid * 64 + c.lane] = f32x2{sx[0] + sx[1], sq[0] + sq[1]};
        if constexpr (s == 26) { SB(); cs_put<NT>(c, TB, cs); }
    });
    SB();
    TMARK(2);
    *(AS3 f32x2*)(c.pv + 2 * c.lane) = (cpart[0] + cpart[1]) + (cpart[2] + cpart[3]);
    const f32x2 stt = (st[0] + st[1]) + (st[2] + st[3]);
    mu = stt[0] * (1.0f / 448.0f); var = fmaxf(stt[1] * (1.0f / 448.0f) - mu * mu, 0.f) + 1e-5f;
}

DI void phase1_qkv(const P1Src& src, char* ws, lptr L, int mt, int cg, int wid, u64 tag TARG) {
    P1Lane c; p1_lane(c, L, wid, cg & 1);
    const int r = c.r, h = c.h, tt = mt * 4 + c.m;
    unsigned qo[3][2];
#pragma unroll
    for (int t = 0; t < 3; ++t) {
        const int T = 3 * cg + t;
        if (T >= 28) { const int cc = (T - 28) * 32 + r, head = cc / 28, d = cc - head * 28;
            qo[t][0] = WS_KVF + ((head * 64 + tt) * 4 + 2) * 1024 + (h * 32 + d) * 16; qo[t][1] = d;
        } else {
            const bool isq = T < 14; const int Tp = isq ? T : T - 14;
#pragma unroll
            for (int e = 0; e < 2; ++e) { const int U = 4 * Tp + 2 * e + h, hp = U / 7, u7 = U - 7 * hp, head = 2 * hp + (u7 > 3), uu = u7 & 3;
                qo[t][e] = (isq ? WS_QF + (head * 64 + tt) * 2048 : WS_KVF + (head * 64 + tt) * 4096) + uu * 512 + r * 16; }
        }
        PIN(qo[t][0]); PIN(qo[t][1]);
    }
    f32x16 acc[3]; bf16x8 xf[2]; float mu, var;
    p1_stream<0, 3, 2>(src, c, L, wid, xf, acc, mu, var TPASS);
    const float rs = __builtin_amdgcn_rsqf(var);
    const f32x2 nmu2 = {-mu, -mu};
    const rsrc_t rw = mkrsrc(ws, WS_BYTES);
    const lptr TV = L + L_TRV + c.m * 2048;
    f32x4 cq[3][4];
#pragma unroll
    for (int t = 0; t < 3; ++t)
#pragma unroll
        for (int gq = 0; gq < 4; ++gq) cq[t][gq] = *(const AS3 f32x4*)(c.pv + 32 * t + 8 * gq + 4 * h);
    SB();
#pragma unroll
    for (int t = 0; t < 3; ++t) {
        const int T = 3 * cg + t;
        const float a0 = rs * (T < 14 ? 0.18898223650461362f * 1.4426950408889634f : 1.0f);
        const f32x2 a2 = {a0, a0};
        unsigned o[8];
#pragma unroll
        for (int gq = 0; gq < 4; ++gq) {
            const f32x4 c4 = cq[t][gq];
            o[2 * gq] = pk2v(a2 * fma2(nmu2, f32x2{c4[0], c4[1]}, f32x2{acc[t][4 * gq], acc[t][4 * gq + 1]}));
            o[2 * gq + 1] = pk2v(a2 * fma2(nmu2, f32x2{c4[2], c4[3]}, f32x2{acc[t][4 * gq + 2], acc[t][4 * gq + 3]}));
        }
        if (T >= 28) {
#pragma unroll
            for (int gq = 0; gq < 4; ++gq) *(AS3 u32x2*)(TV + r * 64 + gq * 16 + 8 * h) = u32x2{o[2 * gq], o[2 * gq + 1]};
            const lptr p = TV + (4 * h + c.q) * 64 + ((c.lane >> 4) & 1) * 32 + 8 * c.p;
#pragma unroll
            for (int sx = 0; sx < 2; ++sx) st4(rw, qo[t][0] + sx * 1024, __builtin_bit_cast(u32x4, cat8(trread(p + sx * 1024), trread(p + sx * 1024 + 512))));
            if (qo[t][1] >= 24u) {
                const unsigned v = qo[t][1] == 24u ? 0x3f803f80u : 0u;
                st4(rw, qo[t][0] + 64, u32x4{v, v, v, v}); st4(rw, qo[t][0] + 1024 + 64, u32x4{v, v, v, v});
            }
        } else {
#pragma unroll
            for (int e = 0; e < 2; ++e) {
                const auto a = __builtin_amdgcn_permlane32_swap(o[4 * e], o[4 * e + 2], false, false), b = __builtin_amdgcn_permlane32_swap(o[4 * e + 1], o[4 * e + 3], false, false);
                const bool split = (qo[t][e] & 0x600u) == 0x600u;
                st4(rw, qo[t][e], u32x4{a[0], b[0], split ? 0x00003f80u : a[1], split ? 0u : b[1]});
                if (split) st4(rw, qo[t][e] + (T < 14 ? 64 * 2048 : 64 * 4096), u32x4{a[1], b[1], 0x00003f80u, 0u});
            }
        }
    }
    TMARK(3);
    publish(lcnt(L, C_PUB1_A), 4, (gu64*)(ws + WS_F1) + cg * 16 + mt, 8, 224, tag);
    TMARK(4);
}

DI void phase1_z(const P1Src& src, const char* wsp, int mi, char* ws, lptr L, int mt, int cg, int wid, u64 tag TARG) {
    P1Lane c; p1_lane(c, L, wid, 0);
    const int lane = c.lane, r = c.r, h = c.h, tt = mt * 4 + c.m;
    bf16x8 xf[28]; f32x16 acc[2]; f32x2 mv;
    { float mu, var; p1_stream<1, 2, 28>(src, c, L, wid, xf, acc, mu, var TPASS); mv = f32x2{mu, var}; }
    const float rs = __builtin_amdgcn_rsqf(mv[1]);
    const f32x2 nmu2 = {-mv[0], -mv[0]};
    const float A1 = -2.3022081985f * rs, A3 = (-2.3022081985f * 0.044715f) * rs * rs * rs, sd = mv[1] * rs;
    const f32x2 A1v = {A1, A1}, A3v = {A3, A3}, sdv = {sd, sd};
    const rsrc_t rw = mkrsrc(ws, WS_BYTES);
    f32x2 gz[2][8], zs = {0.f, 0.f}, zq = {0.f, 0.f};
#pragma unroll
    for (int t = 0; t < 2; ++t) {
        f32x4 cz[4];
#pragma unroll
        for (int gq = 0; gq < 4; ++gq) cz[gq] = *(const AS3 f32x4*)(c.pv + 32 * t + 8 * gq + 4 * h);
        gelu16(gz[t], acc[t], cz, nmu2, A1v, A3v, sdv);
        f32x2 a = gz[t][0] + gz[t][1], b = gz[t][0] * gz[t][0], d = gz[t][1] * gz[t][1];
#pragma unroll
        for (int j = 2; j < 8; j += 2) { zs += gz[t][j]; a += gz[t][j + 1]; b = fma2(gz[t][j], gz[t][j], b); d = fma2(gz[t][j + 1], gz[t][j + 1], d); }
        zs += a; zq += b + d;
        SB();
    }
    const unsigned tagA = (unsigned)tag, tagB = (unsigned)(tag >> 32);
    const float ps = halfsum(zs[0] + zs[1]), pq = halfsum(zq[0] + zq[1]);
    const unsigned go = WS_ST + tt * 14 * 512;
    vm_wait<0>(); larrive(lcnt(L, C_Z1));
    if (h == 0) st4(rw, go + cg * 512 + r * 16, u32x4{__float_as_uint(ps), tagA, __float_as_uint(pq), tagB});
    SB();
    TMARK(3);
    u32x4 gr[7];
    lwait4(lcnt(L, C_Z1)); lwait4(lcnt(L, C_PUB1_A));
    {
        f32x4 cs[2]; bf16x8 w[3][2];
#pragma unroll
        for (int t = 0; t < 2; ++t) { cs[t] = f32x4{0.f, 0.f, 0.f, 0.f}; w[0][t] = wfrag(c, L + P1_WZ1, t, 0); w[1][t] = wfrag(c, L + P1_WZ1, t, 1);
#pragma unroll
            for (int i = 0; i < 16; ++i) acc[t][i] = 0.f; }
        unroll<28>([&](auto S) { constexpr int s = S, n = s + 2;
            unroll<2>([&](auto T) { constexpr int t = T;
                SB();
                acc[t] = MFMA32(w[s % 3][t], xf[s], acc[t]);
                if constexpr ((s & 3) == 0) cs[t] = MFMA16(w[s % 3][t], c.sel, cs[t]);
                SB();
                if constexpr (n < 28) w[n % 3][t] = wfrag(c, L + P1_WZ1 + (n >> 2) * 8192, t, n & 3);
            });
            if constexpr (s >= GRS && s < GRS + 7) gr[s - GRS] = ld4(rw, lane * 16 + ((s - GRS) & 3) * 1024, go + ((s - GRS) >> 2) * 4096);
            if constexpr (s == 25) { SB(); cs_put<2>(c, 2, cs); asm volatile("s_waitcnt lgkmcnt(0)" ::: "memory"); larrive(lcnt(L, C_CS_B)); }
        });
        PIN(acc[0]); PIN(acc[1]);
        SB();
    }
    __builtin_amdgcn_s_setprio(2);
    TMARK(13);
    float t1, t2;
    for (int spin = 0; spin < SPIN_CAP; ++spin) {
        bool ok = true; t1 = 0.f; t2 = 0.f;
#pragma unroll
        for (int i = 0; i < 7; ++i) { ok &= gr[i][1] == tagA && gr[i][3] == tagB; t1 += __uint_as_float(gr[i][0]); t2 += __uint_as_float(gr[i][2]); }
        if (__all(ok)) break;
        asm volatile("" ::: "memory");
#pragma unroll
        for (int i = 0; i < 7; ++i) gr[i] = ld4(rw, lane * 16 + (i & 3) * 1024, go + (i >> 2) * 4096);
    }
    t1 = halfsum(t1); t2 = halfsum(t2);
    const float zm = t1 * (1.0f / 896.0f), zv = fmaxf(t2 * (1.0f / 896.0f) - zm * zm, 0.f);
    const float zr = __builtin_amdgcn_rsqf(zv + 1e-5f);
    const f32x2 zr2 = {zr, zr}, zb2 = {-zm * zr, -zm * zr};
    const lptr T = L + L_TRZ + c.m * 4096;
    const int kq = lane >> 4;
#pragma unroll
    for (int t = 0; t < 2; ++t)
#pragma unroll
        for (int gq = 0; gq < 4; ++gq)
            *(AS3 u32x2*)(T + (2 * t + (gq >> 1)) * 1024 + r * 32 + (gq & 1) * 16 + 8 * h) = u32x2{pk2v(fma2(gz[t][2 * gq], zr2, zb2)), pk2v(fma2(gz[t][2 * gq + 1], zr2, zb2))};
#pragma unroll
    for (int i = 0; i < 4; ++i) {
        const bf16x8 v = cat8(trread(T + i * 1024 + (8 * kq + c.q) * 32 + 8 * c.p), trread(T + i * 1024 + (8 * kq + 4 + c.q) * 32 + 8 * c.p));
        const int F = 4 * cg + i, j = F / 7, f = F - 7 * j;
        st4(rw, WS_Z2N + ((j * 64 + tt) * 7 + f) * 1024 + lane * 16, __builtin_bit_cast(u32x4, v));
    }
    SB();
    lwait4(lcnt(L, C_CS_B));
    {   f32x2 cpart[4];
#pragma unroll
        for (int j = 0; j < 4; ++j) cpart[j] = *(const AS3 f32x2*)(c.cp + 256 * j + 64 + 2 * r);
        *(AS3 f32x2*)(c.pv + 2 * r) = (cpart[0] + cpart[1]) + (cpart[2] + cpart[3]); }
    u32x4 z1p[4];
#pragma unroll
    for (int t = 0; t < 2; ++t) {
        f32x4 cz[4]; f32x2 g1[8];
#pragma unroll
        for (int gq = 0; gq < 4; ++gq) cz[gq] = *(const AS3 f32x4*)(c.pv + 32 * t + 8 * gq + 4 * h);
        gelu16(g1, acc[t], cz, nmu2, A1v, A3v, sdv);
        z1p[2 * t] = u32x4{pk2v(g1[0]), pk2v(g1[1]), pk2v(g1[2]), pk2v(g1[3])}; z1p[2 * t + 1] = u32x4{pk2v(g1[4]), pk2v(g1[5]), pk2v(g1[6]), pk2v(g1[7])};
        PIN(z1p[2 * t]); PIN(z1p[2 * t + 1]);
        SB();
        if (t == PUBT) publish(lcnt(L, C_PUB1_B), 4, (gu64*)(ws + WS_F1) + 2048 + cg * 16 + mt, 8, 224, tag);
    }
    TMARK(4);
#pragma unroll
    for (int i = 0; i < 4; ++i) st4(rw, WS_ZF + (tt * 56 + cg * 4 + i) * 1024 + lane * 16, z1p[i]);
    { const GW gw = make_gw(wsp, L, mi, c.m); gw.tile<0>(); gw.tile<1>(); }
    SB();
}

DI void helper(const char* wqkv, const char* win, const unsigned short* wo, const unsigned short* wout, const char* wsp, char* ws, lptr L, int sp, int wid, u64 tag) {
    const int lane = otid() & 63, gw = sp * 8 + wid, h = lane >> 5;
    const rsrc_t rw = mkrsrc(ws, WS_BYTES);
    for (int b = 0; b < 2; ++b) {
        u32x4 t[16];
#pragma unroll
        for (int i = 0; i < 16; ++i) t[i] = *(const u32x4*)(wsp + (size_t)((b * 16 + i) * 256 + gw) * 1024 + lane * 16);
#pragma unroll
        for (int i = 0; i < 16; ++i) asm volatile("" :: "v"(t[i]));
    }
    unsigned e[5][8];
#pragma unroll
    for (int i = 0; i < 5; ++i) {
        const int f = min(gw + 256 * i, 14 * 88 - 1), nt = f / 88, ks = f - nt * 88, col = nt * 32 + (lane & 31);
        const unsigned short* src = ks < 32 ? wo + (size_t)((ks >> 1) * 28 + (ks & 1) * 16) * 448 : wout + (size_t)(ks - 32) * 16 * 448;
        const int hi = (ks < 32 && (ks & 1)) ? 0 : 4 * h;
#pragma unroll
        for (int j = 0; j < 8; ++j) e[i][j] = src[(8 * (j >> 2) + (j < 4 ? 4 * h : hi) + (j & 3)) * 448 + col];
    }
#pragma unroll
    for (int i = 0; i < 5; ++i) {
        const int f = min(gw + 256 * i, 14 * 88 - 1), ks = f % 88;
        const bool pad = ks < 32 && (ks & 1) && h;
        st4(rw, WS_BF + f * 1024 + lane * 16, u32x4{e[i][0] | (e[i][1] << 16), e[i][2] | (e[i][3] << 16), pad ? 0u : e[i][4] | (e[i][5] << 16), pad ? 0u : e[i][6] | (e[i][7] << 16)});
    }
    publish(lcnt(L, C_PUBH), 8, (gu64*)(ws + WS_FH) + sp, 8, 32, tag);
}

DI void attn_store(char* ws, const f32x16& o, int head, int tt) {
    const int lane = otid() & 63;
    const float inv = 1.0f / hihalf(o[12]);
    const rsrc_t rw = mkrsrc(ws, WS_BYTES);
#pragma unroll
    for (int s = 0; s < 2; ++s)
        st4(rw, WS_AF + (tt * 88 + 2 * head + s) * 1024 + lane * 16,
            u32x4{pk2(o[8 * s] * inv, o[8 * s + 1] * inv), pk2(o[8 * s + 2] * inv, o[8 * s + 3] * inv), pk2(o[8 * s + 4] * inv, o[8 * s + 5] * inv), pk2(o[8 * s + 6] * inv, o[8 * s + 7] * inv)});
}
DI void attn_safe(char* ws, f32x16& o, int head, int qt) {
    const int lane = otid() & 63, h = lane >> 5;
    const rsrc_t rw = mkrsrc(ws, WS_BYTES);
    const unsigned lo = lane * 16, qp = WS_QF + (head * 64 + qt) * 2048;
    const bf16x8 q0 = ld8(rw, lo, qp); u32x4 q1u = ld4(rw, lo + 1024, qp);
    if (h) { q1u[2] = 0u; q1u[3] = 0u; }
    const bf16x8 q1 = __builtin_bit_cast(bf16x8, q1u);
    f32x16 zero;
#pragma unroll
    for (int i = 0; i < 16; ++i) { o[i] = 0.f; zero[i] = 0.f; }
    float m = -3.0e38f;
#pragma unroll 1
    for (int kt = 0; kt < 64; ++kt) {
        const unsigned kp = WS_KVF + (head * 64 + kt) * 4096;
        const bf16x8 k0 = ld8(rw, lo, kp), k1 = ld8(rw, lo + 1024, kp), v0 = ld8(rw, lo + 2048, kp), v1 = ld8(rw, lo + 3072, kp);
        f32x16 s = MFMA32(k0, q0, zero); s = MFMA32(k1, q1, s);
        float mx = s[0];
#pragma unroll
        for (int i = 1; i < 16; ++i) mx = fmaxf(mx, s[i]);
        mx = fmaxf(halfmax(mx), m);
        const float f = EX2(m - mx); m = mx;
        u32x4 p0, p1;
#pragma unroll
        for (int i = 0; i < 16; ++i) { o[i] *= f; s[i] = EX2(s[i] - m); }
#pragma unroll
        for (int i = 0; i < 4; ++i) { p0[i] = pk2(s[2 * i], s[2 * i + 1]); p1[i] = pk2(s[8 + 2 * i], s[9 + 2 * i]); }
        o = MFMA32(v0, __builtin_bit_cast(bf16x8, p0), o); o = MFMA32(v1, __builtin_bit_cast(bf16x8, p1), o);
    }
}

DI void attention(char* ws, lptr L, int head, int qb, int w, int rep, u64 tag TARG) {
    const int lane = otid() & 63, h = lane >> 5;
    if (w == 0) {
        const int c = lane >> 4, cgk = (448 * (1 + (c >> 1)) + 28 * head + 27 * (c & 1)) / 96, cgq = (28 * head + 27 * (lane & 1)) / 96;
        gu64* f = (gu64*)(ws + WS_F1) + rep * 224;
        poll2(f + cgk * 16 + (lane & 15), f + cgq * 16 + qb, tag);
        if (lane == 0) __hip_atomic_store(lcnt(L, C_RDY_A), 1u, RLX_WG);
    } else wait_ready(lcnt(L, C_RDY_A));
    TMARK(5);
    const rsrc_t rw = mkrsrc(ws, WS_BYTES);
    unsigned kv = WS_KVF + head * 64 * 4096;
    const unsigned lo = lane * 16;
    bf16x8 q0[4]; u32x4 q1[4];
    bf16x8 r0 = ld8(rw, lo, kv), r1 = ld8(rw, lo + 1024, kv);
#pragma unroll
    for (int t = 0; t < 4; ++t) { const unsigned qp = WS_QF + (head * 64 + qb * 4 + ((w + t) & 3)) * 2048;
        q0[t] = ld8(rw, lo, qp); q1[t] = ld4(rw, lo + 1024, qp); }
    kv += w * 16 * 4096;
    bf16x8 ka0 = ld8(rw, lo, kv), ka1 = ld8(rw, lo + 1024, kv), va0 = ld8(rw, lo + 2048, kv), va1 = ld8(rw, lo + 3072, kv);
    bf16x8 kb0 = ld8(rw, lo, kv + 4096), kb1 = ld8(rw, lo + 1024, kv + 4096), vb0, vb1;
    f32x16 zero, o[4], sA, sB;
    u32x4 p0, p1 = {0u, 0u, 0u, 0u};
#pragma unroll
    for (int i = 0; i < 16; ++i) zero[i] = 0.f;
#pragma unroll
    for (int i = 0; i < 8; ++i) vb1[i] = 0;
#pragma unroll
    for (int t = 0; t < 4; ++t) {
        q1[t][2] = h ? 0u : q1[t][2]; q1[t][3] = h ? 0u : q1[t][3];
        o[t] = MFMA32(r0, q0[t], zero); o[t] = MFMA32(r1, __builtin_bit_cast(bf16x8, q1[t]), o[t]);
    }
#pragma unroll
    for (int t = 0; t < 4; ++t) {
        float mx = fmaxf(o[t][0], o[t][1]);
#pragma unroll
        for (int i = 2; i < 16; i += 2) mx = fmaxf(fmaxf(mx, o[t][i]), o[t][i + 1]);
        mx = halfmax(mx);
        q1[t][2] = h ? pk2(-(mx + 32.0f), 0.f) : q1[t][2];
#pragma unroll
        for (int i = 0; i < 16; ++i) o[t][i] = 0.f;
    }
#define Q1(t) __builtin_bit_cast(bf16x8, q1[t])
#define PB(x) __builtin_bit_cast(bf16x8, x)
#define ASTEP(A, B, O, V0, OP, VP, K0, K1, QN) do { \
    B = MFMA32(K0, q0[QN], zero); SB(); \
    A[0] = EX2(A[0]); A[1] = EX2(A[1]); A[2] = EX2(A[2]); A[3] = EX2(A[3]); PIN(A); SB(); \
    OP = MFMA32(VP, PB(p1), OP); SB(); \
    p0[0] = pk2(A[0], A[1]); p0[1] = pk2(A[2], A[3]); SB(); A[4] = EX2(A[4]); A[5] = EX2(A[5]); A[6] = EX2(A[6]); A[7] = EX2(A[7]); PIN(A); PIN(p0); SB(); \
    B = MFMA32(K1, Q1(QN), B); SB(); \
    p0[2] = pk2(A[4], A[5]); p0[3] = pk2(A[6], A[7]); SB(); A[8] = EX2(A[8]); A[9] = EX2(A[9]); A[10] = EX2(A[10]); A[11] = EX2(A[11]); PIN(A); PIN(p0); SB(); \
    O = MFMA32(V0, PB(p0), O); SB(); \
    A[12] = EX2(A[12]); A[13] = EX2(A[13]); A[14] = EX2(A[14]); A[15] = EX2(A[15]); SB(); \
    p1[0] = pk2(A[8], A[9]); p1[1] = pk2(A[10], A[11]); SB(); p1[2] = pk2(A[12], A[13]); SB(); p1[3] = pk2(A[14], A[15]); PIN(p1); SB(); } while (0)
#define LDK(K0, K1, off) do { K0 = ld8(rw, lo, kv + (off)); K1 = ld8(rw, lo + 1024, kv + (off)); SB(); } while (0)
#define LDV(V0, V1, off) do { V0 = ld8(rw, lo + 2048, kv + (off)); V1 = ld8(rw, lo + 3072, kv + (off)); SB(); } while (0)
#define APAIR(MORE) do { \
    ASTEP(sA, sB, o[0], va0, o[3], vb1, ka0, ka1, 1); LDV(vb0, vb1, 4096); \
    ASTEP(sB, sA, o[1], va0, o[0], va1, ka0, ka1, 2); \
    ASTEP(sA, sB, o[2], va0, o[1], va1, ka0, ka1, 3); if (MORE) LDK(ka0, ka1, 8192); \
    ASTEP(sB, sA, o[3], va0, o[2], va1, kb0, kb1, 0); \
    ASTEP(sA, sB, o[0], vb0, o[3], va1, kb0, kb1, 1); if (MORE) LDV(va0, va1, 8192); \
    ASTEP(sB, sA, o[1], vb0, o[0], vb1, kb0, kb1, 2); \
    ASTEP(sA, sB, o[2], vb0, o[1], vb1, kb0, kb1, 3); if (MORE) LDK(kb0, kb1, 12288); \
    ASTEP(sB, sA, o[3], vb0, o[2], vb1, ka0, ka1, 0); kv += 8192; } while (0)
    TMARK(6);
    sA = MFMA32(ka0, q0[0], zero); sA = MFMA32(ka1, Q1(0), sA);
    for (int it = 0; it < 7; ++it) APAIR(true);
    APAIR(false);
    o[3] = MFMA32(vb1, PB(p1), o[3]);
    TMARK(7);
    lwait4(lcnt(L, C_PUB1_B));
    const lptr P = L + P2_AT;
#pragma unroll
    for (int t = 1; t < 4; ++t)
#pragma unroll
        for (int g = 0; g < 4; ++g) *(AS3 f32x4*)(P + (w * 3 + t - 1) * 4096 + g * 1024 + lo) = f32x4{o[t][4 * g], o[t][4 * g + 1], o[t][4 * g + 2], o[t][4 * g + 3]};
    role_sync(lcnt(L, C_SYNC_A));
#pragma unroll
    for (int t = 1; t < 4; ++t)
#pragma unroll
        for (int g = 0; g < 4; ++g) { f32x4 v = *(const AS3 f32x4*)(P + (((w - t) & 3) * 3 + t - 1) * 4096 + g * 1024 + lo);
#pragma unroll
            for (int c = 0; c < 4; ++c) o[0][4 * g + c] += v[c]; }
    float chk = 0.f;
#pragma unroll
    for (int i = 0; i < 16; ++i) chk = __builtin_fmaf(o[0][i], 0.f, chk);
    const float l = hihalf(o[0][12]);
    const bool bad = !(chk == 0.f) || !(l > 1.0e-30f) || !(l < 1.0e30f);
    if (__builtin_expect(__any(bad), 0)) attn_safe(ws, o[0], head, qb * 4 + w);
    attn_store(ws, o[0], head, qb * 4 + w);
    TMARK(8);
    publish(lcnt(L, C_PUB2_A), 4, (gu64*)(ws + WS_F2) + qb * 128 + head, 2, 64, tag);
    TMARK(9);
}

constexpr int g_extra(int s) { return s == 7 ? 1 : s == 10 ? 8 : 0; }
constexpr int g_issued_before(int s, int f) {
    int n = 14;
    for (int t = 0; t <= s; ++t) {
        n += g_extra(t);
        for (int g = 0; g < 7; ++g) {
            if (t == s && g == f) return n;
            if (t + 2 < 16) ++n;
            if (!(t & 1) && (t >> 1) + 2 < 8) n += g == 6 ? 2 : 1;
        }
    }
    return n;
}
constexpr int g_pos_z(int s, int f) { return s < 2 ? 7 * s + f + 1 : g_issued_before(s - 2, f) + 1; }
constexpr int g_pos_w(int ds) { return g_issued_before(2 * (ds - 2) + 1, 0) - g_extra(2 * (ds - 2) + 1); }
template <int N> DI void vm_wait_for(bf16x8& v) { asm volatile("s_waitcnt vmcnt(%1)" : "+v"(v) : "i"(N)); }
template <int OFF> DI void abld(bf16x8& d, unsigned voff, const u32x4& rs, unsigned soff) {
    asm volatile("buffer_load_dwordx4 %0, %1, %2, %3 offen offset:%4 sc1" : "=v"(d) : "v"(voff), "s"(rs), "s"(soff), "i"(OFF));
}
template <int OFF> DI void abld_rw(bf16x8& d, unsigned voff, const u32x4& rs, unsigned soff) {
    asm volatile("buffer_load_dwordx4 %0, %1, %2, %3 offen offset:%4 sc1" : "+v"(d) : "v"(voff), "s"(rs), "s"(soff), "i"(OFF));
}

template <int S> struct GStep {
    template <class C> static DI void run(C& c) {
        constexpr int ks = S & 1;
        if constexpr (S + 1 < 16) {
            constexpr int nds = (S + 1) >> 1;
            if constexpr (ks == 1 && nds >= 2) vm_wait<g_issued_before(S, 0) - g_extra(S) - g_pos_w(nds)>();
#pragma unroll
            for (int t = 0; t < 4; ++t) c.wf[(S + 1) & 1][t] = *(const AS3 bf16x8*)(c.W + (nds % 3) * 8192 + t * 2048 + (ks ? c.rd0 : c.rd1));
        }
        if constexpr (S == 7) asm volatile("buffer_load_dwordx2 %0, %1, %2, 0 offen sc1" : "=v"(c.flag) : "v"(c.fo), "s"(c.rw));
        if constexpr (S == 10) c.epi_loads();
        SB();
        group<0>(c); group<1>(c); group<2>(c); group<3>(c); group<4>(c); group<5>(c); group<6>(c);
        if constexpr (S + 1 < 16 && S != 9) GStep<S + 1>::run(c);
    }
    template <int F, class C> static DI void group(C& c) {
        constexpr int ds = S >> 1, ks = S & 1;
        vm_wait_for<g_issued_before(S, F) - g_pos_z(S, F)>(c.z[S & 1][F]);
#pragma unroll
        for (int t = 0; t < 4; ++t) c.acc[F][t] = MFMA16(c.z[S & 1][F], c.wf[S & 1][t], c.acc[F][t]);
        SB();
        if constexpr (S + 2 < 16) { constexpr int u = (S + 2) * 7 + F; abld_rw<(u & 3) * 1024>(c.z[S & 1][F], c.lo, c.rw, c.zsrc + (u >> 2) * 4096); }
        if constexpr (ks == 0 && ds + 2 < 8) { c.gw.template piece<ds + 2, F>(); if constexpr (F == 6) c.gw.template piece<ds + 2, 7>(); }
        if constexpr (S == 15 && F > 0) {
#pragma unroll
            for (int t = 1; t < 4; ++t) *(AS3 f32x4*)(c.W + ((t - 1) * 7 + F - 1) * 1024 + c.lo) = c.acc[F > 0 ? F - 1 : 0][t];
        }
        SB();
    }
};
struct GCtx {
    lptr W; unsigned lo, zsrc, zo, bo, fo; int rd0, rd1; GW gw; u32x4 rw, rb;
    f32x4 acc[7][4]; bf16x8 z[2][7], wf[2][4]; u32x2 z1[7], flag; unsigned braw;
    DI void epi_loads() {
        asm volatile("buffer_load_dwordx2 %0, %4, %5, 0 offen sc1\n\tbuffer_load_dwordx2 %1, %4, %5, 0 offen offset:1024 sc1\n\tbuffer_load_dwordx2 %2, %4, %5, 0 offen offset:2048 sc1\n\tbuffer_load_dwordx2 %3, %4, %5, 0 offen offset:3072 sc1"
                     : "=&v"(z1[0]), "=&v"(z1[1]), "=&v"(z1[2]), "=&v"(z1[3]) : "v"(zo), "s"(rw));
        asm volatile("buffer_load_dwordx2 %0, %4, %5, %6 offen sc1\n\tbuffer_load_dwordx2 %1, %4, %5, %6 offen offset:1024 sc1\n\tbuffer_load_dwordx2 %2, %4, %5, %6 offen offset:2048 sc1\n\tbuffer_load_ushort %3, %7, %8, 0 offen"
                     : "=&v"(z1[4]), "=&v"(z1[5]), "=&v"(z1[6]), "=&v"(braw) : "v"(zo), "s"(rw), "s"(4096u), "v"(bo), "s"(rb));
    }
};

DI void spatial(const char* wsp, const unsigned short* bsp, char* ws, lptr L, int mi, int j, int w, int rep, int z1flag, u64 tag TARG) {
    const int lane = otid() & 63, r = lane & 15, kq = lane >> 4;
    GCtx c;
    c.W = L + w * P2_GP; c.gw = make_gw(wsp, L, mi, w); c.lo = lane * 16;
    c.rw = rawrsrc(ws, WS_BYTES); c.rb = rawrsrc(bsp, 4096);
    c.zsrc = WS_Z2N + (j * 64 + w * 16) * 7 * 1024;
    c.rd0 = r * 128 + ((kq ^ (r >> 1)) << 4); c.rd1 = c.rd0 ^ 64;
    const int tok = mi * 64 + w * 16 + r, tt = mi * 2 + (w >> 1), cgp = min((7 * j) / 4 + kq, (7 * j + 6) / 4);
    c.zo = WS_ZF + (tt * 56 + 7 * j) * 1024 + ((kq & 1) * 32 + (tok & 31)) * 16 + (kq >> 1) * 8; c.bo = tok * 2;
    c.fo = WS_F1 + (4096 + cgp * 16 + (mi >> 1)) * 8;
#pragma unroll
    for (int a = 0; a < 7; ++a)
#pragma unroll
        for (int b = 0; b < 4; ++b) c.acc[a][b] = f32x4{0.f, 0.f, 0.f, 0.f};
    if (z1flag >= 0) publish(lcnt(L, C_PUB1_Z), 4, (gu64*)(ws + WS_F1) + 4096 + z1flag, 1, 0, tag); else vm_wait<0>();
#pragma unroll
    for (int t = 0; t < 4; ++t) c.wf[0][t] = *(const AS3 bf16x8*)(c.W + t * 2048 + c.rd0);
    if (w == 0) {
        gu64* f = (gu64*)(ws + WS_F1) + 2048 + rep * 224 + cgp * 16 + r;
        poll2(f, f, tag);
        if (lane == 0) __hip_atomic_store(lcnt(L, C_RDY_B), 1u, RLX_WG);
    } else wait_ready(lcnt(L, C_RDY_B));
    TMARK(5);
    vm_wait<0>();
#define ZP1(s, f) abld<(((s) * 7 + (f)) & 3) * 1024>(c.z[s][f], c.lo, c.rw, c.zsrc + (((s) * 7 + (f)) >> 2) * 4096)
#define ZPRO(s) ZP1(s, 0); ZP1(s, 1); ZP1(s, 2); ZP1(s, 3); ZP1(s, 4); ZP1(s, 5); ZP1(s, 6)
    ZPRO(0); ZPRO(1);
    SB();
    __builtin_amdgcn_s_setprio(GPRIO);
    TMARK(6);
    GStep<0>::run(c);
    asm volatile("" : "+v"(c.flag));
    if (__builtin_expect(!__all(c.flag[0] == (unsigned)tag && c.flag[1] == (unsigned)(tag >> 32)), 0)) { gu64* f = (gu64*)(ws + c.fo); poll2(f, f, tag); }
    SB();
    GStep<10>::run(c);
    TMARK(7);
#pragma unroll
    for (int t = 1; t < 4; ++t) *(AS3 f32x4*)(c.W + ((t - 1) * 7 + 6) * 1024 + c.lo) = c.acc[6][t];
    role_sync(lcnt(L, C_SYNC_B));
    asm volatile("s_waitcnt vmcnt(0)" : "+v"(c.z1[0]), "+v"(c.z1[1]), "+v"(c.z1[2]), "+v"(c.z1[3]), "+v"(c.z1[4]), "+v"(c.z1[5]), "+v"(c.z1[6]), "+v"(c.braw));
    const float bias = bflo(c.braw);
    u32x2 o[8];
#pragma unroll
    for (int f = 0; f < 7; ++f) {
        f32x4 g = c.acc[f][0];
#pragma unroll
        for (int t = 1; t < 4; ++t) g += *(const AS3 f32x4*)(L + ((w - t) & 3) * P2_GP + ((t - 1) * 7 + f) * 1024 + c.lo);
        o[f] = u32x2{pk2((g[0] + bias) * bflo(c.z1[f][0]), (g[1] + bias) * bfhi(c.z1[f][0])), pk2((g[2] + bias) * bflo(c.z1[f][1]), (g[3] + bias) * bfhi(c.z1[f][1]))};
    }
    o[7] = o[6];
    const rsrc_t rws = mkrsrc(ws, WS_BYTES);
    const unsigned ho = WS_AF + (tt * 88 + 32 + 7 * j) * 1024 + ((kq & 1) * 32 + (tok & 31)) * 16 + (kq >> 1) * 1024;
#pragma unroll
    for (int f = 0; f < 8; f += 2) {
        const auto a = __builtin_amdgcn_permlane32_swap(o[f][0], o[f + 1][0], false, false), b = __builtin_amdgcn_permlane32_swap(o[f][1], o[f + 1][1], false, false);
        if (f < 6 || lane < 32) st4(rws, ho + f * 1024, u32x4{a[0], b[0], a[1], b[1]});
    }
    TMARK(8);
    publish(lcnt(L, C_PUB2_B), 4, (gu64*)(ws + WS_F2) + mi * 64 + 16 + j, 1, 0, tag);
    TMARK(9);
}

constexpr int P3_ROW = 272, P3_TILE = 64 * P3_ROW;
DI void outproj(const char* x, char* out, char* ws, lptr L, int mi, int nj, int w, int rep, u64 tag TARG) {
    const int tid = otid(), lane = tid & 63, r = lane & 31, h = lane >> 5;
    const rsrc_t rw = mkrsrc(ws, WS_BYTES);
    const unsigned lo = lane * 16;
    f32x16 acc[2][2];
#pragma unroll
    for (int i = 0; i < 16; ++i) { acc[0][0][i] = 0.f; acc[0][1][i] = 0.f; acc[1][0][i] = 0.f; acc[1][1][i] = 0.f; }
    auto part = [&](auto NN, int ks0, int f0, int mask) { constexpr int N = NN;
        const unsigned bo = WS_BF + (nj * 2 * 88 + ks0) * 1024, ao = WS_AF + (mi * 2 * 88 + ks0) * 1024;
        bf16x8 a[N][2], b[N][2];
#pragma unroll
        for (int s = 0; s < N; ++s) { b[s][0] = ld8(rw, lo + (s & 3) * 1024, bo + (s >> 2) * 4096); b[s][1] = ld8(rw, lo + (s & 3) * 1024, bo + 88 * 1024 + (s >> 2) * 4096); }
        SB();
        gu64* f = (gu64*)(ws + WS_F2) + mi * 64 + f0 + (lane & mask);
        for (int spin = 0; spin < SPIN_CAP; ++spin) { if (__all(__hip_atomic_load(f, RLX_AGENT) == tag)) break; __builtin_amdgcn_s_sleep(P3SL); }
        asm volatile("" ::: "memory");
        TMARK(10);
#pragma unroll
        for (int s = 0; s < N; ++s) { a[s][0] = ld8(rw, lo + (s & 3) * 1024, ao + (s >> 2) * 4096); a[s][1] = ld8(rw, lo + (s & 3) * 1024, ao + 88 * 1024 + (s >> 2) * 4096); }
        SB();
#pragma unroll
        for (int s = 0; s < N; ++s) {
            acc[0][0] = MFMA32(b[s][0], a[s][0], acc[0][0]); acc[0][1] = MFMA32(b[s][1], a[s][0], acc[0][1]);
            acc[1][0] = MFMA32(b[s][0], a[s][1], acc[1][0]); acc[1][1] = MFMA32(b[s][1], a[s][1], acc[1][1]);
        }
        PIN(acc[0][0]); PIN(acc[0][1]); PIN(acc[1][0]); PIN(acc[1][1]);
        SB();
    };
    { gu64* f = (gu64*)(ws + WS_FH) + rep * 32 + r; poll2(f, f, tag); }
    const u32x4 xr = *(const u32x4*)(x + (unsigned)((mi * 64 + (tid >> 3)) * 896 + nj * 128 + (tid & 7) * 16));
    if (w < 4) part(std::integral_constant<int, 8>{}, 8 * w, 4 * w, 3);
    TMARK(13);
    part(std::integral_constant<int, 7>{}, 32 + 7 * w, 16 + w, 0);
    lwait4(lcnt(L, C_PUB2_A)); lwait4(lcnt(L, C_PUB2_B));
    TMARK(11);
#pragma unroll
    for (int tm = 0; tm < 2; ++tm)
#pragma unroll
        for (int tn = 0; tn < 2; ++tn)
#pragma unroll
            for (int g = 0; g < 4; ++g)
                *(AS3 f32x4*)(L + w * P3_TILE + (tm * 32 + r) * P3_ROW + (tn * 32 + 8 * g + 4 * h) * 4) =
                    f32x4{acc[tm][tn][4 * g], acc[tm][tn][4 * g + 1], acc[tm][tn][4 * g + 2], acc[tm][tn][4 * g + 3]};
    __syncthreads();
    const int row = tid >> 3, c8 = tid & 7;
    f32x4 s0 = {0.f, 0.f, 0.f, 0.f}, s1 = s0;
#pragma unroll
    for (int p = 0; p < 8; ++p) { s0 += *(const AS3 f32x4*)(L + p * P3_TILE + row * P3_ROW + c8 * 32); s1 += *(const AS3 f32x4*)(L + p * P3_TILE + row * P3_ROW + c8 * 32 + 16); }
    const u32x4 o = {pk2(s0[0] + bflo(xr[0]), s0[1] + bfhi(xr[0])), pk2(s0[2] + bflo(xr[1]), s0[3] + bfhi(xr[1])),
                     pk2(s1[0] + bflo(xr[2]), s1[1] + bfhi(xr[2])), pk2(s1[2] + bflo(xr[3]), s1[3] + bfhi(xr[3]))};
    *(u32x4*)(out + (unsigned)((mi * 64 + row) * 896 + nj * 128 + c8 * 16)) = o;
    TMARK(12);
}

__global__ __launch_bounds__(512, 2) void fused_kernel(const char* x, const char* wqkv, const char* win, const unsigned short* wo, const unsigned short* wout,
                                                       const char* wsp, const unsigned short* bsp, char* out, char* ws) {
    extern __shared__ __attribute__((aligned(16))) char smem[];
    TDECL; TMARK(0);
    u64 pc0; unsigned clen;
    asm volatile("s_getpc_b64 %0\nLcode_begin_%=:\n\ts_mov_b32 %1, Lcode_end_mark - Lcode_begin_%=" : "=s"(pc0), "=s"(clen));
    const lptr L = (lptr)smem;
    const int bid = blockIdx.x, xcd = bid & 7, idx = bid >> 3, lane = threadIdx.x & 63;
    const int wid = __builtin_amdgcn_readfirstlane(threadIdx.x >> 6);
    const unsigned lds0 = (unsigned)(uintptr_t)L;
    const int i7 = (idx * 37) >> 8, m7 = idx - 7 * i7;
    const int mt = (xcd >> 1) * 4 + i7, cg = (xcd & 1) * 7 + m7;
    P1Src src;
    {   const unsigned rowo = (mt * 128 + 16 * wid + (lane >> 3)) * 896;
        src.voA0 = rowo + (((lane & 7) ^ (lane >> 4)) << 4); src.voA1 = rowo + 8 * 896 + (((lane & 7) ^ (4 + (lane >> 4))) << 4);
        const int kr8 = 8 * wid + (lane >> 3), c8 = ((lane & 7) ^ (((lane >> 4) & 1) << 2)) << 4;
        src.voZ = kr8 * 3584 + cg * 128 + c8; src.voQ = kr8 * 2688 + ((cg * 192) & ~127) + c8;
        src.ldsx = lds0 + wid * 2048; src.ldsw = lds0 + wid * 1024;
        src.rx = rawrsrc(x, 2048 * 896); src.rq = rawrsrc(wqkv, 448 * 2688); src.rz = rawrsrc(win, 448 * 3584);
        if (idx < 28) p1_stage<0>(src);
        SB();
    }
    unsigned epoch;
    asm volatile("s_load_dword %0, %1, 0x0\n\ts_waitcnt lgkmcnt(0)" : "=s"(epoch) : "s"(ws + WS_EP) : "memory");
    const u64 tag = (u64)(epoch * 0x9E3779B1u + 0x7F4A7C15u) | ((u64)(epoch * 0x85EBCA6Bu + 0x1B873593u) << 32);
    if (threadIdx.x < 32) *lcnt(L, threadIdx.x) = 0u;
    const int mi = (xcd >> 1) * 8 + (idx >> 2), j = (xcd & 1) * 4 + (idx & 3);
    if (idx < 28) {
        if (wid >= 4) phase1_z(src, wsp, mi, ws, L, mt, cg, wid, tag TPASS);
        else phase1_qkv(src, ws, L, mt, cg, wid, tag TPASS);
    } else {
        {
            const rsrc_t rc = mkrsrc((const void*)pc0, clen);
            u32x4 t[3];
#pragma unroll
            for (int i = 0; i < 3; ++i) t[i] = __builtin_amdgcn_raw_buffer_load_b128(rc, (i * 32 + (idx - 28) * 8 + wid) * 1024 + lane * 16, 0, 0);
#pragma unroll
            for (int i = 0; i < 3; ++i) asm volatile("" :: "v"(t[i]));
        }
        __syncthreads();
        helper(wqkv, win, wo, wout, wsp, ws, L, xcd * 4 + idx - 28, wid, tag);
        if (wid < 4) larrive(lcnt(L, C_PUB1_B));
        else {
            const GW gw = make_gw(wsp, L, mi, wid - 4); gw.tile<0>(); gw.tile<1>();
        }
    }
    if (wid < 4) attention(ws, L, 2 * xcd + (idx >> 4), idx & 15, wid, xcd, tag TPASS);
    else spatial(wsp, bsp, ws, L, mi, j, wid - 4, xcd, idx < 28 ? cg * 16 + mt : -1, tag TPASS);
    if (idx < 28) outproj(x, out, ws, L, xcd * 4 + i7, m7, wid, xcd, tag TPASS);
    else if (bid == 255 && wid == 0) {
        const int ln = otid();
        gu64* f = (gu64*)(ws + WS_F2) + (ln >> 4) * 128 + (ln & 15);
        poll2(f, f + 512, tag); poll2(f + 1024, f + 1536, tag);
        if (ln == 0) __hip_atomic_store((gu32*)(ws + WS_EP), epoch + 1u, RLX_AGENT);
    }
    TFLUSH();
    asm volatile("Lcode_end_mark:");
}

extern "C" void kernel_launch(void* const* d_in, const int* in_sizes, int n_in, void* d_out, int out_size, void* d_ws, size_t ws_size, hipStream_t stream) {
    (void)hipFuncSetAttribute((const void*)fused_kernel, hipFuncAttributeMaxDynamicSharedMemorySize, LDS_BYTES);
    fused_kernel<<<256, 512, LDS_BYTES, stream>>>((const char*)d_in[0], (const char*)d_in[1], (const char*)d_in[4], (const unsigned short*)d_in[2], (const unsigned short*)d_in[7],
                                                  (const char*)d_in[5], (const unsigned short*)d_in[6], (char*)d_out, (char*)d_ws);
}
```

```cpp
#include <hip/hip_runtime.h>
#include <hip/hip_bf16.h>
#include <stdint.h>
#include <type_traits>

typedef short bf16x8 __attribute__((ext_vector_type(8)));
typedef short s16x4 __attribute__((ext_vector_type(4)));
typedef float f32x16 __attribute__((ext_vector_type(16)));
typedef float f32x4 __attribute__((ext_vector_type(4)));
typedef float f32x2 __attribute__((ext_vector_type(2)));
typedef unsigned u32x4 __attribute__((ext_vector_type(4)));
typedef unsigned u32x2 __attribute__((ext_vector_type(2)));
typedef __bf16 bf2 __attribute__((ext_vector_type(2)));
typedef unsigned long long u64;
#define AS3 __attribute__((address_space(3)))
typedef AS3 char* lptr;
typedef __attribute__((address_space(1))) unsigned gu32;
typedef __attribute__((address_space(1))) u64 gu64;
typedef __amdgpu_buffer_rsrc_t rsrc_t;
#define DI __device__ __forceinline__
#define SB() __builtin_amdgcn_sched_barrier(0)
#define PIN(x) asm volatile("" : "+v"(x))
#define MFMA32(a, b, c) __builtin_amdgcn_mfma_f32_32x32x16_bf16((a), (b), (c), 0, 0, 0)
#define MFMA16(a, b, c) __builtin_amdgcn_mfma_f32_16x16x32_bf16((a), (b), (c), 0, 0, 0)
#define EX2(x) __builtin_amdgcn_exp2f(x)
#define RLX_AGENT __ATOMIC_RELAXED, __HIP_MEMORY_SCOPE_AGENT
#define RLX_WG __ATOMIC_RELAXED, __HIP_MEMORY_SCOPE_WORKGROUP

constexpr unsigned WS_QF = 0;
constexpr unsigned WS_KVF = 2u << 20;
constexpr unsigned WS_ZF = 6u << 20;
constexpr unsigned WS_Z2N = 10u << 20;
constexpr unsigned WS_AF = 14u << 20;
constexpr unsigned WS_BF = 20u << 20;
constexpr unsigned WS_ST = WS_BF + (3u << 19);
constexpr unsigned WS_EP = 22u << 20;
constexpr unsigned WS_F1 = WS_EP + 4096;
constexpr unsigned WS_F2 = WS_F1 + 49152;
constexpr unsigned WS_FH = WS_F2 + 16384;
constexpr unsigned WS_BYTES = 23u << 20;
constexpr size_t WS_DBG = 28u << 20;

#define TDECL do {} while (0)
#define TARG
#define TPASS
#define TMARK(i) do {} while (0)
#define TFLUSH() do {} while (0)

DI int otid() { int t = threadIdx.x; asm volatile("" : "+v"(t)); return t; }
DI unsigned pk2(float a, float b) { f32x2 v = {a, b}; return __builtin_bit_cast(unsigned, __builtin_convertvector(v, bf2)); }
DI unsigned pk2v(f32x2 v) { return __builtin_bit_cast(unsigned, __builtin_convertvector(v, bf2)); }
DI float bflo(unsigned u) { return __uint_as_float(u << 16); }
DI float bfhi(unsigned u) { return __uint_as_float(u & 0xffff0000u); }
DI f32x2 fma2(f32x2 a, f32x2 b, f32x2 c) { return __builtin_elementwise_fma(a, b, c); }
DI float sadd(float a, float b) { float d; asm("v_add_f32 %0, %1, %2" : "=v"(d) : "v"(a), "v"(b)); return d; }
DI float smul(float a, float b) { float d; asm("v_mul_f32 %0, %1, %2" : "=v"(d) : "v"(a), "v"(b)); return d; }
DI float sfma(float a, float b, float c) { float d; asm("v_fma_f32 %0, %1, %2, %3" : "=v"(d) : "v"(a), "v"(b), "v"(c)); return d; }
#define NOP1() do { SB(); asm volatile("s_nop 0"); SB(); } while (0)
DI float halfsum(float v) { auto r = __builtin_amdgcn_permlane32_swap(__float_as_uint(v), __float_as_uint(v), false, false); return __uint_as_float(r[0]) + __uint_as_float(r[1]); }
DI float halfmax(float v) { auto r = __builtin_amdgcn_permlane32_swap(__float_as_uint(v), __float_as_uint(v), false, false); return fmaxf(__uint_as_float(r[0]), __uint_as_float(r[1])); }
DI float hihalf(float v) { auto r = __builtin_amdgcn_permlane32_swap(__float_as_uint(v), __float_as_uint(v), false, false); return __uint_as_float(r[1]); }
DI s16x4 trread(lptr p) { return __builtin_bit_cast(s16x4, __builtin_amdgcn_ds_read_tr16_b64_v4i16((AS3 s16x4*)p)); }
DI bf16x8 cat8(s16x4 a, s16x4 b) { return __builtin_shufflevector(a, b, 0, 1, 2, 3, 4, 5, 6, 7); }
template <int GO, int LO> DI void dma16(unsigned voff, const u32x4& rs, unsigned soff, unsigned lds) {
    unsigned tmp;
    asm volatile("s_add_u32 m0, %4, %6\n\ts_add_u32 %0, %3, %5\n\tbuffer_load_dwordx4 %1, %2, %0 offen lds"
                 : "=&s"(tmp) : "v"(voff), "s"(rs), "s"(soff), "s"(lds), "i"(GO), "i"(LO) : "memory", "scc");
}
DI u32x4 rawrsrc(const void* p, unsigned bytes) { const u64 a = (u64)p; return u32x4{(unsigned)a, (unsigned)(a >> 32) & 0xffffu, bytes, 0x00020000u}; }
template <int N, class F> DI void unroll(F f) { if constexpr (N > 0) { unroll<N - 1>(f); f(std::integral_constant<int, N - 1>{}); } }
DI rsrc_t mkrsrc(const void* p, unsigned bytes) { return __builtin_amdgcn_make_buffer_rsrc((void*)p, 0, bytes, 0x00020000); }
DI u32x4 ld4(rsrc_t r, unsigned voff, unsigned soff) { return __builtin_amdgcn_raw_buffer_load_b128(r, voff, soff, 16); }
DI bf16x8 ld8(rsrc_t r, unsigned voff, unsigned soff) { return __builtin_bit_cast(bf16x8, __builtin_amdgcn_raw_buffer_load_b128(r, voff, soff, 16)); }
DI void st4(rsrc_t r, unsigned voff, u32x4 v) { __builtin_amdgcn_raw_buffer_store_b128(v, r, voff, 0, 16); }
DI void st2(rsrc_t r, unsigned voff, u32x2 v) { __builtin_amdgcn_raw_buffer_store_b64(v, r, voff, 0, 16); }

constexpr int P1_SLOT = 40960, P1_WZ = 16384, P1_WQ = 24576, P1_WZ1 = P1_SLOT + 8192;
constexpr int P2_GP = 24576, P2_AT = 4 * P2_GP;
constexpr int P1_CP = P2_AT + 4 * 12288, P1_SP = P1_CP + 4096, P1_PRIV = P1_SP + 4096;
constexpr int L_CNT = P1_PRIV + 8 * 512, LDS_BYTES = L_CNT + 128;
constexpr int L_TRZ = 3 * P1_SLOT, L_TRV = L_TRZ + 16384;
static_assert(LDS_BYTES <= 163840 && L_TRV + 8192 <= P1_CP, "LDS map");
enum { C_SYNC_A, C_SYNC_B, C_PUB2_A, C_PUB2_B, C_PUB1_A, C_PUB1_B, C_RDY_A, C_RDY_B, C_PUBH, C_PUB1_Z, C_CS_B, C_Z1, C_FH };
DI AS3 unsigned* lcnt(lptr L, int i) { return (AS3 unsigned*)(L + L_CNT) + i; }
DI void larrive(AS3 unsigned* c) { if ((otid() & 63) == 0) __hip_atomic_fetch_add(c, 1u, RLX_WG); }
DI void lwait4(AS3 unsigned* c, unsigned n = 4u) { while (__hip_atomic_load(c, RLX_WG) < n) __builtin_amdgcn_s_sleep(0); asm volatile("" ::: "memory"); }
#define P3SL 4
#define TPRIO 2
#define GRS 14
#define PUBT 0
#define NO_PF
#define GS0 12
#define GS1 6
#define GPRIO 0
constexpr int SPIN_CAP = 100000;

DI void role_sync(AS3 unsigned* cnt) {
    asm volatile("s_waitcnt lgkmcnt(0)" ::: "memory");
    if ((otid() & 63) == 0) __hip_atomic_fetch_add(cnt, 1u, RLX_WG);
    while (__hip_atomic_load(cnt, RLX_WG) < 4u) __builtin_amdgcn_s_sleep(1);
    asm volatile("" ::: "memory");
}
DI void publish(AS3 unsigned* cnt, unsigned n, gu64* flag, int copies, int stride, u64 tag) {
    asm volatile("s_waitcnt vmcnt(0)" ::: "memory");
    const int lane = otid() & 63;
    unsigned old = 0;
    if (lane == 0) old = __hip_atomic_fetch_add(cnt, 1u, RLX_WG);
    if (__builtin_amdgcn_readfirstlane(old) == n - 1 && lane < copies) __hip_atomic_store(flag + lane * stride, tag, RLX_AGENT);
}
DI void wait_ready(AS3 unsigned* w) { while (__hip_atomic_load(w, RLX_WG) == 0u) __builtin_amdgcn_s_sleep(1); asm volatile("" ::: "memory"); }
DI void poll2(gu64* a, gu64* b, u64 tag) {
    for (int spin = 0; spin < SPIN_CAP; ++spin) {
        const u64 va = __hip_atomic_load(a, RLX_AGENT), vb = __hip_atomic_load(b, RLX_AGENT);
        if (__all(va == tag && vb == tag)) break;
        __builtin_amdgcn_s_sleep(2);
    }
    asm volatile("" ::: "memory");
}

template <int N> DI void vm_wait() { asm volatile("s_waitcnt vmcnt(%0)" :: "i"(N) : "memory"); }
template <int N> DI void waitbar() { asm volatile("s_waitcnt vmcnt(%0) lgkmcnt(0)\n\ts_barrier" :: "i"(N) : "memory"); }
DI void lbar() { asm volatile("s_waitcnt lgkmcnt(0)\n\ts_barrier" ::: "memory"); }
struct P1Src { unsigned voA0, voA1, voZ, voQ, ldsx, ldsw; u32x4 rx, rz, rq; };
template <int KT, int J> DI void p1_piece(const P1Src& s) {
    constexpr int so = (KT % 3) * P1_SLOT;
    if constexpr (KT < 7) {
        if (J == 0) dma16<KT * 128, so>(s.voA0, s.rx, 0, s.ldsx);
        if (J == 1) dma16<KT * 128, so + 1024>(s.voA1, s.rx, 0, s.ldsx);
        if (J == 2) dma16<KT * 64 * 3584 + 1792, so + P1_WZ>(s.voZ, s.rz, 0, s.ldsw);
        if (J == 3) dma16<KT * 64 * 2688, so + P1_WQ>(s.voQ, s.rq, 0, s.ldsw);
        if (J == 4) dma16<KT * 64 * 2688 + 128, so + P1_WQ + 8192>(s.voQ, s.rq, 0, s.ldsw);
    } else if constexpr ((KT - 7) * 4 + J < 7 && J < 4) {
        constexpr int k = (KT - 7) * 4 + J;
        dma16<k * 64 * 3584, P1_WZ1 + k * 8192>(s.voZ, s.rz, 0, s.ldsw);
    }
}
template <int KT> DI void p1_stage(const P1Src& s) { p1_piece<KT, 0>(s); p1_piece<KT, 1>(s); p1_piece<KT, 2>(s); p1_piece<KT, 3>(s); p1_piece<KT, 4>(s); }
struct GW { unsigned wv0, wv1, wdst, rowg[4]; u32x4 rs;
    template <int DS, int I> DI void piece() const { dma16<(I & 1) * 8 * 4096 + DS * 128, (DS % 3) * 8192 + I * 1024>((I & 1) ? wv1 : wv0, rs, rowg[I >> 1], wdst); }
    template <int DS> DI void tile() const { piece<DS, 0>(); piece<DS, 1>(); piece<DS, 2>(); piece<DS, 3>(); piece<DS, 4>(); piece<DS, 5>(); piece<DS, 6>(); piece<DS, 7>(); } };

DI GW make_gw(const char* wsp, lptr L, int mi, int w) {
    GW g; const int lane = otid() & 63;
    const unsigned wvo = (mi * 64 + (lane >> 3)) * 4096 + w * 1024;
    g.wv0 = wvo + (((lane & 7) ^ (lane >> 4)) << 4); g.wv1 = wvo + (((lane & 7) ^ (4 + (lane >> 4))) << 4);
    g.wdst = (unsigned)(uintptr_t)L + w * P2_GP; g.rs = rawrsrc(wsp, 2048 * 4096);
#pragma unroll
    for (int k = 0; k < 4; ++k) g.rowg[k] = ((k + w) & 3) * 16 * 4096;
    return g;
}
DI void gelu16(f32x2 (&g)[8], const f32x16& acc, const f32x4 (&cs)[4], f32x2 nmu2, f32x2 A1v, f32x2 A3v, f32x2 sdv) {
    f32x2 u[8], e[8];
#pragma unroll
    for (int j = 0; j < 8; ++j) u[j] = fma2(nmu2, f32x2{cs[j >> 1][2 * (j & 1)], cs[j >> 1][2 * (j & 1) + 1]}, f32x2{acc[2 * j], acc[2 * j + 1]});
    SB();
#pragma unroll
    for (int j = 0; j < 8; ++j) e[j] = u[j] * u[j];
    SB();
#pragma unroll
    for (int j = 0; j < 8; ++j) e[j] = fma2(e[j], A3v, A1v);
    SB();
#pragma unroll
    for (int j = 0; j < 8; ++j) e[j] = e[j] * u[j];
    SB();
#pragma unroll
    for (int j = 0; j < 8; ++j) e[j] = f32x2{EX2(e[j][0]), EX2(e[j][1])};
    SB();
#pragma unroll
    for (int j = 0; j < 8; ++j) e[j] = fma2(e[j], sdv, sdv);
    SB();
#pragma unroll
    for (int j = 0; j < 8; ++j) e[j] = f32x2{__builtin_amdgcn_rcpf(e[j][0]), __builtin_amdgcn_rcpf(e[j][1])};
    SB();
#pragma unroll
    for (int j = 0; j < 8; ++j) g[j] = e[j] * u[j];
    SB();
}

struct P1Lane { int lane, r, h, m, q, p, offA[4], offW[4][3]; bf16x8 sel; AS3 float* cp; AS3 float* pv; };
DI void p1_lane(P1Lane& c, lptr L, int wid, int t0) {
    c.lane = otid() & 63; c.r = c.lane & 31; c.h = c.lane >> 5; c.m = wid & 3; c.q = (c.lane & 15) >> 2; c.p = c.lane & 3;
#pragma unroll
    for (int i = 0; i < 4; ++i) {
        const int ks = (i + wid + (wid >> 2) * 2) & 3;
        c.offA[i] = (c.m * 32 + c.r) * 128 + (((2 * ks + c.h) ^ ((c.r >> 1) & 7)) << 4);
#pragma unroll
        for (int t = 0; t < 3; ++t) c.offW[i][t] = ((t0 + t) >> 1) * 8192 + (16 * ks + 8 * c.h + c.q) * 128 + ((((t0 + t) ^ (c.q >> 1)) & 1) << 6) + 32 * ((c.lane >> 4) & 1) + 8 * c.p;
    }
    const unsigned selw = (c.lane & 15) == ((c.lane >> 4) & 1) ? 0x3f803f80u : 0u;
    c.sel = __builtin_bit_cast(bf16x8, u32x4{selw, selw, selw, selw});
    c.cp = (AS3 float*)(L + P1_CP);
    c.pv = (AS3 float*)(L + P1_PRIV + wid * 512);
}
DI bf16x8 wfrag(const P1Lane& c, lptr base, int t, int i) { return cat8(trread(base + c.offW[i][t]), trread(base + c.offW[i][t] + 512)); }
template <int NT> DI void cs_put(const P1Lane& c, int tb, const f32x4 (&cs)[NT]) {
    if ((c.lane & 15) < 2) {
#pragma unroll
        for (int t = 0; t < NT; ++t) *(AS3 f32x4*)(c.cp + c.m * 256 + (tb + t) * 32 + (c.lane & 15) * 16 + (c.lane >> 4) * 4) = cs[t];
    }
}
DI void gelu16s(unsigned (&o)[8], const f32x16& acc, const f32x4 (&cs)[4], float nmu, float A1, float A3, float sd) {
    float u[16], e[16];
#pragma unroll
    for (int j = 0; j < 16; ++j) u[j] = sfma(nmu, cs[j >> 2][j & 3], acc[j]);
#pragma unroll
    for (int j = 0; j < 16; ++j) e[j] = smul(u[j], u[j]);
#pragma unroll
    for (int j = 0; j < 16; ++j) e[j] = sfma(e[j], A3, A1);
#pragma unroll
    for (int j = 0; j < 16; ++j) e[j] = EX2(smul(e[j], u[j]));
    NOP1();
#pragma unroll
    for (int j = 0; j < 16; ++j) e[j] = __builtin_amdgcn_rcpf(sfma(e[j], sd, sd));
    NOP1();
#pragma unroll
    for (int j = 0; j < 8; ++j) o[j] = pk2(smul(e[2 * j], u[2 * j]), smul(e[2 * j + 1], u[2 * j + 1]));
    SB();
}
template <int ROLE, int NT, int NX>
DI void p1_stream(const P1Src& src, const P1Lane& c, lptr L, int wid, bf16x8 (&xf)[NX], f32x16 (&acc)[NT], float& mu, float& var TARG) {
    constexpr int WB = ROLE ? P1_WZ : P1_WQ, TB = ROLE ? 0 : 4;
    f32x4 cs[NT]; bf16x8 w[2][NT];
#pragma unroll
    for (int t = 0; t < NT; ++t) { cs[t] = f32x4{0.f, 0.f, 0.f, 0.f};
#pragma unroll
        for (int i = 0; i < 16; ++i) acc[t][i] = 0.f; }
    float sx[2] = {0.f, 0.f}, sq[2] = {0.f, 0.f};
    auto stat = [&](unsigned u) { const float lo = bflo(u), hi = bfhi(u);
        sx[0] += lo; sx[1] += hi; sq[0] = __builtin_fmaf(lo, lo, sq[0]); sq[1] = __builtin_fmaf(hi, hi, sq[1]);
        PIN(sx[0]); PIN(sx[1]); PIN(sq[0]); PIN(sq[1]); };
    AS3 f32x2* sp = (AS3 f32x2*)(L + P1_SP);
    f32x2 cpart[4], st[4];
    SB();
    p1_stage<1>(src);
    waitbar<5>();
    TMARK(1);
    xf[0] = *(const AS3 bf16x8*)(L + c.offA[0]);
#pragma unroll
    for (int t = 0; t < NT; ++t) w[0][t] = wfrag(c, L + WB, t, 0);
    unroll<28>([&](auto S) { constexpr int s = S, kt = s >> 2, i = s & 3, n = s + 1, nb = ((n >> 2) % 3) * P1_SLOT;
        SB();
        if constexpr (i == 3) { if constexpr (kt < 5) waitbar<5>(); else if constexpr (kt == 5) waitbar<4>(); else lbar(); }
        if constexpr (n < 28) xf[n % NX] = *(const AS3 bf16x8*)(L + nb + c.offA[n & 3]);
        else {
#pragma unroll
            for (int j = 0; j < 4; ++j) cpart[j] = *(const AS3 f32x2*)(c.cp + 256 * j + TB * 32 + 2 * c.lane);
            st[0] = sp[c.m * 64 + c.r]; st[1] = sp[c.m * 64 + 32 + c.r]; st[2] = sp[(c.m + 4) * 64 + c.r]; st[3] = sp[(c.m + 4) * 64 + 32 + c.r];
        }
        const u32x4 ua = __builtin_bit_cast(u32x4, xf[s % NX]);
        constexpr int pk = i == 3 ? kt + 3 : kt + 2;
        constexpr bool pon = pk < 9 && (i != 2 || kt == 0);
        constexpr int pj = kt == 0 && i < 3 ? 2 * i : 2 * ((i + 1) & 3);
        unroll<NT>([&](auto T) { constexpr int t = T;
            SB();
            acc[t] = MFMA32(w[s & 1][t], xf[s % NX], acc[t]);
            if constexpr (i == 2) cs[t] = MFMA16(w[s & 1][t], c.sel, cs[t]);
            SB();
            if constexpr (n < 28) w[n & 1][t] = wfrag(c, L + nb + WB, t, n & 3);
            if constexpr (i < 2) { if constexpr (NT == 2) { stat(ua[2 * t]); stat(ua[2 * t + 1]); } else { stat(ua[t]); if constexpr (t == 2) stat(ua[3]); } }
            if constexpr (pon && t < 2 && pj + t < 5) p1_piece<pon ? pk : 0, pj + t>(src);
        });
        if constexpr (s == 25) sp[wid * 64 + c.lane] = f32x2{sx[0] + sx[1], sq[0] + sq[1]};
        if constexpr (s == 26) { SB(); cs_put<NT>(c, TB, cs); }
    });
    SB();
    TMARK(2);
    *(AS3 f32x2*)(c.pv + 2 * c.lane) = (cpart[0] + cpart[1]) + (cpart[2] + cpart[3]);
    const f32x2 stt = (st[0] + st[1]) + (st[2] + st[3]);
    mu = stt[0] * (1.0f / 448.0f); var = fmaxf(stt[1] * (1.0f / 448.0f) - mu * mu, 0.f) + 1e-5f;
}

DI void phase1_qkv(const P1Src& src, char* ws, lptr L, int mt, int cg, int wid, u64 tag TARG) {
    P1Lane c; p1_lane(c, L, wid, cg & 1);
    const int r = c.r, h = c.h, tt = mt * 4 + c.m;
    unsigned qo[3][2];
#pragma unroll
    for (int t = 0; t < 3; ++t) {
        const int T = 3 * cg + t;
        if (T >= 28) { const int cc = (T - 28) * 32 + r, head = cc / 28, d = cc - head * 28;
            qo[t][0] = WS_KVF + ((head * 64 + tt) * 4 + 2) * 1024 + (h * 32 + d) * 16; qo[t][1] = d;
        } else {
            const bool isq = T < 14; const int Tp = isq ? T : T - 14;
#pragma unroll
            for (int e = 0; e < 2; ++e) { const int U = 4 * Tp + 2 * e + h, hp = U / 7, u7 = U - 7 * hp, head = 2 * hp + (u7 > 3), uu = u7 & 3;
                qo[t][e] = (isq ? WS_QF + (head * 64 + tt) * 2048 : WS_KVF + (head * 64 + tt) * 4096) + uu * 512 + r * 16; }
        }
        PIN(qo[t][0]); PIN(qo[t][1]);
    }
    f32x16 acc[3]; bf16x8 xf[2]; float mu, var;
    p1_stream<0, 3, 2>(src, c, L, wid, xf, acc, mu, var TPASS);
    const float rs = __builtin_amdgcn_rsqf(var);
    const f32x2 nmu2 = {-mu, -mu};
    const rsrc_t rw = mkrsrc(ws, WS_BYTES);
    const lptr TV0 = L + c.m * 6144;
    f32x4 cq[3][4];
#pragma unroll
    for (int t = 0; t < 3; ++t)
#pragma unroll
        for (int gq = 0; gq < 4; ++gq) cq[t][gq] = *(const AS3 f32x4*)(c.pv + 32 * t + 8 * gq + 4 * h);
    SB();
#pragma unroll
    for (int t = 0; t < 3; ++t) {
        const int T = 3 * cg + t;
        const float a0 = rs * (T < 14 ? 0.18898223650461362f * 1.4426950408889634f : 1.0f);
        const f32x2 a2 = {a0, a0};
        unsigned o[8];
#pragma unroll
        for (int gq = 0; gq < 4; ++gq) {
            const f32x4 c4 = cq[t][gq];
            o[2 * gq] = pk2v(a2 * fma2(nmu2, f32x2{c4[0], c4[1]}, f32x2{acc[t][4 * gq], acc[t][4 * gq + 1]}));
            o[2 * gq + 1] = pk2v(a2 * fma2(nmu2, f32x2{c4[2], c4[3]}, f32x2{acc[t][4 * gq + 2], acc[t][4 * gq + 3]}));
        }
        if (T >= 28) {
#pragma unroll
            for (int gq = 0; gq < 4; ++gq) *(AS3 u32x2*)(TV0 + t * 2048 + r * 64 + ((gq ^ (r >> 1)) & 3) * 16 + 8 * h) = u32x2{o[2 * gq], o[2 * gq + 1]};
            if (qo[t][1] >= 24u) {
                const unsigned v = qo[t][1] == 24u ? 0x3f803f80u : 0u;
                st4(rw, qo[t][0] + 64, u32x4{v, v, v, v}); st4(rw, qo[t][0] + 1024 + 64, u32x4{v, v, v, v});
            }
        } else {
#pragma unroll
            for (int e = 0; e < 2; ++e) {
                const auto a = __builtin_amdgcn_permlane32_swap(o[4 * e], o[4 * e + 2], false, false), b = __builtin_amdgcn_permlane32_swap(o[4 * e + 1], o[4 * e + 3], false, false);
                const bool split = (qo[t][e] & 0x600u) == 0x600u;
                st4(rw, qo[t][e], u32x4{a[0], b[0], split ? 0x00003f80u : a[1], split ? 0u : b[1]});
                if (split) st4(rw, qo[t][e] + (T < 14 ? 64 * 2048 : 64 * 4096), u32x4{a[1], b[1], 0x00003f80u, 0u});
            }
        }
    }
    if (cg >= 9) {
        const int row = 4 * h + c.q, chunk = ((c.lane >> 4) & 1) * 2 + (c.p >> 1);
        const lptr p = TV0 + row * 64 + ((chunk ^ (row >> 1)) & 3) * 16 + 8 * (c.p & 1);
        bf16x8 v[3][2];
#pragma unroll
        for (int t = 0; t < 3; ++t)
#pragma unroll
            for (int sx = 0; sx < 2; ++sx) v[t][sx] = cat8(trread(p + t * 2048 + sx * 1024), trread(p + t * 2048 + sx * 1024 + 512));
#pragma unroll
        for (int t = 0; t < 3; ++t) if (3 * cg + t >= 28) { st4(rw, qo[t][0], __builtin_bit_cast(u32x4, v[t][0])); st4(rw, qo[t][0] + 1024, __builtin_bit_cast(u32x4, v[t][1])); }
    }
    TMARK(3);
    publish(lcnt(L, C_PUB1_A), 4, (gu64*)(ws + WS_F1) + cg * 16 + mt, 8, 224, tag);
    TMARK(4);
}

DI void phase1_z(const P1Src& src, const char* wsp, int mi, char* ws, lptr L, int mt, int cg, int wid, u64 tag TARG) {
    P1Lane c; p1_lane(c, L, wid, 0);
    const int lane = c.lane, r = c.r, h = c.h, tt = mt * 4 + c.m;
    bf16x8 xf[28]; f32x16 acc[2]; f32x2 mv;
    { float mu, var; p1_stream<1, 2, 28>(src, c, L, wid, xf, acc, mu, var TPASS); mv = f32x2{mu, var}; }
    const float rs = __builtin_amdgcn_rsqf(mv[1]);
    const f32x2 nmu2 = {-mv[0], -mv[0]};
    const float A1 = -2.3022081985f * rs, A3 = (-2.3022081985f * 0.044715f) * rs * rs * rs, sd = mv[1] * rs;
    const f32x2 A1v = {A1, A1}, A3v = {A3, A3}, sdv = {sd, sd};
    const rsrc_t rw = mkrsrc(ws, WS_BYTES);
    f32x2 gz[2][8], zs = {0.f, 0.f}, zq = {0.f, 0.f};
#pragma unroll
    for (int t = 0; t < 2; ++t) {
        f32x4 cz[4];
#pragma unroll
        for (int gq = 0; gq < 4; ++gq) cz[gq] = *(const AS3 f32x4*)(c.pv + 32 * t + 8 * gq + 4 * h);
        gelu16(gz[t], acc[t], cz, nmu2, A1v, A3v, sdv);
        f32x2 a = gz[t][0] + gz[t][1], b = gz[t][0] * gz[t][0], d = gz[t][1] * gz[t][1];
#pragma unroll
        for (int j = 2; j < 8; j += 2) { zs += gz[t][j]; a += gz[t][j + 1]; b = fma2(gz[t][j], gz[t][j], b); d = fma2(gz[t][j + 1], gz[t][j + 1], d); }
        zs += a; zq += b + d;
        SB();
    }
    const unsigned tagA = (unsigned)tag, tagB = (unsigned)(tag >> 32);
    const float ps = halfsum(zs[0] + zs[1]), pq = halfsum(zq[0] + zq[1]);
    const unsigned go = WS_ST + tt * 14 * 512;
    vm_wait<0>(); larrive(lcnt(L, C_Z1));
    if (h == 0) st4(rw, go + cg * 512 + r * 16, u32x4{__float_as_uint(ps), tagA, __float_as_uint(pq), tagB});
    SB();
    TMARK(3);
    u32x4 gr[7];
    lwait4(lcnt(L, C_Z1)); lwait4(lcnt(L, C_PUB1_A));
    {
        f32x4 cs[2]; bf16x8 w[3][2];
#pragma unroll
        for (int t = 0; t < 2; ++t) { cs[t] = f32x4{0.f, 0.f, 0.f, 0.f}; w[0][t] = wfrag(c, L + P1_WZ1, t, 0); w[1][t] = wfrag(c, L + P1_WZ1, t, 1);
#pragma unroll
            for (int i = 0; i < 16; ++i) acc[t][i] = 0.f; }
        unroll<28>([&](auto S) { constexpr int s = S, n = s + 2;
            unroll<2>([&](auto T) { constexpr int t = T;
                SB();
                acc[t] = MFMA32(w[s % 3][t], xf[s], acc[t]);
                if constexpr ((s & 3) == 0) cs[t] = MFMA16(w[s % 3][t], c.sel, cs[t]);
                SB();
                if constexpr (n < 28) w[n % 3][t] = wfrag(c, L + P1_WZ1 + (n >> 2) * 8192, t, n & 3);
            });
            if constexpr (s >= GRS && s < GRS + 7) gr[s - GRS] = ld4(rw, lane * 16 + ((s - GRS) & 3) * 1024, go + ((s - GRS) >> 2) * 4096);
            if constexpr (s == 25) { SB(); cs_put<2>(c, 2, cs); asm volatile("s_waitcnt lgkmcnt(0)" ::: "memory"); larrive(lcnt(L, C_CS_B)); }
        });
        PIN(acc[0]); PIN(acc[1]);
        SB();
    }
    __builtin_amdgcn_s_setprio(2);
    TMARK(13);
    float t1, t2;
    for (int spin = 0; spin < SPIN_CAP; ++spin) {
        bool ok = true; t1 = 0.f; t2 = 0.f;
#pragma unroll
        for (int i = 0; i < 7; ++i) { ok &= gr[i][1] == tagA && gr[i][3] == tagB; t1 += __uint_as_float(gr[i][0]); t2 += __uint_as_float(gr[i][2]); }
        if (__all(ok)) break;
        asm volatile("" ::: "memory");
#pragma unroll
        for (int i = 0; i < 7; ++i) gr[i] = ld4(rw, lane * 16 + (i & 3) * 1024, go + (i >> 2) * 4096);
    }
    t1 = halfsum(t1); t2 = halfsum(t2);
    const float zm = t1 * (1.0f / 896.0f), zv = fmaxf(t2 * (1.0f / 896.0f) - zm * zm, 0.f);
    const float zr = __builtin_amdgcn_rsqf(zv + 1e-5f);
    const f32x2 zr2 = {zr, zr}, zb2 = {-zm * zr, -zm * zr};
    const lptr T = L + L_TRZ + c.m * 4096;
    const int kq = lane >> 4;
#pragma unroll
    for (int t = 0; t < 2; ++t)
#pragma unroll
        for (int gq = 0; gq < 4; ++gq)
            *(AS3 u32x2*)(T + (2 * t + (gq >> 1)) * 1024 + r * 32 + (gq & 1) * 16 + 8 * h) = u32x2{pk2v(fma2(gz[t][2 * gq], zr2, zb2)), pk2v(fma2(gz[t][2 * gq + 1], zr2, zb2))};
#pragma unroll
    for (int i = 0; i < 4; ++i) {
        const bf16x8 v = cat8(trread(T + i * 1024 + (8 * kq + c.q) * 32 + 8 * c.p), trread(T + i * 1024 + (8 * kq + 4 + c.q) * 32 + 8 * c.p));
        const int F = 4 * cg + i, j = F / 7, f = F - 7 * j;
        st4(rw, WS_Z2N + ((j * 64 + tt) * 7 + f) * 1024 + lane * 16, __builtin_bit_cast(u32x4, v));
    }
    SB();
    lwait4(lcnt(L, C_CS_B));
    {   f32x2 cpart[4];
#pragma unroll
        for (int j = 0; j < 4; ++j) cpart[j] = *(const AS3 f32x2*)(c.cp + 256 * j + 64 + 2 * r);
        *(AS3 f32x2*)(c.pv + 2 * r) = (cpart[0] + cpart[1]) + (cpart[2] + cpart[3]); }
    u32x4 z1p[4];
#pragma unroll
    for (int t = 0; t < 2; ++t) {
        f32x4 cz[4];
#pragma unroll
        for (int gq = 0; gq < 4; ++gq) cz[gq] = *(const AS3 f32x4*)(c.pv + 32 * t + 8 * gq + 4 * h);
        unsigned g1[8];
        gelu16s(g1, acc[t], cz, -mv[0], A1, A3, sd);
        z1p[2 * t] = u32x4{g1[0], g1[1], g1[2], g1[3]}; z1p[2 * t + 1] = u32x4{g1[4], g1[5], g1[6], g1[7]};
        PIN(z1p[2 * t]); PIN(z1p[2 * t + 1]);
        SB();
        if (t == PUBT) publish(lcnt(L, C_PUB1_B), 4, (gu64*)(ws + WS_F1) + 2048 + cg * 16 + mt, 8, 224, tag);
    }
    TMARK(4);
#pragma unroll
    for (int i = 0; i < 4; ++i) st4(rw, WS_ZF + (tt * 56 + cg * 4 + i) * 1024 + lane * 16, z1p[i]);
    { const GW gw = make_gw(wsp, L, mi, c.m); gw.tile<0>(); gw.tile<1>(); }
    SB();
}

DI void helper(const char* wqkv, const char* win, const unsigned short* wo, const unsigned short* wout, const char* wsp, char* ws, lptr L, int sp, int wid, u64 tag) {
    const int lane = otid() & 63, gw = sp * 8 + wid, h = lane >> 5;
    const rsrc_t rw = mkrsrc(ws, WS_BYTES);
    for (int b = 0; b < 2; ++b) {
        u32x4 t[16];
#pragma unroll
        for (int i = 0; i < 16; ++i) t[i] = *(const u32x4*)(wsp + (size_t)((b * 16 + i) * 256 + gw) * 1024 + lane * 16);
#pragma unroll
        for (int i = 0; i < 16; ++i) asm volatile("" :: "v"(t[i]));
    }
    unsigned e[5][8];
#pragma unroll
    for (int i = 0; i < 5; ++i) {
        const int f = min(gw + 256 * i, 14 * 88 - 1), nt = f / 88, ks = f - nt * 88, col = nt * 32 + (lane & 31);
        const unsigned short* src = ks < 32 ? wo + (size_t)((ks >> 1) * 28 + (ks & 1) * 16) * 448 : wout + (size_t)(ks - 32) * 16 * 448;
        const int hi = (ks < 32 && (ks & 1)) ? 0 : 4 * h;
#pragma unroll
        for (int j = 0; j < 8; ++j) e[i][j] = src[(8 * (j >> 2) + (j < 4 ? 4 * h : hi) + (j & 3)) * 448 + col];
    }
#pragma unroll
    for (int i = 0; i < 5; ++i) {
        const int f = min(gw + 256 * i, 14 * 88 - 1), ks = f % 88;
        const bool pad = ks < 32 && (ks & 1) && h;
        st4(rw, WS_BF + f * 1024 + lane * 16, u32x4{e[i][0] | (e[i][1] << 16), e[i][2] | (e[i][3] << 16), pad ? 0u : e[i][4] | (e[i][5] << 16), pad ? 0u : e[i][6] | (e[i][7] << 16)});
    }
    publish(lcnt(L, C_PUBH), 8, (gu64*)(ws + WS_FH) + sp, 8, 32, tag);
}

DI void attn_store(char* ws, const f32x16& o, int head, int tt) {
    const int lane = otid() & 63;
    const float inv = 1.0f / hihalf(o[12]);
    const rsrc_t rw = mkrsrc(ws, WS_BYTES);
#pragma unroll
    for (int s = 0; s < 2; ++s)
        st4(rw, WS_AF + (tt * 88 + 2 * head + s) * 1024 + lane * 16,
            u32x4{pk2(smul(o[8 * s], inv), smul(o[8 * s + 1], inv)), pk2(smul(o[8 * s + 2], inv), smul(o[8 * s + 3], inv)), pk2(smul(o[8 * s + 4], inv), smul(o[8 * s + 5], inv)), pk2(smul(o[8 * s + 6], inv), smul(o[8 * s + 7], inv))});
}
DI void attn_safe(char* ws, f32x16& o, int head, int qt) {
    const int lane = otid() & 63, h = lane >> 5;
    const rsrc_t rw = mkrsrc(ws, WS_BYTES);
    const unsigned lo = lane * 16, qp = WS_QF + (head * 64 + qt) * 2048;
    const bf16x8 q0 = ld8(rw, lo, qp); u32x4 q1u = ld4(rw, lo + 1024, qp);
    if (h) { q1u[2] = 0u; q1u[3] = 0u; }
    const bf16x8 q1 = __builtin_bit_cast(bf16x8, q1u);
    f32x16 zero;
#pragma unroll
    for (int i = 0; i < 16; ++i) { o[i] = 0.f; zero[i] = 0.f; }
    float m = -3.0e38f;
#pragma unroll 1
    for (int kt = 0; kt < 64; ++kt) {
        const unsigned kp = WS_KVF + (head * 64 + kt) * 4096;
        const bf16x8 k0 = ld8(rw, lo, kp), k1 = ld8(rw, lo + 1024, kp), v0 = ld8(rw, lo + 2048, kp), v1 = ld8(rw, lo + 3072, kp);
        f32x16 s = MFMA32(k0, q0, zero); s = MFMA32(k1, q1, s);
        float mx = s[0];
#pragma unroll
        for (int i = 1; i < 16; ++i) mx = fmaxf(mx, s[i]);
        mx = fmaxf(halfmax(mx), m);
        const float f = EX2(m - mx); m = mx;
        u32x4 p0, p1;
#pragma unroll
        for (int i = 0; i < 16; ++i) { o[i] *= f; s[i] = EX2(s[i] - m); }
#pragma unroll
        for (int i = 0; i < 4; ++i) { p0[i] = pk2(s[2 * i], s[2 * i + 1]); p1[i] = pk2(s[8 + 2 * i], s[9 + 2 * i]); }
        o = MFMA32(v0, __builtin_bit_cast(bf16x8, p0), o); o = MFMA32(v1, __builtin_bit_cast(bf16x8, p1), o);
    }
}

DI void attention(char* ws, lptr L, int head, int qb, int w, int rep, u64 tag TARG) {
    const int lane = otid() & 63, h = lane >> 5;
    if (w == 0) {
        const int c = lane >> 4, cgk = (448 * (1 + (c >> 1)) + 28 * head + 27 * (c & 1)) / 96, cgq = (28 * head + 27 * (lane & 1)) / 96;
        gu64* f = (gu64*)(ws + WS_F1) + rep * 224;
        poll2(f + cgk * 16 + (lane & 15), f + cgq * 16 + qb, tag);
        if (lane == 0) __hip_atomic_store(lcnt(L, C_RDY_A), 1u, RLX_WG);
    } else wait_ready(lcnt(L, C_RDY_A));
    TMARK(5);
    const rsrc_t rw = mkrsrc(ws, WS_BYTES);
    unsigned kv = WS_KVF + head * 64 * 4096;
    const unsigned lo = lane * 16;
    bf16x8 q0[4]; u32x4 q1[4];
    bf16x8 r0 = ld8(rw, lo, kv), r1 = ld8(rw, lo + 1024, kv);
#pragma unroll
    for (int t = 0; t < 4; ++t) { const unsigned qp = WS_QF + (head * 64 + qb * 4 + ((w + t) & 3)) * 2048;
        q0[t] = ld8(rw, lo, qp); q1[t] = ld4(rw, lo + 1024, qp); }
    kv += w * 16 * 4096;
    bf16x8 ka0 = ld8(rw, lo, kv), ka1 = ld8(rw, lo + 1024, kv), va0 = ld8(rw, lo + 2048, kv), va1 = ld8(rw, lo + 3072, kv);
    bf16x8 kb0 = ld8(rw, lo, kv + 4096), kb1 = ld8(rw, lo + 1024, kv + 4096), vb0, vb1;
    f32x16 zero, o[4], sA, sB;
    u32x4 p0, p1 = {0u, 0u, 0u, 0u};
#pragma unroll
    for (int i = 0; i < 16; ++i) zero[i] = 0.f;
#pragma unroll
    for (int i = 0; i < 8; ++i) vb1[i] = 0;
#pragma unroll
    for (int t = 0; t < 4; ++t) {
        q1[t][2] = h ? 0u : q1[t][2]; q1[t][3] = h ? 0u : q1[t][3];
        o[t] = MFMA32(r0, q0[t], zero); o[t] = MFMA32(r1, __builtin_bit_cast(bf16x8, q1[t]), o[t]);
    }
#pragma unroll
    for (int t = 0; t < 4; ++t) {
        float mx = fmaxf(o[t][0], o[t][1]);
#pragma unroll
        for (int i = 2; i < 16; i += 2) mx = fmaxf(fmaxf(mx, o[t][i]), o[t][i + 1]);
        mx = halfmax(mx);
        q1[t][2] = h ? pk2(-(mx + 88.0f), 0.f) : q1[t][2];
#pragma unroll
        for (int i = 0; i < 16; ++i) o[t][i] = 0.f;
    }
#define Q1(t) __builtin_bit_cast(bf16x8, q1[t])
#define PB(x) __builtin_bit_cast(bf16x8, x)
#define ASTEP(A, B, O, V0, OP, VP, K0, K1, QN) do { \
    B = MFMA32(K0, q0[QN], zero); SB(); \
    A[0] = EX2(A[0]); A[1] = EX2(A[1]); A[2] = EX2(A[2]); A[3] = EX2(A[3]); PIN(A); SB(); \
    OP = MFMA32(VP, PB(p1), OP); SB(); \
    p0[0] = pk2(A[0], A[1]); p0[1] = pk2(A[2], A[3]); SB(); A[4] = EX2(A[4]); A[5] = EX2(A[5]); A[6] = EX2(A[6]); A[7] = EX2(A[7]); PIN(A); PIN(p0); SB(); \
    B = MFMA32(K1, Q1(QN), B); SB(); \
    p0[2] = pk2(A[4], A[5]); p0[3] = pk2(A[6], A[7]); SB(); A[8] = EX2(A[8]); A[9] = EX2(A[9]); A[10] = EX2(A[10]); A[11] = EX2(A[11]); PIN(A); PIN(p0); SB(); \
    O = MFMA32(V0, PB(p0), O); SB(); \
    A[12] = EX2(A[12]); A[13] = EX2(A[13]); A[14] = EX2(A[14]); A[15] = EX2(A[15]); SB(); \
    p1[0] = pk2(A[8], A[9]); p1[1] = pk2(A[10], A[11]); SB(); p1[2] = pk2(A[12], A[13]); SB(); p1[3] = pk2(A[14], A[15]); PIN(p1); SB(); } while (0)
#define LDK(K0, K1, off) do { K0 = ld8(rw, lo, kv + (off)); K1 = ld8(rw, lo + 1024, kv + (off)); SB(); } while (0)
#define LDV(V0, V1, off) do { V0 = ld8(rw, lo + 2048, kv + (off)); V1 = ld8(rw, lo + 3072, kv + (off)); SB(); } while (0)
#define APAIR(MORE) do { \
    ASTEP(sA, sB, o[0], va0, o[3], vb1, ka0, ka1, 1); LDV(vb0, vb1, 4096); \
    ASTEP(sB, sA, o[1], va0, o[0], va1, ka0, ka1, 2); \
    ASTEP(sA, sB, o[2], va0, o[1], va1, ka0, ka1, 3); if (MORE) LDK(ka0, ka1, 8192); \
    ASTEP(sB, sA, o[3], va0, o[2], va1, kb0, kb1, 0); \
    ASTEP(sA, sB, o[0], vb0, o[3], va1, kb0, kb1, 1); if (MORE) LDV(va0, va1, 8192); \
    ASTEP(sB, sA, o[1], vb0, o[0], vb1, kb0, kb1, 2); \
    ASTEP(sA, sB, o[2], vb0, o[1], vb1, kb0, kb1, 3); if (MORE) LDK(kb0, kb1, 12288); \
    ASTEP(sB, sA, o[3], vb0, o[2], vb1, ka0, ka1, 0); kv += 8192; } while (0)
    TMARK(6);
    sA = MFMA32(ka0, q0[0], zero); sA = MFMA32(ka1, Q1(0), sA);
    for (int it = 0; it < 7; ++it) APAIR(true);
    APAIR(false);
    o[3] = MFMA32(vb1, PB(p1), o[3]);
    TMARK(7);
    lwait4(lcnt(L, C_PUB1_B));
    const lptr P = L + P2_AT;
#pragma unroll
    for (int t = 1; t < 4; ++t)
#pragma unroll
        for (int g = 0; g < 4; ++g) *(AS3 f32x4*)(P + (w * 3 + t - 1) * 4096 + g * 1024 + lo) = f32x4{o[t][4 * g], o[t][4 * g + 1], o[t][4 * g + 2], o[t][4 * g + 3]};
    role_sync(lcnt(L, C_SYNC_A));
#pragma unroll
    for (int t = 1; t < 4; ++t)
#pragma unroll
        for (int g = 0; g < 4; ++g) { f32x4 v = *(const AS3 f32x4*)(P + (((w - t) & 3) * 3 + t - 1) * 4096 + g * 1024 + lo);
#pragma unroll
            for (int c = 0; c < 4; ++c) o[0][4 * g + c] = sadd(o[0][4 * g + c], v[c]); }
    float chk = 0.f;
#pragma unroll
    for (int i = 0; i < 16; ++i) chk = __builtin_fmaf(o[0][i], 0.f, chk);
    const float l = hihalf(o[0][12]);
    const bool bad = !(chk == 0.f) || !(l > 0x1p-96f) || !(l < 0x1p120f);
    if (__builtin_expect(__any(bad), 0)) { attn_safe(ws, o[0], head, qb * 4 + w); SB(); asm volatile("s_nop 15" : "+v"(o[0])); }
    attn_store(ws, o[0], head, qb * 4 + w);
    TMARK(8);
    publish(lcnt(L, C_PUB2_A), 4, (gu64*)(ws + WS_F2) + qb * 128 + head, 2, 64, tag);
    TMARK(9);
}

constexpr int g_extra(int s) { return s == 7 ? 1 : s == 10 ? 8 : 0; }
constexpr int g_issued_before(int s, int f) {
    int n = 14;
    for (int t = 0; t <= s; ++t) {
        n += g_extra(t);
        for (int g = 0; g < 7; ++g) {
            if (t == s && g == f) return n;
            if (t + 2 < 16) ++n;
            if (!(t & 1) && (t >> 1) + 2 < 8) n += g == 6 ? 2 : 1;
        }
    }
    return n;
}
constexpr int g_pos_z(int s, int f) { return s < 2 ? 7 * s + f + 1 : g_issued_before(s - 2, f) + 1; }
constexpr int g_pos_w(int ds) { return g_issued_before(2 * (ds - 2) + 1, 0) - g_extra(2 * (ds - 2) + 1); }
template <int N> DI void vm_wait_for(bf16x8& v) { asm volatile("s_waitcnt vmcnt(%1)" : "+v"(v) : "i"(N)); }
template <int OFF> DI void abld(bf16x8& d, unsigned voff, const u32x4& rs, unsigned soff) {
    asm volatile("buffer_load_dwordx4 %0, %1, %2, %3 offen offset:%4 sc1" : "=v"(d) : "v"(voff), "s"(rs), "s"(soff), "i"(OFF));
}
template <int OFF> DI void abld_rw(bf16x8& d, unsigned voff, const u32x4& rs, unsigned soff) {
    asm volatile("buffer_load_dwordx4 %0, %1, %2, %3 offen offset:%4 sc1" : "+v"(d) : "v"(voff), "s"(rs), "s"(soff), "i"(OFF));
}

template <int S> struct GStep {
    template <class C> static DI void run(C& c) {
        constexpr int ks = S & 1;
        if constexpr (S + 1 < 16) {
            constexpr int nds = (S + 1) >> 1;
            if constexpr (ks == 1 && nds >= 2) vm_wait<g_issued_before(S, 0) - g_extra(S) - g_pos_w(nds)>();
#pragma unroll
            for (int t = 0; t < 4; ++t) c.wf[(S + 1) & 1][t] = *(const AS3 bf16x8*)(c.W + (nds % 3) * 8192 + t * 2048 + (ks ? c.rd0 : c.rd1));
        }
        if constexpr (S == 7) asm volatile("buffer_load_dwordx2 %0, %1, %2, 0 offen sc1" : "=v"(c.flag) : "v"(c.fo), "s"(c.rw));
        if constexpr (S == 10) c.epi_loads();
        SB();
        group<0>(c); group<1>(c); group<2>(c); group<3>(c); group<4>(c); group<5>(c); group<6>(c);
        if constexpr (S + 1 < 16 && S != 9) GStep<S + 1>::run(c);
    }
    template <int F, class C> static DI void group(C& c) {
        constexpr int ds = S >> 1, ks = S & 1;
        vm_wait_for<g_issued_before(S, F) - g_pos_z(S, F)>(c.z[S & 1][F]);
#pragma unroll
        for (int t = 0; t < 4; ++t) c.acc[F][t] = MFMA16(c.z[S & 1][F], c.wf[S & 1][t], c.acc[F][t]);
        SB();
        if constexpr (S + 2 < 16) { constexpr int u = (S + 2) * 7 + F; abld_rw<(u & 3) * 1024>(c.z[S & 1][F], c.lo, c.rw, c.zsrc + (u >> 2) * 4096); }
        if constexpr (ks == 0 && ds + 2 < 8) { c.gw.template piece<ds + 2, F>(); if constexpr (F == 6) c.gw.template piece<ds + 2, 7>(); }
        if constexpr (S == 15 && F > 0) {
#pragma unroll
            for (int t = 1; t < 4; ++t) *(AS3 f32x4*)(c.W + ((t - 1) * 7 + F - 1) * 1024 + c.lo) = c.acc[F > 0 ? F - 1 : 0][t];
        }
        SB();
    }
};
struct GCtx {
    lptr W; unsigned lo, zsrc, zo, bo, fo; int rd0, rd1; GW gw; u32x4 rw, rb;
    f32x4 acc[7][4]; bf16x8 z[2][7], wf[2][4]; u32x2 z1[7], flag; unsigned braw;
    DI void epi_loads() {
        asm volatile("buffer_load_dwordx2 %0, %4, %5, 0 offen sc1\n\tbuffer_load_dwordx2 %1, %4, %5, 0 offen offset:1024 sc1\n\tbuffer_load_dwordx2 %2, %4, %5, 0 offen offset:2048 sc1\n\tbuffer_load_dwordx2 %3, %4, %5, 0 offen offset:3072 sc1"
                     : "=&v"(z1[0]), "=&v"(z1[1]), "=&v"(z1[2]), "=&v"(z1[3]) : "v"(zo), "s"(rw));
        asm volatile("buffer_load_dwordx2 %0, %4, %5, %6 offen sc1\n\tbuffer_load_dwordx2 %1, %4, %5, %6 offen offset:1024 sc1\n\tbuffer_load_dwordx2 %2, %4, %5, %6 offen offset:2048 sc1\n\tbuffer_load_ushort %3, %7, %8, 0 offen"
                     : "=&v"(z1[4]), "=&v"(z1[5]), "=&v"(z1[6]), "=&v"(braw) : "v"(zo), "s"(rw), "s"(4096u), "v"(bo), "s"(rb));
    }
};

DI void spatial(const char* wsp, const unsigned short* bsp, char* ws, lptr L, int mi, int j, int w, int rep, int z1flag, u64 tag TARG) {
    const int lane = otid() & 63, r = lane & 15, kq = lane >> 4;
    GCtx c;
    c.W = L + w * P2_GP; c.gw = make_gw(wsp, L, mi, w); c.lo = lane * 16;
    c.rw = rawrsrc(ws, WS_BYTES); c.rb = rawrsrc(bsp, 4096);
    c.zsrc = WS_Z2N + (j * 64 + w * 16) * 7 * 1024;
    c.rd0 = r * 128 + ((kq ^ (r >> 1)) << 4); c.rd1 = c.rd0 ^ 64;
    const int tok = mi * 64 + w * 16 + r, tt = mi * 2 + (w >> 1), cgp = min((7 * j) / 4 + kq, (7 * j + 6) / 4);
    c.zo = WS_ZF + (tt * 56 + 7 * j) * 1024 + ((kq & 1) * 32 + (tok & 31)) * 16 + (kq >> 1) * 8; c.bo = tok * 2;
    c.fo = lane < 32 ? WS_F1 + (4096 + min((7 * j) / 4 + (lane & 3), (7 * j + 6) / 4) * 16 + (mi >> 1)) * 8 : WS_FH + (rep * 32 + lane - 32) * 8;
#pragma unroll
    for (int a = 0; a < 7; ++a)
#pragma unroll
        for (int b = 0; b < 4; ++b) c.acc[a][b] = f32x4{0.f, 0.f, 0.f, 0.f};
    if (z1flag >= 0) publish(lcnt(L, C_PUB1_Z), 4, (gu64*)(ws + WS_F1) + 4096 + z1flag, 1, 0, tag); else vm_wait<0>();
#pragma unroll
    for (int t = 0; t < 4; ++t) c.wf[0][t] = *(const AS3 bf16x8*)(c.W + t * 2048 + c.rd0);
    if (w == 0) {
        gu64* f = (gu64*)(ws + WS_F1) + 2048 + rep * 224 + cgp * 16 + r;
        poll2(f, f, tag);
        if (lane == 0) __hip_atomic_store(lcnt(L, C_RDY_B), 1u, RLX_WG);
    } else wait_ready(lcnt(L, C_RDY_B));
    TMARK(5);
    vm_wait<0>();
#define ZP1(s, f) abld<(((s) * 7 + (f)) & 3) * 1024>(c.z[s][f], c.lo, c.rw, c.zsrc + (((s) * 7 + (f)) >> 2) * 4096)
#define ZPRO(s) ZP1(s, 0); ZP1(s, 1); ZP1(s, 2); ZP1(s, 3); ZP1(s, 4); ZP1(s, 5); ZP1(s, 6)
    ZPRO(0); ZPRO(1);
    SB();
    __builtin_amdgcn_s_setprio(GPRIO);
    TMARK(6);
    GStep<0>::run(c);
    asm volatile("" : "+v"(c.flag));
    if (__builtin_expect(!__all(c.flag[0] == (unsigned)tag && c.flag[1] == (unsigned)(tag >> 32)), 0)) { gu64* f = (gu64*)(ws + c.fo); poll2(f, f, tag); }
    if (lane == 0) __hip_atomic_store(lcnt(L, C_FH), 1u, RLX_WG);
    SB();
    GStep<10>::run(c);
    TMARK(7);
    __builtin_amdgcn_s_setprio(TPRIO);
#pragma unroll
    for (int t = 1; t < 4; ++t) *(AS3 f32x4*)(c.W + ((t - 1) * 7 + 6) * 1024 + c.lo) = c.acc[6][t];
    role_sync(lcnt(L, C_SYNC_B));
    asm volatile("s_waitcnt vmcnt(0)" : "+v"(c.z1[0]), "+v"(c.z1[1]), "+v"(c.z1[2]), "+v"(c.z1[3]), "+v"(c.z1[4]), "+v"(c.z1[5]), "+v"(c.z1[6]), "+v"(c.braw));
    const float bias = bflo(c.braw);
    u32x2 o[8];
#pragma unroll
    for (int f = 0; f < 7; ++f) {
        f32x4 g = c.acc[f][0];
#pragma unroll
        for (int t = 1; t < 4; ++t) { const f32x4 v = *(const AS3 f32x4*)(L + ((w - t) & 3) * P2_GP + ((t - 1) * 7 + f) * 1024 + c.lo);
#pragma unroll
            for (int e = 0; e < 4; ++e) g[e] = sadd(g[e], v[e]); }
        o[f] = u32x2{pk2(smul(sadd(g[0], bias), bflo(c.z1[f][0])), smul(sadd(g[1], bias), bfhi(c.z1[f][0]))), pk2(smul(sadd(g[2], bias), bflo(c.z1[f][1])), smul(sadd(g[3], bias), bfhi(c.z1[f][1])))};
    }
    o[7] = o[6];
    const rsrc_t rws = mkrsrc(ws, WS_BYTES);
    const unsigned ho = WS_AF + (tt * 88 + 32 + 7 * j) * 1024 + ((kq & 1) * 32 + (tok & 31)) * 16 + (kq >> 1) * 1024;
#pragma unroll
    for (int f = 0; f < 8; f += 2) {
        const auto a = __builtin_amdgcn_permlane32_swap(o[f][0], o[f + 1][0], false, false), b = __builtin_amdgcn_permlane32_swap(o[f][1], o[f + 1][1], false, false);
        if (f < 6 || lane < 32) st4(rws, ho + f * 1024, u32x4{a[0], b[0], a[1], b[1]});
    }
    TMARK(8);
    publish(lcnt(L, C_PUB2_B), 4, (gu64*)(ws + WS_F2) + mi * 64 + 16 + j, 1, 0, tag);
    TMARK(9);
}

constexpr int P3_ROW = 272, P3_TILE = 64 * P3_ROW;
DI void outproj(const char* x, char* out, char* ws, lptr L, int mi, int nj, int w, int rep, u64 tag TARG) {
    const int tid = otid(), lane = tid & 63, r = lane & 31, h = lane >> 5;
    const rsrc_t rw = mkrsrc(ws, WS_BYTES);
    const unsigned lo = lane * 16;
    f32x16 acc[2][2];
#pragma unroll
    for (int i = 0; i < 16; ++i) { acc[0][0][i] = 0.f; acc[0][1][i] = 0.f; acc[1][0][i] = 0.f; acc[1][1][i] = 0.f; }
    auto part = [&](auto NN, int ks0, int f0, int mask) { constexpr int N = NN;
        const unsigned bo = WS_BF + (nj * 2 * 88 + ks0) * 1024, ao = WS_AF + (mi * 2 * 88 + ks0) * 1024;
        bf16x8 a[N][2], b[N][2];
#pragma unroll
        for (int s = 0; s < N; ++s) { b[s][0] = ld8(rw, lo + (s & 3) * 1024, bo + (s >> 2) * 4096); b[s][1] = ld8(rw, lo + (s & 3) * 1024, bo + 88 * 1024 + (s >> 2) * 4096); }
        SB();
        gu64* f = (gu64*)(ws + WS_F2) + mi * 64 + f0 + (lane & mask);
        for (int spin = 0; spin < SPIN_CAP; ++spin) { if (__all(__hip_atomic_load(f, RLX_AGENT) == tag)) break; __builtin_amdgcn_s_sleep(P3SL); }
        asm volatile("" ::: "memory");
        TMARK(10);
#pragma unroll
        for (int s = 0; s < N; ++s) { a[s][0] = ld8(rw, lo + (s & 3) * 1024, ao + (s >> 2) * 4096); a[s][1] = ld8(rw, lo + (s & 3) * 1024, ao + 88 * 1024 + (s >> 2) * 4096); }
        SB();
#pragma unroll
        for (int s = 0; s < N; ++s) {
            acc[0][0] = MFMA32(b[s][0], a[s][0], acc[0][0]); acc[0][1] = MFMA32(b[s][1], a[s][0], acc[0][1]);
            acc[1][0] = MFMA32(b[s][0], a[s][1], acc[1][0]); acc[1][1] = MFMA32(b[s][1], a[s][1], acc[1][1]);
        }
        PIN(acc[0][0]); PIN(acc[0][1]); PIN(acc[1][0]); PIN(acc[1][1]);
        SB();
    };
    wait_ready(lcnt(L, C_FH));
    const u32x4 xr = *(const u32x4*)(x + (unsigned)((mi * 64 + (tid >> 3)) * 896 + nj * 128 + (tid & 7) * 16));
    if (w < 4) part(std::integral_constant<int, 8>{}, 8 * w, 4 * w, 3);
    TMARK(13);
    part(std::integral_constant<int, 7>{}, 32 + 7 * w, 16 + w, 0);
    lwait4(lcnt(L, C_PUB2_A)); lwait4(lcnt(L, C_PUB2_B));
    TMARK(11);
#pragma unroll
    for (int tm = 0; tm < 2; ++tm)
#pragma unroll
        for (int tn = 0; tn < 2; ++tn)
#pragma unroll
            for (int g = 0; g < 4; ++g)
                *(AS3 f32x4*)(L + w * P3_TILE + (tm * 32 + r) * P3_ROW + (tn * 32 + 8 * g + 4 * h) * 4) =
                    f32x4{acc[tm][tn][4 * g], acc[tm][tn][4 * g + 1], acc[tm][tn][4 * g + 2], acc[tm][tn][4 * g + 3]};
    __syncthreads();
    const int row = tid >> 3, c8 = tid & 7;
    f32x4 s0 = {0.f, 0.f, 0.f, 0.f}, s1 = s0;
#pragma unroll
    for (int p = 0; p < 8; ++p) { s0 += *(const AS3 f32x4*)(L + p * P3_TILE + row * P3_ROW + c8 * 32); s1 += *(const AS3 f32x4*)(L + p * P3_TILE + row * P3_ROW + c8 * 32 + 16); }
    const u32x4 o = {pk2(s0[0] + bflo(xr[0]), s0[1] + bfhi(xr[0])), pk2(s0[2] + bflo(xr[1]), s0[3] + bfhi(xr[1])),
                     pk2(s1[0] + bflo(xr[2]), s1[1] + bfhi(xr[2])), pk2(s1[2] + bflo(xr[3]), s1[3] + bfhi(xr[3]))};
    *(u32x4*)(out + (unsigned)((mi * 64 + row) * 896 + nj * 128 + c8 * 16)) = o;
    TMARK(12);
}

__global__ __launch_bounds__(512, 2) void fused_kernel(const char* x, const char* wqkv, const char* win, const unsigned short* wo, const unsigned short* wout,
                                                       const char* wsp, const unsigned short* bsp, char* out, char* ws) {
    extern __shared__ __attribute__((aligned(16))) char smem[];
    TDECL; TMARK(0);
    u64 pc0; unsigned clen;
    asm volatile("s_getpc_b64 %0\nLcode_begin_%=:\n\ts_mov_b32 %1, Lcode_end_mark - Lcode_begin_%=" : "=s"(pc0), "=s"(clen));
    const lptr L = (lptr)smem;
    const int bid = blockIdx.x, xcd = bid & 7, idx = bid >> 3, lane = threadIdx.x & 63;
    const int wid = __builtin_amdgcn_readfirstlane(threadIdx.x >> 6);
    const unsigned lds0 = (unsigned)(uintptr_t)L;
    const int i7 = (idx * 37) >> 8, m7 = idx - 7 * i7;
    const int mt = (xcd >> 1) * 4 + i7, cg = (xcd & 1) * 7 + m7;
    P1Src src;
    {   const unsigned rowo = (mt * 128 + 16 * wid + (lane >> 3)) * 896;
        src.voA0 = rowo + (((lane & 7) ^ (lane >> 4)) << 4); src.voA1 = rowo + 8 * 896 + (((lane & 7) ^ (4 + (lane >> 4))) << 4);
        const int kr8 = 8 * wid + (lane >> 3), c8 = ((lane & 7) ^ (((lane >> 4) & 1) << 2)) << 4;
        src.voZ = kr8 * 3584 + cg * 128 + c8; src.voQ = kr8 * 2688 + ((cg * 192) & ~127) + c8;
        src.ldsx = lds0 + wid * 2048; src.ldsw = lds0 + wid * 1024;
        src.rx = rawrsrc(x, 2048 * 896); src.rq = rawrsrc(wqkv, 448 * 2688); src.rz = rawrsrc(win, 448 * 3584);
        if (idx < 28) p1_stage<0>(src);
        SB();
    }
    unsigned epoch;
    asm volatile("s_load_dword %0, %1, 0x0\n\ts_waitcnt lgkmcnt(0)" : "=s"(epoch) : "s"(ws + WS_EP) : "memory");
    const u64 tag = (u64)(epoch * 0x9E3779B1u + 0x7F4A7C15u) | ((u64)(epoch * 0x85EBCA6Bu + 0x1B873593u) << 32);
    if (threadIdx.x < 32) *lcnt(L, threadIdx.x) = 0u;
    const int mi = (xcd >> 1) * 8 + (idx >> 2), j = (xcd & 1) * 4 + (idx & 3);
    if (idx < 28) {
        if (wid >= 4) phase1_z(src, wsp, mi, ws, L, mt, cg, wid, tag TPASS);
        else phase1_qkv(src, ws, L, mt, cg, wid, tag TPASS);
    } else {
        {
            const rsrc_t rc = mkrsrc((const void*)pc0, clen);
            u32x4 t[3];
#pragma unroll
            for (int i = 0; i < 3; ++i) t[i] = __builtin_amdgcn_raw_buffer_load_b128(rc, (i * 32 + (idx - 28) * 8 + wid) * 1024 + lane * 16, 0, 0);
#pragma unroll
            for (int i = 0; i < 3; ++i) asm volatile("" :: "v"(t[i]));
        }
        __syncthreads();
        helper(wqkv, win, wo, wout, wsp, ws, L, xcd * 4 + idx - 28, wid, tag);
        if (wid < 4) larrive(lcnt(L, C_PUB1_B));
        else {
            const GW gw = make_gw(wsp, L, mi, wid - 4); gw.tile<0>(); gw.tile<1>();
        }
    }
    if (wid < 4) attention(ws, L, 2 * xcd + (idx >> 4), idx & 15, wid, xcd, tag TPASS);
    else spatial(wsp, bsp, ws, L, mi, j, wid - 4, xcd, idx < 28 ? cg * 16 + mt : -1, tag TPASS);
    if (idx < 28) outproj(x, out, ws, L, xcd * 4 + i7, m7, wid, xcd, tag TPASS);
    else if (bid == 255 && wid == 0) {
        const int ln = otid();
        gu64* f = (gu64*)(ws + WS_F2) + (ln >> 4) * 128 + (ln & 15);
        poll2(f, f + 512, tag); poll2(f + 1024, f + 1536, tag);
        if (ln == 0) __hip_atomic_store((gu32*)(ws + WS_EP), epoch + 1u, RLX_AGENT);
    }
    TFLUSH();
    asm volatile("Lcode_end_mark:");
}

extern "C" void kernel_launch(void* const* d_in, const int* in_sizes, int n_in, void* d_out, int out_size, void* d_ws, size_t ws_size, hipStream_t stream) {
    (void)hipFuncSetAttribute((const void*)fused_kernel, hipFuncAttributeMaxDynamicSharedMemorySize, LDS_BYTES);
    fused_kernel<<<256, 512, LDS_BYTES, stream>>>((const char*)d_in[0], (const char*)d_in[1], (const char*)d_in[4], (const unsigned short*)d_in[2], (const unsigned short*)d_in[7],
                                                  (const char*)d_in[5], (const unsigned short*)d_in[6], (char*)d_out, (char*)d_ws);
}
```

```cpp
#include <hip/hip_runtime.h>
#include <hip/hip_bf16.h>
#include <stdint.h>
#include <type_traits>

typedef short bf16x8 __attribute__((ext_vector_type(8)));
typedef short s16x4 __attribute__((ext_vector_type(4)));
typedef float f32x16 __attribute__((ext_vector_type(16)));
typedef float f32x4 __attribute__((ext_vector_type(4)));
typedef float f32x2 __attribute__((ext_vector_type(2)));
typedef unsigned u32x4 __attribute__((ext_vector_type(4)));
typedef unsigned u32x2 __attribute__((ext_vector_type(2)));
typedef __bf16 bf2 __attribute__((ext_vector_type(2)));
typedef unsigned long long u64;
#define AS3 __attribute__((address_space(3)))
typedef AS3 char* lptr;
typedef __attribute__((address_space(1))) unsigned gu32;
typedef __attribute__((address_space(1))) u64 gu64;
typedef __amdgpu_buffer_rsrc_t rsrc_t;
#define DI __device__ __forceinline__
#define SB() __builtin_amdgcn_sched_barrier(0)
#define PIN(x) asm volatile("" : "+v"(x))
#define MFMA32(a, b, c) __builtin_amdgcn_mfma_f32_32x32x16_bf16((a), (b), (c), 0, 0, 0)
#define MFMA16(a, b, c) __builtin_amdgcn_mfma_f32_16x16x32_bf16((a), (b), (c), 0, 0, 0)
#define EX2(x) __builtin_amdgcn_exp2f(x)
#define RLX_AGENT __ATOMIC_RELAXED, __HIP_MEMORY_SCOPE_AGENT
#define RLX_WG __ATOMIC_RELAXED, __HIP_MEMORY_SCOPE_WORKGROUP

constexpr unsigned WS_QF = 0;
constexpr unsigned WS_KVF = 2u << 20;
constexpr unsigned WS_ZF = 6u << 20;
constexpr unsigned WS_Z2N = 10u << 20;
constexpr unsigned WS_AF = 14u << 20;
constexpr unsigned WS_BF = 20u << 20;
constexpr unsigned WS_ST = WS_BF + (3u << 19);
constexpr unsigned WS_EP = 22u << 20;
constexpr unsigned WS_F1 = WS_EP + 4096;
constexpr unsigned WS_F2 = WS_F1 + 49152 * 4;
constexpr unsigned WS_FH = WS_F2 + 65536;
constexpr unsigned WS_BYTES = 23u << 20;
#define F1P(i) ((gu64*)(ws + WS_F1) + (i) * 4)


DI int otid() { int t = threadIdx.x; asm volatile("" : "+v"(t)); return t; }
DI unsigned pk2(float a, float b) { f32x2 v = {a, b}; return __builtin_bit_cast(unsigned, __builtin_convertvector(v, bf2)); }
DI unsigned pk2v(f32x2 v) { return __builtin_bit_cast(unsigned, __builtin_convertvector(v, bf2)); }
DI float bflo(unsigned u) { return __uint_as_float(u << 16); }
DI float bfhi(unsigned u) { return __uint_as_float(u & 0xffff0000u); }
DI f32x2 fma2(f32x2 a, f32x2 b, f32x2 c) { return __builtin_elementwise_fma(a, b, c); }
DI float sadd(float a, float b) { float d; asm("v_add_f32 %0, %1, %2" : "=v"(d) : "v"(a), "v"(b)); return d; }
DI float smul(float a, float b) { float d; asm("v_mul_f32 %0, %1, %2" : "=v"(d) : "v"(a), "v"(b)); return d; }
DI float halfsum(float v) { auto r = __builtin_amdgcn_permlane32_swap(__float_as_uint(v), __float_as_uint(v), false, false); return __uint_as_float(r[0]) + __uint_as_float(r[1]); }
DI float halfmax(float v) { auto r = __builtin_amdgcn_permlane32_swap(__float_as_uint(v), __float_as_uint(v), false, false); return fmaxf(__uint_as_float(r[0]), __uint_as_float(r[1])); }
DI float hihalf(float v) { auto r = __builtin_amdgcn_permlane32_swap(__float_as_uint(v), __float_as_uint(v), false, false); return __uint_as_float(r[1]); }
DI s16x4 trread(lptr p) { return __builtin_bit_cast(s16x4, __builtin_amdgcn_ds_read_tr16_b64_v4i16((AS3 s16x4*)p)); }
DI bf16x8 cat8(s16x4 a, s16x4 b) { return __builtin_shufflevector(a, b, 0, 1, 2, 3, 4, 5, 6, 7); }
template <int GO, int LO> DI void dma16(unsigned voff, const u32x4& rs, unsigned soff, unsigned lds) {
    unsigned tmp;
    asm volatile("s_add_u32 m0, %4, %6\n\ts_add_u32 %0, %3, %5\n\tbuffer_load_dwordx4 %1, %2, %0 offen lds"
                 : "=&s"(tmp) : "v"(voff), "s"(rs), "s"(soff), "s"(lds), "i"(GO), "i"(LO) : "memory", "scc");
}
DI u32x4 rawrsrc(const void* p, unsigned bytes) { const u64 a = (u64)p; return u32x4{(unsigned)a, (unsigned)(a >> 32) & 0xffffu, bytes, 0x00020000u}; }
template <int N, class F> DI void unroll(F f) { if constexpr (N > 0) { unroll<N - 1>(f); f(std::integral_constant<int, N - 1>{}); } }
DI rsrc_t mkrsrc(const void* p, unsigned bytes) { return __builtin_amdgcn_make_buffer_rsrc((void*)p, 0, bytes, 0x00020000); }
DI u32x4 ld4(rsrc_t r, unsigned voff, unsigned soff) { return __builtin_amdgcn_raw_buffer_load_b128(r, voff, soff, 16); }
DI bf16x8 ld8(rsrc_t r, unsigned voff, unsigned soff) { return __builtin_bit_cast(bf16x8, __builtin_amdgcn_raw_buffer_load_b128(r, voff, soff, 16)); }
DI void st4(rsrc_t r, unsigned voff, u32x4 v) { __builtin_amdgcn_raw_buffer_store_b128(v, r, voff, 0, 16); }

constexpr int P1_SLOT = 40960, P1_WZ = 16384, P1_WQ = 24576, P1_WZ1 = P1_SLOT + 8192;
constexpr int P2_GP = 24576, P2_AT = 4 * P2_GP;
constexpr int P1_CP = P2_AT + 4 * 12288, P1_SP = P1_CP + 4096, P1_PRIV = P1_SP + 4096;
constexpr int L_CNT = P1_PRIV + 8 * 512, LDS_BYTES = L_CNT + 128;
constexpr int L_TRZ = 3 * P1_SLOT;
static_assert(LDS_BYTES <= 163840 && L_TRZ + 16384 <= P1_CP, "LDS map");
enum { C_SYNC_A, C_SYNC_B, C_PUB2_A, C_PUB2_B, C_PUB1_A, C_PUB1_B, C_PUBH, C_PUB1_Z, C_CS_B, C_Z1, C_FH };
DI AS3 unsigned* lcnt(lptr L, int i) { return (AS3 unsigned*)(L + L_CNT) + i; }
DI void larrive(AS3 unsigned* c) { if ((otid() & 63) == 0) __hip_atomic_fetch_add(c, 1u, RLX_WG); }
DI void lwait4(AS3 unsigned* c, unsigned n = 4u) { while (__hip_atomic_load(c, RLX_WG) < n) __builtin_amdgcn_s_sleep(0); asm volatile("" ::: "memory"); }
constexpr int SPIN_CAP = 100000;

DI void role_sync(AS3 unsigned* cnt) {
    asm volatile("s_waitcnt lgkmcnt(0)" ::: "memory");
    if ((otid() & 63) == 0) __hip_atomic_fetch_add(cnt, 1u, RLX_WG);
    while (__hip_atomic_load(cnt, RLX_WG) < 4u) __builtin_amdgcn_s_sleep(1);
    asm volatile("" ::: "memory");
}
DI void publish(AS3 unsigned* cnt, unsigned n, gu64* flag, int copies, int stride, u64 tag) {
    asm volatile("s_waitcnt vmcnt(0)" ::: "memory");
    const int lane = otid() & 63;
    unsigned old = 0;
    if (lane == 0) old = __hip_atomic_fetch_add(cnt, 1u, RLX_WG);
    if (__builtin_amdgcn_readfirstlane(old) == n - 1 && lane < copies) __hip_atomic_store(flag + lane * stride, tag, RLX_AGENT);
}
DI void wait_ready(AS3 unsigned* w) { while (__hip_atomic_load(w, RLX_WG) == 0u) __builtin_amdgcn_s_sleep(1); asm volatile("" ::: "memory"); }
DI void poll1(gu64* a, u64 tag) {
    for (int spin = 0; spin < SPIN_CAP; ++spin) {
        if (__all(__hip_atomic_load(a, RLX_AGENT) == tag)) break;
        __builtin_amdgcn_s_sleep(3);
    }
    asm volatile("" ::: "memory");
}
DI void poll2(gu64* a, gu64* b, u64 tag) {
    for (int spin = 0; spin < SPIN_CAP; ++spin) {
        const u64 va = __hip_atomic_load(a, RLX_AGENT), vb = __hip_atomic_load(b, RLX_AGENT);
        if (__all(va == tag && vb == tag)) break;
        __builtin_amdgcn_s_sleep(2);
    }
    asm volatile("" ::: "memory");
}

template <int N> DI void vm_wait() { asm volatile("s_waitcnt vmcnt(%0)" :: "i"(N) : "memory"); }
template <int N> DI void waitbar() { asm volatile("s_waitcnt vmcnt(%0) lgkmcnt(0)\n\ts_barrier" :: "i"(N) : "memory"); }
DI void lbar() { asm volatile("s_waitcnt lgkmcnt(0)\n\ts_barrier" ::: "memory"); }
struct P1Src { unsigned voA0, voA1, voZ, voQ, ldsx, ldsw; u32x4 rx, rz, rq; };
template <int KT, int J> DI void p1_piece(const P1Src& s) {
    constexpr int so = (KT % 3) * P1_SLOT;
    if constexpr (KT < 7) {
        if (J == 0) dma16<KT * 128, so>(s.voA0, s.rx, 0, s.ldsx);
        if (J == 1) dma16<KT * 128, so + 1024>(s.voA1, s.rx, 0, s.ldsx);
        if (J == 2) dma16<KT * 64 * 3584 + 1792, so + P1_WZ>(s.voZ, s.rz, 0, s.ldsw);
        if (J == 3) dma16<KT * 64 * 2688, so + P1_WQ>(s.voQ, s.rq, 0, s.ldsw);
        if (J == 4) dma16<KT * 64 * 2688 + 128, so + P1_WQ + 8192>(s.voQ, s.rq, 0, s.ldsw);
    } else if constexpr ((KT - 7) * 4 + J < 7 && J < 4) {
        constexpr int k = (KT - 7) * 4 + J;
        dma16<k * 64 * 3584, P1_WZ1 + k * 8192>(s.voZ, s.rz, 0, s.ldsw);
    }
}
template <int KT> DI void p1_stage(const P1Src& s) { p1_piece<KT, 0>(s); p1_piece<KT, 1>(s); p1_piece<KT, 2>(s); p1_piece<KT, 3>(s); p1_piece<KT, 4>(s); }
struct GW { unsigned wv0, wv1, wdst, rowg[4]; u32x4 rs;
    template <int DS, int I> DI void piece() const { dma16<(I & 1) * 8 * 4096 + DS * 128, (DS % 3) * 8192 + I * 1024>((I & 1) ? wv1 : wv0, rs, rowg[I >> 1], wdst); }
    template <int DS> DI void tile() const { piece<DS, 0>(); piece<DS, 1>(); piece<DS, 2>(); piece<DS, 3>(); piece<DS, 4>(); piece<DS, 5>(); piece<DS, 6>(); piece<DS, 7>(); } };

DI GW make_gw(const char* wsp, lptr L, int mi, int w) {
    GW g; const int lane = otid() & 63;
    const unsigned wvo = (mi * 64 + (lane >> 3)) * 4096 + w * 1024;
    g.wv0 = wvo + (((lane & 7) ^ (lane >> 4)) << 4); g.wv1 = wvo + (((lane & 7) ^ (4 + (lane >> 4))) << 4);
    g.wdst = (unsigned)(uintptr_t)L + w * P2_GP; g.rs = rawrsrc(wsp, 2048 * 4096);
#pragma unroll
    for (int k = 0; k < 4; ++k) g.rowg[k] = ((k + w) & 3) * 16 * 4096;
    return g;
}
DI void gelu16(f32x2 (&g)[8], const f32x16& acc, const f32x4 (&cs)[4], f32x2 nmu2, f32x2 A1v, f32x2 A3v, f32x2 sdv) {
    f32x2 u[8], e[8];
#pragma unroll
    for (int j = 0; j < 8; ++j) u[j] = fma2(nmu2, f32x2{cs[j >> 1][2 * (j & 1)], cs[j >> 1][2 * (j & 1) + 1]}, f32x2{acc[2 * j], acc[2 * j + 1]});
    SB();
#pragma unroll
    for (int j = 0; j < 8; ++j) e[j] = u[j] * u[j];
    SB();
#pragma unroll
    for (int j = 0; j < 8; ++j) e[j] = fma2(e[j], A3v, A1v);
    SB();
#pragma unroll
    for (int j = 0; j < 8; ++j) e[j] = e[j] * u[j];
    SB();
#pragma unroll
    for (int j = 0; j < 8; ++j) e[j] = f32x2{EX2(e[j][0]), EX2(e[j][1])};
    SB();
#pragma unroll
    for (int j = 0; j < 8; ++j) e[j] = fma2(e[j], sdv, sdv);
    SB();
#pragma unroll
    for (int j = 0; j < 8; ++j) e[j] = f32x2{__builtin_amdgcn_rcpf(e[j][0]), __builtin_amdgcn_rcpf(e[j][1])};
    SB();
#pragma unroll
    for (int j = 0; j < 8; ++j) g[j] = e[j] * u[j];
    SB();
}

struct P1Lane { int lane, r, h, m, q, p, offA[4], offW[4][3]; bf16x8 sel; AS3 float* cp; AS3 float* pv; };
DI void p1_lane(P1Lane& c, lptr L, int wid, int t0) {
    c.lane = otid() & 63; c.r = c.lane & 31; c.h = c.lane >> 5; c.m = wid & 3; c.q = (c.lane & 15) >> 2; c.p = c.lane & 3;
#pragma unroll
    for (int i = 0; i < 4; ++i) {
        const int ks = (i + wid + (wid >> 2) * 2) & 3;
        c.offA[i] = (c.m * 32 + c.r) * 128 + (((2 * ks + c.h) ^ ((c.r >> 1) & 7)) << 4);
#pragma unroll
        for (int t = 0; t < 3; ++t) c.offW[i][t] = ((t0 + t) >> 1) * 8192 + (16 * ks + 8 * c.h + c.q) * 128 + ((((t0 + t) ^ (c.q >> 1)) & 1) << 6) + 32 * ((c.lane >> 4) & 1) + 8 * c.p;
    }
    const unsigned selw = (c.lane & 15) == ((c.lane >> 4) & 1) ? 0x3f803f80u : 0u;
    c.sel = __builtin_bit_cast(bf16x8, u32x4{selw, selw, selw, selw});
    c.cp = (AS3 float*)(L + P1_CP);
    c.pv = (AS3 float*)(L + P1_PRIV + wid * 512);
}
DI bf16x8 wfrag(const P1Lane& c, lptr base, int t, int i) { return cat8(trread(base + c.offW[i][t]), trread(base + c.offW[i][t] + 512)); }
template <int NT> DI void cs_put(const P1Lane& c, int tb, const f32x4 (&cs)[NT]) {
    if ((c.lane & 15) < 2) {
#pragma unroll
        for (int t = 0; t < NT; ++t) *(AS3 f32x4*)(c.cp + c.m * 256 + (tb + t) * 32 + (c.lane & 15) * 16 + (c.lane >> 4) * 4) = cs[t];
    }
}
template <int ROLE, int NT, int NX>
DI void p1_stream(const P1Src& src, const P1Lane& c, lptr L, int wid, bf16x8 (&xf)[NX], f32x16 (&acc)[NT], float& mu, float& var) {
    constexpr int WB = ROLE ? P1_WZ : P1_WQ, TB = ROLE ? 0 : 4;
    f32x4 cs[NT]; bf16x8 w[2][NT];
#pragma unroll
    for (int t = 0; t < NT; ++t) { cs[t] = f32x4{0.f, 0.f, 0.f, 0.f};
#pragma unroll
        for (int i = 0; i < 16; ++i) acc[t][i] = 0.f; }
    float sx[2] = {0.f, 0.f}, sq[2] = {0.f, 0.f};
    auto stat = [&](unsigned u) { const float lo = bflo(u), hi = bfhi(u);
        sx[0] += lo; sx[1] += hi; sq[0] = __builtin_fmaf(lo, lo, sq[0]); sq[1] = __builtin_fmaf(hi, hi, sq[1]);
        PIN(sx[0]); PIN(sx[1]); PIN(sq[0]); PIN(sq[1]); };
    AS3 f32x2* sp = (AS3 f32x2*)(L + P1_SP);
    f32x2 cpart[4], st[4];
    SB();
    p1_stage<1>(src);
    waitbar<5>();
    xf[0] = *(const AS3 bf16x8*)(L + c.offA[0]);
#pragma unroll
    for (int t = 0; t < NT; ++t) w[0][t] = wfrag(c, L + WB, t, 0);
    unroll<28>([&](auto S) { constexpr int s = S, kt = s >> 2, i = s & 3, n = s + 1, nb = ((n >> 2) % 3) * P1_SLOT;
        SB();
        if constexpr (i == 3) { if constexpr (kt < 5) waitbar<5>(); else if constexpr (kt == 5) waitbar<4>(); else lbar(); }
        if constexpr (n < 28) xf[n % NX] = *(const AS3 bf16x8*)(L + nb + c.offA[n & 3]);
        else {
#pragma unroll
            for (int j = 0; j < 4; ++j) cpart[j] = *(const AS3 f32x2*)(c.cp + 256 * j + TB * 32 + 2 * c.lane);
            st[0] = sp[c.m * 64 + c.r]; st[1] = sp[c.m * 64 + 32 + c.r]; st[2] = sp[(c.m + 4) * 64 + c.r]; st[3] = sp[(c.m + 4) * 64 + 32 + c.r];
        }
        const u32x4 ua = __builtin_bit_cast(u32x4, xf[s % NX]);
        constexpr int pk = i == 3 ? kt + 3 : kt + 2;
        constexpr bool pon = pk < 9 && (i != 2 || kt == 0);
        constexpr int pj = kt == 0 && i < 3 ? 2 * i : 2 * ((i + 1) & 3);
        unroll<NT>([&](auto T) { constexpr int t = T;
            SB();
            acc[t] = MFMA32(w[s & 1][t], xf[s % NX], acc[t]);
            if constexpr (i == 2) cs[t] = MFMA16(w[s & 1][t], c.sel, cs[t]);
            SB();
            if constexpr (n < 28) w[n & 1][t] = wfrag(c, L + nb + WB, t, n & 3);
            if constexpr (i < 2) { if constexpr (NT == 2) { stat(ua[2 * t]); stat(ua[2 * t + 1]); } else { stat(ua[t]); if constexpr (t == 2) stat(ua[3]); } }
            if constexpr (pon && t < 2 && pj + t < 5) p1_piece<pon ? pk : 0, pj + t>(src);
        });
        if constexpr (s == 25) sp[wid * 64 + c.lane] = f32x2{sx[0] + sx[1], sq[0] + sq[1]};
        if constexpr (s == 26) { SB(); cs_put<NT>(c, TB, cs); }
    });
    SB();
    *(AS3 f32x2*)(c.pv + 2 * c.lane) = (cpart[0] + cpart[1]) + (cpart[2] + cpart[3]);
    const f32x2 stt = (st[0] + st[1]) + (st[2] + st[3]);
    mu = stt[0] * (1.0f / 448.0f); var = fmaxf(stt[1] * (1.0f / 448.0f) - mu * mu, 0.f) + 1e-5f;
}

DI void phase1_qkv(const P1Src& src, char* ws, lptr L, int mt, int cg, int wid, u64 tag) {
    P1Lane c; p1_lane(c, L, wid, cg & 1);
    const int r = c.r, h = c.h, tt = mt * 4 + c.m;
    unsigned qo[3][2];
#pragma unroll
    for (int t = 0; t < 3; ++t) {
        const int T = 3 * cg + t;
        if (T >= 28) { const int cc = (T - 28) * 32 + r, head = cc / 28, d = cc - head * 28;
            qo[t][0] = WS_KVF + ((head * 64 + tt) * 4 + 2) * 1024 + (h * 32 + d) * 16; qo[t][1] = d;
        } else {
            const bool isq = T < 14; const int Tp = isq ? T : T - 14;
#pragma unroll
            for (int e = 0; e < 2; ++e) { const int U = 4 * Tp + 2 * e + h, hp = U / 7, u7 = U - 7 * hp, head = 2 * hp + (u7 > 3), uu = u7 & 3;
                qo[t][e] = (isq ? WS_QF + (head * 64 + tt) * 2048 : WS_KVF + (head * 64 + tt) * 4096) + uu * 512 + r * 16; }
        }
        PIN(qo[t][0]); PIN(qo[t][1]);
    }
    f32x16 acc[3]; bf16x8 xf[2]; float mu, var;
    p1_stream<0, 3, 2>(src, c, L, wid, xf, acc, mu, var);
    const float rs = __builtin_amdgcn_rsqf(var);
    const f32x2 nmu2 = {-mu, -mu};
    const rsrc_t rw = mkrsrc(ws, WS_BYTES);
    const lptr TV0 = L + c.m * 6144;
    f32x4 cq[3][4];
#pragma unroll
    for (int t = 0; t < 3; ++t)
#pragma unroll
        for (int gq = 0; gq < 4; ++gq) cq[t][gq] = *(const AS3 f32x4*)(c.pv + 32 * t + 8 * gq + 4 * h);
    SB();
#pragma unroll
    for (int t = 0; t < 3; ++t) {
        const int T = 3 * cg + t;
        const float a0 = rs * (T < 14 ? 0.18898223650461362f * 1.4426950408889634f : 1.0f);
        const f32x2 a2 = {a0, a0};
        unsigned o[8];
#pragma unroll
        for (int gq = 0; gq < 4; ++gq) {
            const f32x4 c4 = cq[t][gq];
            o[2 * gq] = pk2v(a2 * fma2(nmu2, f32x2{c4[0], c4[1]}, f32x2{acc[t][4 * gq], acc[t][4 * gq + 1]}));
            o[2 * gq + 1] = pk2v(a2 * fma2(nmu2, f32x2{c4[2], c4[3]}, f32x2{acc[t][4 * gq + 2], acc[t][4 * gq + 3]}));
        }
        if (T >= 28) {
#pragma unroll
            for (int gq = 0; gq < 4; ++gq) *(AS3 u32x2*)(TV0 + t * 2048 + r * 64 + ((gq ^ (r >> 1)) & 3) * 16 + 8 * h) = u32x2{o[2 * gq], o[2 * gq + 1]};
            if (qo[t][1] >= 24u) {
                const unsigned v = qo[t][1] == 24u ? 0x3f803f80u : 0u;
                st4(rw, qo[t][0] + 64, u32x4{v, v, v, v}); st4(rw, qo[t][0] + 1024 + 64, u32x4{v, v, v, v});
            }
        } else {
#pragma unroll
            for (int e = 0; e < 2; ++e) {
                const auto a = __builtin_amdgcn_permlane32_swap(o[4 * e], o[4 * e + 2], false, false), b = __builtin_amdgcn_permlane32_swap(o[4 * e + 1], o[4 * e + 3], false, false);
                const bool split = (qo[t][e] & 0x600u) == 0x600u;
                st4(rw, qo[t][e], u32x4{a[0], b[0], split ? 0x00003f80u : a[1], split ? 0u : b[1]});
                if (split) st4(rw, qo[t][e] + (T < 14 ? 64 * 2048 : 64 * 4096), u32x4{a[1], b[1], 0x00003f80u, 0u});
            }
        }
    }
    if (cg >= 9) {
        const int row = 4 * h + c.q, chunk = ((c.lane >> 4) & 1) * 2 + (c.p >> 1);
        const lptr p = TV0 + row * 64 + ((chunk ^ (row >> 1)) & 3) * 16 + 8 * (c.p & 1);
        bf16x8 v[3][2];
#pragma unroll
        for (int t = 0; t < 3; ++t)
#pragma unroll
            for (int sx = 0; sx < 2; ++sx) v[t][sx] = cat8(trread(p + t * 2048 + sx * 1024), trread(p + t * 2048 + sx * 1024 + 512));
#pragma unroll
        for (int t = 0; t < 3; ++t) if (3 * cg + t >= 28) { st4(rw, qo[t][0], __builtin_bit_cast(u32x4, v[t][0])); st4(rw, qo[t][0] + 1024, __builtin_bit_cast(u32x4, v[t][1])); }
    }
    publish(lcnt(L, C_PUB1_A), 4, F1P(cg * 16 + mt), 8, 224 * 4, tag);
}

DI void phase1_z(const P1Src& src, const char* wsp, int mi, char* ws, lptr L, int mt, int cg, int wid, u64 tag) {
    P1Lane c; p1_lane(c, L, wid, 0);
    const int lane = c.lane, r = c.r, h = c.h, tt = mt * 4 + c.m;
    bf16x8 xf[28]; f32x16 acc[2]; f32x2 mv;
    { float mu, var; p1_stream<1, 2, 28>(src, c, L, wid, xf, acc, mu, var); mv = f32x2{mu, var}; }
    const float rs = __builtin_amdgcn_rsqf(mv[1]);
    const f32x2 nmu2 = {-mv[0], -mv[0]};
    const float A1 = -2.3022081985f * rs, A3 = (-2.3022081985f * 0.044715f) * rs * rs * rs, sd = mv[1] * rs;
    const f32x2 A1v = {A1, A1}, A3v = {A3, A3}, sdv = {sd, sd};
    const rsrc_t rw = mkrsrc(ws, WS_BYTES);
    f32x2 gz[2][8], zs = {0.f, 0.f}, zq = {0.f, 0.f};
#pragma unroll
    for (int t = 0; t < 2; ++t) {
        f32x4 cz[4];
#pragma unroll
        for (int gq = 0; gq < 4; ++gq) cz[gq] = *(const AS3 f32x4*)(c.pv + 32 * t + 8 * gq + 4 * h);
        gelu16(gz[t], acc[t], cz, nmu2, A1v, A3v, sdv);
        f32x2 a = gz[t][0] + gz[t][1], b = gz[t][0] * gz[t][0], d = gz[t][1] * gz[t][1];
#pragma unroll
        for (int j = 2; j < 8; j += 2) { zs += gz[t][j]; a += gz[t][j + 1]; b = fma2(gz[t][j], gz[t][j], b); d = fma2(gz[t][j + 1], gz[t][j + 1], d); }
        zs += a; zq += b + d;
        SB();
    }
    const unsigned tagA = (unsigned)tag, tagB = (unsigned)(tag >> 32);
    const float ps = halfsum(zs[0] + zs[1]), pq = halfsum(zq[0] + zq[1]);
    const unsigned go = WS_ST + tt * 14 * 512;
    vm_wait<0>(); larrive(lcnt(L, C_Z1));
    if (h == 0) st4(rw, go + cg * 512 + r * 16, u32x4{__float_as_uint(ps), tagA, __float_as_uint(pq), tagB});
    SB();
    u32x4 gr[7];
    lwait4(lcnt(L, C_Z1)); lwait4(lcnt(L, C_PUB1_A));
    {
        f32x4 cs[2]; bf16x8 w[3][2];
#pragma unroll
        for (int t = 0; t < 2; ++t) { cs[t] = f32x4{0.f, 0.f, 0.f, 0.f}; w[0][t] = wfrag(c, L + P1_WZ1, t, 0); w[1][t] = wfrag(c, L + P1_WZ1, t, 1);
#pragma unroll
            for (int i = 0; i < 16; ++i) acc[t][i] = 0.f; }
        unroll<28>([&](auto S) { constexpr int s = S, n = s + 2;
            unroll<2>([&](auto T) { constexpr int t = T;
                SB();
                acc[t] = MFMA32(w[s % 3][t], xf[s], acc[t]);
                if constexpr ((s & 3) == 0) cs[t] = MFMA16(w[s % 3][t], c.sel, cs[t]);
                SB();
                if constexpr (n < 28) w[n % 3][t] = wfrag(c, L + P1_WZ1 + (n >> 2) * 8192, t, n & 3);
            });
            if constexpr (s >= 14 && s < 14 + 7) gr[s - 14] = ld4(rw, lane * 16 + ((s - 14) & 3) * 1024, go + ((s - 14) >> 2) * 4096);
            if constexpr (s == 25) { SB(); cs_put<2>(c, 2, cs); asm volatile("s_waitcnt lgkmcnt(0)" ::: "memory"); larrive(lcnt(L, C_CS_B)); }
        });
        PIN(acc[0]); PIN(acc[1]);
        SB();
    }
    __builtin_amdgcn_s_setprio(2);
    float t1, t2;
    for (int spin = 0; spin < SPIN_CAP; ++spin) {
        bool ok = true; t1 = 0.f; t2 = 0.f;
#pragma unroll
        for (int i = 0; i < 7; ++i) { ok &= gr[i][1] == tagA && gr[i][3] == tagB; t1 += __uint_as_float(gr[i][0]); t2 += __uint_as_float(gr[i][2]); }
        if (__all(ok)) break;
        asm volatile("" ::: "memory");
#pragma unroll
        for (int i = 0; i < 7; ++i) gr[i] = ld4(rw, lane * 16 + (i & 3) * 1024, go + (i >> 2) * 4096);
    }
    t1 = halfsum(t1); t2 = halfsum(t2);
    const float zm = t1 * (1.0f / 896.0f), zv = fmaxf(t2 * (1.0f / 896.0f) - zm * zm, 0.f);
    const float zr = __builtin_amdgcn_rsqf(zv + 1e-5f);
    const f32x2 zr2 = {zr, zr}, zb2 = {-zm * zr, -zm * zr};
    const lptr T = L + L_TRZ + c.m * 4096;
    const int kq = lane >> 4;
#pragma unroll
    for (int t = 0; t < 2; ++t)
#pragma unroll
        for (int gq = 0; gq < 4; ++gq)
            *(AS3 u32x2*)(T + (2 * t + (gq >> 1)) * 1024 + r * 32 + (gq & 1) * 16 + 8 * h) = u32x2{pk2v(fma2(gz[t][2 * gq], zr2, zb2)), pk2v(fma2(gz[t][2 * gq + 1], zr2, zb2))};
#pragma unroll
    for (int i = 0; i < 4; ++i) {
        const bf16x8 v = cat8(trread(T + i * 1024 + (8 * kq + c.q) * 32 + 8 * c.p), trread(T + i * 1024 + (8 * kq + 4 + c.q) * 32 + 8 * c.p));
        const int F = 4 * cg + i, j = F / 7, f = F - 7 * j;
        st4(rw, WS_Z2N + ((j * 64 + tt) * 7 + f) * 1024 + lane * 16, __builtin_bit_cast(u32x4, v));
    }
    SB();
    lwait4(lcnt(L, C_CS_B));
    {   f32x2 cpart[4];
#pragma unroll
        for (int j = 0; j < 4; ++j) cpart[j] = *(const AS3 f32x2*)(c.cp + 256 * j + 64 + 2 * r);
        *(AS3 f32x2*)(c.pv + 2 * r) = (cpart[0] + cpart[1]) + (cpart[2] + cpart[3]); }
    u32x4 z1p[4];
#pragma unroll
    for (int t = 0; t < 2; ++t) {
        f32x4 cz[4];
#pragma unroll
        for (int gq = 0; gq < 4; ++gq) cz[gq] = *(const AS3 f32x4*)(c.pv + 32 * t + 8 * gq + 4 * h);
        f32x2 g1[8];
        gelu16(g1, acc[t], cz, nmu2, A1v, A3v, sdv);
        z1p[2 * t] = u32x4{pk2v(g1[0]), pk2v(g1[1]), pk2v(g1[2]), pk2v(g1[3])}; z1p[2 * t + 1] = u32x4{pk2v(g1[4]), pk2v(g1[5]), pk2v(g1[6]), pk2v(g1[7])};
        PIN(z1p[2 * t]); PIN(z1p[2 * t + 1]);
        SB();
        if (t == 0) publish(lcnt(L, C_PUB1_B), 4, F1P(2048 + cg * 16 + mt), 8, 224 * 4, tag);
    }
#pragma unroll
    for (int i = 0; i < 4; ++i) st4(rw, WS_ZF + (tt * 56 + cg * 4 + i) * 1024 + lane * 16, z1p[i]);
    { const GW gw = make_gw(wsp, L, mi, c.m); gw.tile<0>(); gw.tile<1>(); }
    SB();
}

DI void helper(const unsigned short* wo, const unsigned short* wout, const char* wsp, char* ws, lptr L, int sp, int wid, u64 tag) {
    const int lane = otid() & 63, gw = sp * 8 + wid, h = lane >> 5;
    const rsrc_t rw = mkrsrc(ws, WS_BYTES);
    for (int b = 0; b < 2; ++b) {
        u32x4 t[16];
#pragma unroll
        for (int i = 0; i < 16; ++i) t[i] = *(const u32x4*)(wsp + (size_t)((b * 16 + i) * 256 + gw) * 1024 + lane * 16);
#pragma unroll
        for (int i = 0; i < 16; ++i) asm volatile("" :: "v"(t[i]));
    }
    unsigned e[5][8];
#pragma unroll
    for (int i = 0; i < 5; ++i) {
        const int f = min(gw + 256 * i, 14 * 88 - 1), nt = f / 88, ks = f - nt * 88, col = nt * 32 + (lane & 31);
        const unsigned short* src = ks < 32 ? wo + (size_t)((ks >> 1) * 28 + (ks & 1) * 16) * 448 : wout + (size_t)(ks - 32) * 16 * 448;
        const int hi = (ks < 32 && (ks & 1)) ? 0 : 4 * h;
#pragma unroll
        for (int j = 0; j < 8; ++j) e[i][j] = src[(8 * (j >> 2) + (j < 4 ? 4 * h : hi) + (j & 3)) * 448 + col];
    }
#pragma unroll
    for (int i = 0; i < 5; ++i) {
        const int f = min(gw + 256 * i, 14 * 88 - 1), ks = f % 88;
        const bool pad = ks < 32 && (ks & 1) && h;
        st4(rw, WS_BF + f * 1024 + lane * 16, u32x4{e[i][0] | (e[i][1] << 16), e[i][2] | (e[i][3] << 16), pad ? 0u : e[i][4] | (e[i][5] << 16), pad ? 0u : e[i][6] | (e[i][7] << 16)});
    }
    publish(lcnt(L, C_PUBH), 8, (gu64*)(ws + WS_FH) + sp, 8, 32, tag);
}

DI void attn_store(char* ws, const f32x16& o, int head, int tt) {
    const int lane = otid() & 63;
    const float inv = 1.0f / hihalf(o[12]);
    const rsrc_t rw = mkrsrc(ws, WS_BYTES);
#pragma unroll
    for (int s = 0; s < 2; ++s)
        st4(rw, WS_AF + (tt * 88 + 2 * head + s) * 1024 + lane * 16,
            u32x4{pk2(smul(o[8 * s], inv), smul(o[8 * s + 1], inv)), pk2(smul(o[8 * s + 2], inv), smul(o[8 * s + 3], inv)), pk2(smul(o[8 * s + 4], inv), smul(o[8 * s + 5], inv)), pk2(smul(o[8 * s + 6], inv), smul(o[8 * s + 7], inv))});
}
DI void attn_safe(char* ws, f32x16& o, int head, int qt) {
    const int lane = otid() & 63, h = lane >> 5;
    const rsrc_t rw = mkrsrc(ws, WS_BYTES);
    const unsigned lo = lane * 16, qp = WS_QF + (head * 64 + qt) * 2048;
    const bf16x8 q0 = ld8(rw, lo, qp); u32x4 q1u = ld4(rw, lo + 1024, qp);
    if (h) { q1u[2] = 0u; q1u[3] = 0u; }
    const bf16x8 q1 = __builtin_bit_cast(bf16x8, q1u);
    f32x16 zero;
#pragma unroll
    for (int i = 0; i < 16; ++i) { o[i] = 0.f; zero[i] = 0.f; }
    float m = -3.0e38f;
#pragma unroll 1
    for (int kt = 0; kt < 64; ++kt) {
        const unsigned kp = WS_KVF + (head * 64 + kt) * 4096;
        const bf16x8 k0 = ld8(rw, lo, kp), k1 = ld8(rw, lo + 1024, kp), v0 = ld8(rw, lo + 2048, kp), v1 = ld8(rw, lo + 3072, kp);
        f32x16 s = MFMA32(k0, q0, zero); s = MFMA32(k1, q1, s);
        float mx = s[0];
#pragma unroll
        for (int i = 1; i < 16; ++i) mx = fmaxf(mx, s[i]);
        mx = fmaxf(halfmax(mx), m);
        const float f = EX2(m - mx); m = mx;
        u32x4 p0, p1;
#pragma unroll
        for (int i = 0; i < 16; ++i) { o[i] *= f; s[i] = EX2(s[i] - m); }
#pragma unroll
        for (int i = 0; i < 4; ++i) { p0[i] = pk2(s[2 * i], s[2 * i + 1]); p1[i] = pk2(s[8 + 2 * i], s[9 + 2 * i]); }
        o = MFMA32(v0, __builtin_bit_cast(bf16x8, p0), o); o = MFMA32(v1, __builtin_bit_cast(bf16x8, p1), o);
    }
}

DI void attention(char* ws, lptr L, int head, int qb, int w, int rep, u64 tag) {
    const int lane = otid() & 63, h = lane >> 5;
    {
        const int l = lane & 31, blk = l < 16 ? 1 + (l >> 3) : l < 24 ? 1 : 0, mtp = l < 16 ? 4 * w + (l & 3) : l < 24 ? 0 : qb;
        poll1(F1P(rep * 224 + ((448 * blk + 28 * head + 27 * ((l >> 2) & 1)) / 96) * 16 + mtp), tag);
    }
    const rsrc_t rw = mkrsrc(ws, WS_BYTES);
    unsigned kv = WS_KVF + head * 64 * 4096;
    const unsigned lo = lane * 16;
    bf16x8 q0[4]; u32x4 q1[4];
    bf16x8 r0 = ld8(rw, lo, kv), r1 = ld8(rw, lo + 1024, kv);
#pragma unroll
    for (int t = 0; t < 4; ++t) { const unsigned qp = WS_QF + (head * 64 + qb * 4 + ((w + t) & 3)) * 2048;
        q0[t] = ld8(rw, lo, qp); q1[t] = ld4(rw, lo + 1024, qp); }
    kv += w * 16 * 4096;
    bf16x8 ka0 = ld8(rw, lo, kv), ka1 = ld8(rw, lo + 1024, kv), va0 = ld8(rw, lo + 2048, kv), va1 = ld8(rw, lo + 3072, kv);
    bf16x8 kb0 = ld8(rw, lo, kv + 4096), kb1 = ld8(rw, lo + 1024, kv + 4096), vb0, vb1;
    f32x16 zero, o[4], sA, sB;
    u32x4 p0, p1 = {0u, 0u, 0u, 0u};
#pragma unroll
    for (int i = 0; i < 16; ++i) zero[i] = 0.f;
#pragma unroll
    for (int i = 0; i < 8; ++i) vb1[i] = 0;
#pragma unroll
    for (int t = 0; t < 4; ++t) {
        q1[t][2] = h ? 0u : q1[t][2]; q1[t][3] = h ? 0u : q1[t][3];
        o[t] = MFMA32(r0, q0[t], zero); o[t] = MFMA32(r1, __builtin_bit_cast(bf16x8, q1[t]), o[t]);
    }
#pragma unroll
    for (int t = 0; t < 4; ++t) {
        float mx = fmaxf(o[t][0], o[t][1]);
#pragma unroll
        for (int i = 2; i < 16; i += 2) mx = fmaxf(fmaxf(mx, o[t][i]), o[t][i + 1]);
        mx = halfmax(mx);
        q1[t][2] = h ? pk2(-(mx + 88.0f), 0.f) : q1[t][2];
#pragma unroll
        for (int i = 0; i < 16; ++i) o[t][i] = 0.f;
    }
#define Q1(t) __builtin_bit_cast(bf16x8, q1[t])
#define PB(x) __builtin_bit_cast(bf16x8, x)
#define ASTEP(A, B, O, V0, OP, VP, K0, K1, QN) do { \
    B = MFMA32(K0, q0[QN], zero); SB(); \
    A[0] = EX2(A[0]); A[1] = EX2(A[1]); A[2] = EX2(A[2]); A[3] = EX2(A[3]); PIN(A); SB(); \
    OP = MFMA32(VP, PB(p1), OP); SB(); \
    p0[0] = pk2(A[0], A[1]); p0[1] = pk2(A[2], A[3]); SB(); A[4] = EX2(A[4]); A[5] = EX2(A[5]); A[6] = EX2(A[6]); A[7] = EX2(A[7]); PIN(A); PIN(p0); SB(); \
    B = MFMA32(K1, Q1(QN), B); SB(); \
    p0[2] = pk2(A[4], A[5]); p0[3] = pk2(A[6], A[7]); SB(); A[8] = EX2(A[8]); A[9] = EX2(A[9]); A[10] = EX2(A[10]); A[11] = EX2(A[11]); PIN(A); PIN(p0); SB(); \
    O = MFMA32(V0, PB(p0), O); SB(); \
    A[12] = EX2(A[12]); A[13] = EX2(A[13]); A[14] = EX2(A[14]); A[15] = EX2(A[15]); SB(); \
    p1[0] = pk2(A[8], A[9]); p1[1] = pk2(A[10], A[11]); SB(); p1[2] = pk2(A[12], A[13]); SB(); p1[3] = pk2(A[14], A[15]); PIN(p1); SB(); } while (0)
#define LDK(K0, K1, off) do { K0 = ld8(rw, lo, kv + (off)); K1 = ld8(rw, lo + 1024, kv + (off)); SB(); } while (0)
#define LDV(V0, V1, off) do { V0 = ld8(rw, lo + 2048, kv + (off)); V1 = ld8(rw, lo + 3072, kv + (off)); SB(); } while (0)
#define APAIR(MORE) do { \
    ASTEP(sA, sB, o[0], va0, o[3], vb1, ka0, ka1, 1); LDV(vb0, vb1, 4096); \
    ASTEP(sB, sA, o[1], va0, o[0], va1, ka0, ka1, 2); \
    ASTEP(sA, sB, o[2], va0, o[1], va1, ka0, ka1, 3); if (MORE) LDK(ka0, ka1, 8192); \
    ASTEP(sB, sA, o[3], va0, o[2], va1, kb0, kb1, 0); \
    ASTEP(sA, sB, o[0], vb0, o[3], va1, kb0, kb1, 1); if (MORE) LDV(va0, va1, 8192); \
    ASTEP(sB, sA, o[1], vb0, o[0], vb1, kb0, kb1, 2); \
    ASTEP(sA, sB, o[2], vb0, o[1], vb1, kb0, kb1, 3); if (MORE) LDK(kb0, kb1, 12288); \
    ASTEP(sB, sA, o[3], vb0, o[2], vb1, ka0, ka1, 0); kv += 8192; } while (0)
    sA = MFMA32(ka0, q0[0], zero); sA = MFMA32(ka1, Q1(0), sA);
    for (int it = 0; it < 7; ++it) APAIR(true);
    APAIR(false);
    o[3] = MFMA32(vb1, PB(p1), o[3]);
    lwait4(lcnt(L, C_PUB1_B));
    const lptr P = L + P2_AT;
#pragma unroll
    for (int t = 1; t < 4; ++t)
#pragma unroll
        for (int g = 0; g < 4; ++g) *(AS3 f32x4*)(P + (w * 3 + t - 1) * 4096 + g * 1024 + lo) = f32x4{o[t][4 * g], o[t][4 * g + 1], o[t][4 * g + 2], o[t][4 * g + 3]};
    role_sync(lcnt(L, C_SYNC_A));
#pragma unroll
    for (int t = 1; t < 4; ++t)
#pragma unroll
        for (int g = 0; g < 4; ++g) { f32x4 v = *(const AS3 f32x4*)(P + (((w - t) & 3) * 3 + t - 1) * 4096 + g * 1024 + lo);
#pragma unroll
            for (int c = 0; c < 4; ++c) o[0][4 * g + c] = sadd(o[0][4 * g + c], v[c]); }
    float chk = 0.f;
#pragma unroll
    for (int i = 0; i < 16; ++i) chk = __builtin_fmaf(o[0][i], 0.f, chk);
    float big = fmaxf(fabsf(o[0][0]), fabsf(o[0][1]));
#pragma unroll
    for (int i = 2; i < 12; i += 2) big = fmaxf(fmaxf(big, fabsf(o[0][i])), fabsf(o[0][i + 1]));
    const float l = hihalf(o[0][12]);
    const bool bad = !(chk == 0.f) || !(l > 0x1p-96f) || !(l < 0x1p120f) || !(halfmax(big) > 0x1p-104f);
    if (__builtin_expect(__any(bad), 0)) { attn_safe(ws, o[0], head, qb * 4 + w); SB(); asm volatile("s_nop 15" : "+v"(o[0])); }
    attn_store(ws, o[0], head, qb * 4 + w);
    publish(lcnt(L, C_PUB2_A), 4, (gu64*)(ws + WS_F2) + qb * 384 + head * 4, 2, 192, tag);
}

constexpr int g_extra(int s) { return s == 7 ? 1 : s == 10 ? 8 : 0; }
constexpr int g_issued_before(int s, int f) {
    int n = 14;
    for (int t = 0; t <= s; ++t) {
        n += g_extra(t);
        for (int g = 0; g < 7; ++g) {
            if (t == s && g == f) return n;
            if (t + 2 < 16) ++n;
            if (!(t & 1) && (t >> 1) + 2 < 8) n += g == 6 ? 2 : 1;
        }
    }
    return n;
}
constexpr int g_pos_z(int s, int f) { return s < 2 ? 7 * s + f + 1 : g_issued_before(s - 2, f) + 1; }
constexpr int g_pos_w(int ds) { return g_issued_before(2 * (ds - 2) + 1, 0) - g_extra(2 * (ds - 2) + 1); }
template <int N> DI void vm_wait_for(bf16x8& v) { asm volatile("s_waitcnt vmcnt(%1)" : "+v"(v) : "i"(N)); }
template <int OFF> DI void abld(bf16x8& d, unsigned voff, const u32x4& rs, unsigned soff) {
    asm volatile("buffer_load_dwordx4 %0, %1, %2, %3 offen offset:%4 sc1" : "=v"(d) : "v"(voff), "s"(rs), "s"(soff), "i"(OFF));
}
template <int OFF> DI void abld_rw(bf16x8& d, unsigned voff, const u32x4& rs, unsigned soff) {
    asm volatile("buffer_load_dwordx4 %0, %1, %2, %3 offen offset:%4 sc1" : "+v"(d) : "v"(voff), "s"(rs), "s"(soff), "i"(OFF));
}

template <int S> struct GStep {
    template <class C> static DI void run(C& c) {
        constexpr int ks = S & 1;
        if constexpr (S + 1 < 16) {
            constexpr int nds = (S + 1) >> 1;
            if constexpr (ks == 1 && nds >= 2) vm_wait<g_issued_before(S, 0) - g_extra(S) - g_pos_w(nds)>();
#pragma unroll
            for (int t = 0; t < 4; ++t) c.wf[(S + 1) & 1][t] = *(const AS3 bf16x8*)(c.W + (nds % 3) * 8192 + t * 2048 + (ks ? c.rd0 : c.rd1));
        }
        if constexpr (S == 7) asm volatile("buffer_load_dwordx2 %0, %1, %2, 0 offen sc1" : "=v"(c.flag) : "v"(c.fo), "s"(c.rw));
        if constexpr (S == 10) c.epi_loads();
        SB();
        group<0>(c); group<1>(c); group<2>(c); group<3>(c); group<4>(c); group<5>(c); group<6>(c);
        if constexpr (S + 1 < 16 && S != 9) GStep<S + 1>::run(c);
    }
    template <int F, class C> static DI void group(C& c) {
        constexpr int ds = S >> 1, ks = S & 1;
        vm_wait_for<g_issued_before(S, F) - g_pos_z(S, F)>(c.z[S & 1][F]);
#pragma unroll
        for (int t = 0; t < 4; ++t) c.acc[F][t] = MFMA16(c.z[S & 1][F], c.wf[S & 1][t], c.acc[F][t]);
        SB();
        if constexpr (S + 2 < 16) { constexpr int u = (S + 2) * 7 + F; abld_rw<(u & 3) * 1024>(c.z[S & 1][F], c.lo, c.rw, c.zsrc + (u >> 2) * 4096); }
        if constexpr (ks == 0 && ds + 2 < 8) { c.gw.template piece<ds + 2, F>(); if constexpr (F == 6) c.gw.template piece<ds + 2, 7>(); }
        if constexpr (S == 15 && F > 0) {
#pragma unroll
            for (int t = 1; t < 4; ++t) *(AS3 f32x4*)(c.W + ((t - 1) * 7 + F - 1) * 1024 + c.lo) = c.acc[F > 0 ? F - 1 : 0][t];
        }
        SB();
    }
};
struct GCtx {
    lptr W; unsigned lo, zsrc, zo, bo, fo; int rd0, rd1; GW gw; u32x4 rw, rb;
    f32x4 acc[7][4]; bf16x8 z[2][7], wf[2][4]; u32x2 z1[7], flag; unsigned braw;
    DI void epi_loads() {
        asm volatile("buffer_load_dwordx2 %0, %4, %5, 0 offen sc1\n\tbuffer_load_dwordx2 %1, %4, %5, 0 offen offset:1024 sc1\n\tbuffer_load_dwordx2 %2, %4, %5, 0 offen offset:2048 sc1\n\tbuffer_load_dwordx2 %3, %4, %5, 0 offen offset:3072 sc1"
                     : "=&v"(z1[0]), "=&v"(z1[1]), "=&v"(z1[2]), "=&v"(z1[3]) : "v"(zo), "s"(rw));
        asm volatile("buffer_load_dwordx2 %0, %4, %5, %6 offen sc1\n\tbuffer_load_dwordx2 %1, %4, %5, %6 offen offset:1024 sc1\n\tbuffer_load_dwordx2 %2, %4, %5, %6 offen offset:2048 sc1\n\tbuffer_load_ushort %3, %7, %8, 0 offen"
                     : "=&v"(z1[4]), "=&v"(z1[5]), "=&v"(z1[6]), "=&v"(braw) : "v"(zo), "s"(rw), "s"(4096u), "v"(bo), "s"(rb));
    }
};

DI void spatial(const char* wsp, const unsigned short* bsp, char* ws, lptr L, int mi, int j, int w, int rep, int z1flag, u64 tag) {
    const int lane = otid() & 63, r = lane & 15, kq = lane >> 4;
    GCtx c;
    c.W = L + w * P2_GP; c.gw = make_gw(wsp, L, mi, w); c.lo = lane * 16;
    c.rw = rawrsrc(ws, WS_BYTES); c.rb = rawrsrc(bsp, 4096);
    c.zsrc = WS_Z2N + (j * 64 + w * 16) * 7 * 1024;
    c.rd0 = r * 128 + ((kq ^ (r >> 1)) << 4); c.rd1 = c.rd0 ^ 64;
    const int tok = mi * 64 + w * 16 + r, tt = mi * 2 + (w >> 1), cgp = min((7 * j) / 4 + kq, (7 * j + 6) / 4);
    c.zo = WS_ZF + (tt * 56 + 7 * j) * 1024 + ((kq & 1) * 32 + (tok & 31)) * 16 + (kq >> 1) * 8; c.bo = tok * 2;
    c.fo = lane < 32 ? WS_F1 + (4096 + min((7 * j) / 4 + (lane & 3), (7 * j + 6) / 4) * 16 + (mi >> 1)) * 8 * 4 : WS_FH + (rep * 32 + lane - 32) * 8;
#pragma unroll
    for (int a = 0; a < 7; ++a)
#pragma unroll
        for (int b = 0; b < 4; ++b) c.acc[a][b] = f32x4{0.f, 0.f, 0.f, 0.f};
    if (z1flag >= 0) publish(lcnt(L, C_PUB1_Z), 4, F1P(4096 + z1flag), 1, 0, tag); else vm_wait<0>();
#pragma unroll
    for (int t = 0; t < 4; ++t) c.wf[0][t] = *(const AS3 bf16x8*)(c.W + t * 2048 + c.rd0);
    poll1(F1P(2048 + rep * 224 + cgp * 16 + 4 * w + (r & 3)), tag);
    vm_wait<0>();
#define ZP1(s, f) abld<(((s) * 7 + (f)) & 3) * 1024>(c.z[s][f], c.lo, c.rw, c.zsrc + (((s) * 7 + (f)) >> 2) * 4096)
#define ZPRO(s) ZP1(s, 0); ZP1(s, 1); ZP1(s, 2); ZP1(s, 3); ZP1(s, 4); ZP1(s, 5); ZP1(s, 6)
    ZPRO(0); ZPRO(1);
    SB();
    __builtin_amdgcn_s_setprio(0);
    GStep<0>::run(c);
    asm volatile("" : "+v"(c.flag));
    if (__builtin_expect(!__all(c.flag[0] == (unsigned)tag && c.flag[1] == (unsigned)(tag >> 32)), 0)) { gu64* f = (gu64*)(ws + c.fo); poll2(f, f, tag); }
    if (lane == 0) __hip_atomic_store(lcnt(L, C_FH), 1u, RLX_WG);
    SB();
    GStep<10>::run(c);
    __builtin_amdgcn_s_setprio(2);
#pragma unroll
    for (int t = 1; t < 4; ++t) *(AS3 f32x4*)(c.W + ((t - 1) * 7 + 6) * 1024 + c.lo) = c.acc[6][t];
    role_sync(lcnt(L, C_SYNC_B));
    asm volatile("s_waitcnt vmcnt(0)" : "+v"(c.z1[0]), "+v"(c.z1[1]), "+v"(c.z1[2]), "+v"(c.z1[3]), "+v"(c.z1[4]), "+v"(c.z1[5]), "+v"(c.z1[6]), "+v"(c.braw));
    const float bias = bflo(c.braw);
    u32x2 o[8];
#pragma unroll
    for (int f = 0; f < 7; ++f) {
        f32x4 g = c.acc[f][0];
#pragma unroll
        for (int t = 1; t < 4; ++t) { const f32x4 v = *(const AS3 f32x4*)(L + ((w - t) & 3) * P2_GP + ((t - 1) * 7 + f) * 1024 + c.lo);
#pragma unroll
            for (int e = 0; e < 4; ++e) g[e] = sadd(g[e], v[e]); }
        o[f] = u32x2{pk2(smul(sadd(g[0], bias), bflo(c.z1[f][0])), smul(sadd(g[1], bias), bfhi(c.z1[f][0]))), pk2(smul(sadd(g[2], bias), bflo(c.z1[f][1])), smul(sadd(g[3], bias), bfhi(c.z1[f][1])))};
    }
    o[7] = o[6];
    const rsrc_t rws = mkrsrc(ws, WS_BYTES);
    const unsigned ho = WS_AF + (tt * 88 + 32 + 7 * j) * 1024 + ((kq & 1) * 32 + (tok & 31)) * 16 + (kq >> 1) * 1024;
#pragma unroll
    for (int f = 0; f < 8; f += 2) {
        const auto a = __builtin_amdgcn_permlane32_swap(o[f][0], o[f + 1][0], false, false), b = __builtin_amdgcn_permlane32_swap(o[f][1], o[f + 1][1], false, false);
        if (f < 6 || lane < 32) st4(rws, ho + f * 1024, u32x4{a[0], b[0], a[1], b[1]});
    }
    publish(lcnt(L, C_PUB2_B), 4, (gu64*)(ws + WS_F2) + mi * 192 + 64 + j * 16, 1, 0, tag);
}

constexpr int P3_ROW = 272, P3_TILE = 64 * P3_ROW;
DI void outproj(const char* x, char* out, char* ws, lptr L, int mi, int nj, int w, u64 tag) {
    const int tid = otid(), lane = tid & 63, r = lane & 31, h = lane >> 5;
    const rsrc_t rw = mkrsrc(ws, WS_BYTES);
    const unsigned lo = lane * 16;
    f32x16 acc[2][2];
#pragma unroll
    for (int i = 0; i < 16; ++i) { acc[0][0][i] = 0.f; acc[0][1][i] = 0.f; acc[1][0][i] = 0.f; acc[1][1][i] = 0.f; }
    auto part = [&](auto NN, int ks0, int f0, int mask) { constexpr int N = NN;
        const unsigned bo = WS_BF + (nj * 2 * 88 + ks0) * 1024, ao = WS_AF + (mi * 2 * 88 + ks0) * 1024;
        bf16x8 a[N][2], b[N][2];
#pragma unroll
        for (int s = 0; s < N; ++s) { b[s][0] = ld8(rw, lo + (s & 3) * 1024, bo + (s >> 2) * 4096); b[s][1] = ld8(rw, lo + (s & 3) * 1024, bo + 88 * 1024 + (s >> 2) * 4096); }
        SB();
        gu64* f = (gu64*)(ws + WS_F2) + mi * 192 + f0 + (lane & mask) * 4;
        for (int spin = 0; spin < SPIN_CAP; ++spin) { if (__all(__hip_atomic_load(f, RLX_AGENT) == tag)) break; __builtin_amdgcn_s_sleep(4); }
        asm volatile("" ::: "memory");
#pragma unroll
        for (int s = 0; s < N; ++s) { a[s][0] = ld8(rw, lo + (s & 3) * 1024, ao + (s >> 2) * 4096); a[s][1] = ld8(rw, lo + (s & 3) * 1024, ao + 88 * 1024 + (s >> 2) * 4096); }
        SB();
#pragma unroll
        for (int s = 0; s < N; ++s) {
            acc[0][0] = MFMA32(b[s][0], a[s][0], acc[0][0]); acc[0][1] = MFMA32(b[s][1], a[s][0], acc[0][1]);
            acc[1][0] = MFMA32(b[s][0], a[s][1], acc[1][0]); acc[1][1] = MFMA32(b[s][1], a[s][1], acc[1][1]);
        }
        PIN(acc[0][0]); PIN(acc[0][1]); PIN(acc[1][0]); PIN(acc[1][1]);
        SB();
    };
    wait_ready(lcnt(L, C_FH));
    const u32x4 xr = *(const u32x4*)(x + (unsigned)((mi * 64 + (tid >> 3)) * 896 + nj * 128 + (tid & 7) * 16));
    if (w < 4) part(std::integral_constant<int, 8>{}, 8 * w, 16 * w, 3);
    part(std::integral_constant<int, 7>{}, 32 + 7 * w, 64 + 16 * w, 0);
    lwait4(lcnt(L, C_PUB2_A)); lwait4(lcnt(L, C_PUB2_B));
#pragma unroll
    for (int tm = 0; tm < 2; ++tm)
#pragma unroll
        for (int tn = 0; tn < 2; ++tn)
#pragma unroll
            for (int g = 0; g < 4; ++g)
                *(AS3 f32x4*)(L + w * P3_TILE + (tm * 32 + r) * P3_ROW + (tn * 32 + 8 * g + 4 * h) * 4) =
                    f32x4{acc[tm][tn][4 * g], acc[tm][tn][4 * g + 1], acc[tm][tn][4 * g + 2], acc[tm][tn][4 * g + 3]};
    __syncthreads();
    const int row = tid >> 3, c8 = tid & 7;
    f32x4 s0 = {0.f, 0.f, 0.f, 0.f}, s1 = s0;
#pragma unroll
    for (int p = 0; p < 8; ++p) { s0 += *(const AS3 f32x4*)(L + p * P3_TILE + row * P3_ROW + c8 * 32); s1 += *(const AS3 f32x4*)(L + p * P3_TILE + row * P3_ROW + c8 * 32 + 16); }
    const u32x4 o = {pk2(s0[0] + bflo(xr[0]), s0[1] + bfhi(xr[0])), pk2(s0[2] + bflo(xr[1]), s0[3] + bfhi(xr[1])),
                     pk2(s1[0] + bflo(xr[2]), s1[1] + bfhi(xr[2])), pk2(s1[2] + bflo(xr[3]), s1[3] + bfhi(xr[3]))};
    *(u32x4*)(out + (unsigned)((mi * 64 + row) * 896 + nj * 128 + c8 * 16)) = o;
}

__global__ __launch_bounds__(512, 2) void fused_kernel(const char* x, const char* wqkv, const char* win, const unsigned short* wo, const unsigned short* wout,
                                                       const char* wsp, const unsigned short* bsp, char* out, char* ws) {
    extern __shared__ __attribute__((aligned(16))) char smem[];
    u64 pc0; unsigned clen;
    asm volatile("s_getpc_b64 %0\nLcode_begin_%=:\n\ts_mov_b32 %1, Lcode_end_mark - Lcode_begin_%=" : "=s"(pc0), "=s"(clen));
    const lptr L = (lptr)smem;
    const int bid = blockIdx.x, xcd = bid & 7, idx = bid >> 3, lane = threadIdx.x & 63;
    const int wid = __builtin_amdgcn_readfirstlane(threadIdx.x >> 6);
    const unsigned lds0 = (unsigned)(uintptr_t)L;
    const int i7 = (idx * 37) >> 8, m7 = idx - 7 * i7;
    const int mt = (xcd >> 1) * 4 + i7, cg = (xcd & 1) * 7 + m7;
    P1Src src;
    {   const unsigned rowo = (mt * 128 + 16 * wid + (lane >> 3)) * 896;
        src.voA0 = rowo + (((lane & 7) ^ (lane >> 4)) << 4); src.voA1 = rowo + 8 * 896 + (((lane & 7) ^ (4 + (lane >> 4))) << 4);
        const int kr8 = 8 * wid + (lane >> 3), c8 = ((lane & 7) ^ (((lane >> 4) & 1) << 2)) << 4;
        src.voZ = kr8 * 3584 + cg * 128 + c8; src.voQ = kr8 * 2688 + ((cg * 192) & ~127) + c8;
        src.ldsx = lds0 + wid * 2048; src.ldsw = lds0 + wid * 1024;
        src.rx = rawrsrc(x, 2048 * 896); src.rq = rawrsrc(wqkv, 448 * 2688); src.rz = rawrsrc(win, 448 * 3584);
        if (idx < 28) p1_stage<0>(src);
        SB();
    }
    unsigned epoch;
    asm volatile("s_load_dword %0, %1, 0x0\n\ts_waitcnt lgkmcnt(0)" : "=s"(epoch) : "s"(ws + WS_EP) : "memory");
    const u64 tag = (u64)(epoch * 0x9E3779B1u + 0x7F4A7C15u) | ((u64)(epoch * 0x85EBCA6Bu + 0x1B873593u) << 32);
    if (threadIdx.x < 32) *lcnt(L, threadIdx.x) = 0u;
    const int mi = (xcd >> 1) * 8 + (idx >> 2), j = (xcd & 1) * 4 + (idx & 3);
    if (idx < 28) {
        if (wid >= 4) phase1_z(src, wsp, mi, ws, L, mt, cg, wid, tag);
        else phase1_qkv(src, ws, L, mt, cg, wid, tag);
    } else {
        {
            const rsrc_t rc = mkrsrc((const void*)pc0, clen);
            u32x4 t[3];
#pragma unroll
            for (int i = 0; i < 3; ++i) t[i] = __builtin_amdgcn_raw_buffer_load_b128(rc, (i * 32 + (idx - 28) * 8 + wid) * 1024 + lane * 16, 0, 0);
#pragma unroll
            for (int i = 0; i < 3; ++i) asm volatile("" :: "v"(t[i]));
        }
        __syncthreads();
        helper(wo, wout, wsp, ws, L, xcd * 4 + idx - 28, wid, tag);
        if (wid < 4) larrive(lcnt(L, C_PUB1_B));
        else {
            const GW gw = make_gw(wsp, L, mi, wid - 4); gw.tile<0>(); gw.tile<1>();
        }
    }
    if (wid < 4) attention(ws, L, 2 * xcd + (idx >> 4), idx & 15, wid, xcd, tag);
    else spatial(wsp, bsp, ws, L, mi, j, wid - 4, xcd, idx < 28 ? cg * 16 + mt : -1, tag);
    if (idx < 28) outproj(x, out, ws, L, xcd * 4 + i7, m7, wid, tag);
    else if (bid == 255 && wid == 0) {
        const int ln = otid();
        gu64* f = (gu64*)(ws + WS_F2) + (ln >> 4) * 384 + (ln & 15) * 4;
        poll2(f, f + 1536, tag); poll2(f + 3072, f + 4608, tag);
        if (ln == 0) __hip_atomic_store((gu32*)(ws + WS_EP), epoch + 1u, RLX_AGENT);
    }
    asm volatile("Lcode_end_mark:");
}

extern "C" void kernel_launch(void* const* d_in, const int* in_sizes, int n_in, void* d_out, int out_size, void* d_ws, size_t ws_size, hipStream_t stream) {
    (void)hipFuncSetAttribute((const void*)fused_kernel, hipFuncAttributeMaxDynamicSharedMemorySize, LDS_BYTES);
    fused_kernel<<<256, 512, LDS_BYTES, stream>>>((const char*)d_in[0], (const char*)d_in[1], (const char*)d_in[4], (const unsigned short*)d_in[2], (const unsigned short*)d_in[7],
                                                  (const char*)d_in[5], (const unsigned short*)d_in[6], (char*)d_out, (char*)d_ws);
}
```

```cpp
#include <hip/hip_runtime.h>
#include <hip/hip_bf16.h>
#include <stdint.h>
#include <type_traits>

typedef short bf16x8 __attribute__((ext_vector_type(8)));
typedef short s16x4 __attribute__((ext_vector_type(4)));
typedef float f32x16 __attribute__((ext_vector_type(16)));
typedef float f32x4 __attribute__((ext_vector_type(4)));
typedef float f32x2 __attribute__((ext_vector_type(2)));
typedef unsigned u32x4 __attribute__((ext_vector_type(4)));
typedef unsigned u32x2 __attribute__((ext_vector_type(2)));
typedef __bf16 bf2 __attribute__((ext_vector_type(2)));
typedef unsigned long long u64;
#define AS3 __attribute__((address_space(3)))
typedef AS3 char* lptr;
typedef __attribute__((address_space(1))) unsigned gu32;
typedef __attribute__((address_space(1))) u64 gu64;
typedef __amdgpu_buffer_rsrc_t rsrc_t;
#define DI __device__ __forceinline__
#define SB() __builtin_amdgcn_sched_barrier(0)
#define PIN(x) asm volatile("" : "+v"(x))
#define MFMA32(a, b, c) __builtin_amdgcn_mfma_f32_32x32x16_bf16((a), (b), (c), 0, 0, 0)
#define MFMA16(a, b, c) __builtin_amdgcn_mfma_f32_16x16x32_bf16((a), (b), (c), 0, 0, 0)
#define EX2(x) __builtin_amdgcn_exp2f(x)
#define RLX_AGENT __ATOMIC_RELAXED, __HIP_MEMORY_SCOPE_AGENT
#define RLX_WG __ATOMIC_RELAXED, __HIP_MEMORY_SCOPE_WORKGROUP

constexpr unsigned WS_QF = 0;
constexpr unsigned WS_KVF = 2u << 20;
constexpr unsigned WS_ZF = 6u << 20;
constexpr unsigned WS_Z2N = 10u << 20;
constexpr unsigned WS_AF = 14u << 20;
constexpr unsigned WS_BF = 20u << 20;
constexpr unsigned WS_ST = WS_BF + (3u << 19);
constexpr unsigned WS_EP = 22u << 20;
constexpr unsigned WS_F1 = WS_EP + 4096;
constexpr unsigned WS_F2 = WS_F1 + 49152 * 4;
constexpr unsigned WS_FH = WS_F2 + 65536;
constexpr unsigned WS_BYTES = 23u << 20;
#define F1P(i) ((gu64*)(ws + WS_F1) + (i) * 4)


DI int otid() { int t = threadIdx.x; asm volatile("" : "+v"(t)); return t; }
DI unsigned pk2(float a, float b) { f32x2 v = {a, b}; return __builtin_bit_cast(unsigned, __builtin_convertvector(v, bf2)); }
DI unsigned pk2v(f32x2 v) { return __builtin_bit_cast(unsigned, __builtin_convertvector(v, bf2)); }
DI float bflo(unsigned u) { return __uint_as_float(u << 16); }
DI float bfhi(unsigned u) { return __uint_as_float(u & 0xffff0000u); }
DI f32x2 fma2(f32x2 a, f32x2 b, f32x2 c) { return __builtin_elementwise_fma(a, b, c); }
DI float sadd(float a, float b) { float d; asm("v_add_f32 %0, %1, %2" : "=v"(d) : "v"(a), "v"(b)); return d; }
DI float smul(float a, float b) { float d; asm("v_mul_f32 %0, %1, %2" : "=v"(d) : "v"(a), "v"(b)); return d; }
DI float halfsum(float v) { auto r = __builtin_amdgcn_permlane32_swap(__float_as_uint(v), __float_as_uint(v), false, false); return __uint_as_float(r[0]) + __uint_as_float(r[1]); }
DI float halfmax(float v) { auto r = __builtin_amdgcn_permlane32_swap(__float_as_uint(v), __float_as_uint(v), false, false); return fmaxf(__uint_as_float(r[0]), __uint_as_float(r[1])); }
DI float hihalf(float v) { auto r = __builtin_amdgcn_permlane32_swap(__float_as_uint(v), __float_as_uint(v), false, false); return __uint_as_float(r[1]); }
DI s16x4 trread(lptr p) { return __builtin_bit_cast(s16x4, __builtin_amdgcn_ds_read_tr16_b64_v4i16((AS3 s16x4*)p)); }
DI bf16x8 cat8(s16x4 a, s16x4 b) { return __builtin_shufflevector(a, b, 0, 1, 2, 3, 4, 5, 6, 7); }
template <int GO, int LO> DI void dma16(unsigned voff, const u32x4& rs, unsigned soff, unsigned lds) {
    unsigned tmp;
    asm volatile("s_add_u32 m0, %4, %6\n\ts_add_u32 %0, %3, %5\n\tbuffer_load_dwordx4 %1, %2, %0 offen lds"
                 : "=&s"(tmp) : "v"(voff), "s"(rs), "s"(soff), "s"(lds), "i"(GO), "i"(LO) : "memory", "scc");
}
DI u32x4 rawrsrc(const void* p, unsigned bytes) { const u64 a = (u64)p; return u32x4{(unsigned)a, (unsigned)(a >> 32) & 0xffffu, bytes, 0x00020000u}; }
template <int N, class F> DI void unroll(F f) { if constexpr (N > 0) { unroll<N - 1>(f); f(std::integral_constant<int, N - 1>{}); } }
DI rsrc_t mkrsrc(const void* p, unsigned bytes) { return __builtin_amdgcn_make_buffer_rsrc((void*)p, 0, bytes, 0x00020000); }
DI u32x4 ld4(rsrc_t r, unsigned voff, unsigned soff) { return __builtin_amdgcn_raw_buffer_load_b128(r, voff, soff, 16); }
DI bf16x8 ld8(rsrc_t r, unsigned voff, unsigned soff) { return __builtin_bit_cast(bf16x8, __builtin_amdgcn_raw_buffer_load_b128(r, voff, soff, 16)); }
DI void st4(rsrc_t r, unsigned voff, u32x4 v) { __builtin_amdgcn_raw_buffer_store_b128(v, r, voff, 0, 16); }

constexpr int P1_SLOT = 40960, P1_WZ = 16384, P1_WQ = 24576, P1_WZ1 = P1_SLOT + 8192;
constexpr int P2_GP = 24576, P2_AT = 4 * P2_GP;
constexpr int P1_CP = P2_AT + 4 * 12288, P1_SP = P1_CP + 4096, P1_PRIV = P1_SP + 4096;
constexpr int L_CNT = P1_PRIV + 8 * 512, LDS_BYTES = L_CNT + 128;
constexpr int L_TRZ = 3 * P1_SLOT;
static_assert(LDS_BYTES <= 163840 && L_TRZ + 16384 <= P1_CP, "LDS map");
enum { C_SYNC_A, C_SYNC_B, C_PUB2_A, C_PUB2_B, C_PUB1_A, C_PUB1_B, C_PUBH, C_PUB1_Z, C_CS_B, C_Z1, C_FH };
DI AS3 unsigned* lcnt(lptr L, int i) { return (AS3 unsigned*)(L + L_CNT) + i; }
DI void larrive(AS3 unsigned* c) { if ((otid() & 63) == 0) __hip_atomic_fetch_add(c, 1u, RLX_WG); }
DI void lwait4(AS3 unsigned* c, unsigned n = 4u) { while (__hip_atomic_load(c, RLX_WG) < n) __builtin_amdgcn_s_sleep(0); asm volatile("" ::: "memory"); }
constexpr int SPIN_CAP = 100000;

DI void role_sync(AS3 unsigned* cnt) {
    asm volatile("s_waitcnt lgkmcnt(0)" ::: "memory");
    if ((otid() & 63) == 0) __hip_atomic_fetch_add(cnt, 1u, RLX_WG);
    while (__hip_atomic_load(cnt, RLX_WG) < 4u) __builtin_amdgcn_s_sleep(1);
    asm volatile("" ::: "memory");
}
DI void publish(AS3 unsigned* cnt, unsigned n, gu64* flag, int copies, int stride, u64 tag) {
    asm volatile("s_waitcnt vmcnt(0)" ::: "memory");
    const int lane = otid() & 63;
    unsigned old = 0;
    if (lane == 0) old = __hip_atomic_fetch_add(cnt, 1u, RLX_WG);
    if (__builtin_amdgcn_readfirstlane(old) == n - 1 && lane < copies) __hip_atomic_store(flag + lane * stride, tag, RLX_AGENT);
}
DI void wait_ready(AS3 unsigned* w) { while (__hip_atomic_load(w, RLX_WG) == 0u) __builtin_amdgcn_s_sleep(1); asm volatile("" ::: "memory"); }
DI void poll1(gu64* a, u64 tag) {
    for (int spin = 0; spin < SPIN_CAP; ++spin) {
        if (__all(__hip_atomic_load(a, RLX_AGENT) == tag)) break;
        __builtin_amdgcn_s_sleep(3);
    }
    asm volatile("" ::: "memory");
}
DI void poll2(gu64* a, gu64* b, u64 tag) {
    for (int spin = 0; spin < SPIN_CAP; ++spin) {
        const u64 va = __hip_atomic_load(a, RLX_AGENT), vb = __hip_atomic_load(b, RLX_AGENT);
        if (__all(va == tag && vb == tag)) break;
        __builtin_amdgcn_s_sleep(2);
    }
    asm volatile("" ::: "memory");
}

template <int N> DI void vm_wait() { asm volatile("s_waitcnt vmcnt(%0)" :: "i"(N) : "memory"); }
template <int N> DI void waitbar() { asm volatile("s_waitcnt vmcnt(%0) lgkmcnt(0)\n\ts_barrier" :: "i"(N) : "memory"); }
DI void lbar() { asm volatile("s_waitcnt lgkmcnt(0)\n\ts_barrier" ::: "memory"); }
struct P1Src { unsigned voA0, voA1, voZ, voQ, ldsx, ldsw; u32x4 rx, rz, rq; };
template <int KT, int J> DI void p1_piece(const P1Src& s) {
    constexpr int so = (KT % 3) * P1_SLOT;
    if constexpr (KT < 7) {
        if (J == 0) dma16<KT * 128, so>(s.voA0, s.rx, 0, s.ldsx);
        if (J == 1) dma16<KT * 128, so + 1024>(s.voA1, s.rx, 0, s.ldsx);
        if (J == 2) dma16<KT * 64 * 3584 + 1792, so + P1_WZ>(s.voZ, s.rz, 0, s.ldsw);
        if (J == 3) dma16<KT * 64 * 2688, so + P1_WQ>(s.voQ, s.rq, 0, s.ldsw);
        if (J == 4) dma16<KT * 64 * 2688 + 128, so + P1_WQ + 8192>(s.voQ, s.rq, 0, s.ldsw);
    } else if constexpr ((KT - 7) * 4 + J < 7 && J < 4) {
        constexpr int k = (KT - 7) * 4 + J;
        dma16<k * 64 * 3584, P1_WZ1 + k * 8192>(s.voZ, s.rz, 0, s.ldsw);
    }
}
template <int KT> DI void p1_stage(const P1Src& s) { p1_piece<KT, 0>(s); p1_piece<KT, 1>(s); p1_piece<KT, 2>(s); p1_piece<KT, 3>(s); p1_piece<KT, 4>(s); }
struct GW { unsigned wv0, wv1, wdst, rowg[4]; u32x4 rs;
    template <int DS, int I> DI void piece() const { dma16<(I & 1) * 8 * 4096 + DS * 128, (DS % 3) * 8192 + I * 1024>((I & 1) ? wv1 : wv0, rs, rowg[I >> 1], wdst); }
    template <int DS> DI void tile() const { piece<DS, 0>(); piece<DS, 1>(); piece<DS, 2>(); piece<DS, 3>(); piece<DS, 4>(); piece<DS, 5>(); piece<DS, 6>(); piece<DS, 7>(); } };

DI GW make_gw(const char* wsp, lptr L, int mi, int w) {
    GW g; const int lane = otid() & 63;
    const unsigned wvo = (mi * 64 + (lane >> 3)) * 4096 + w * 1024;
    g.wv0 = wvo + (((lane & 7) ^ (lane >> 4)) << 4); g.wv1 = wvo + (((lane & 7) ^ (4 + (lane >> 4))) << 4);
    g.wdst = (unsigned)(uintptr_t)L + w * P2_GP; g.rs = rawrsrc(wsp, 2048 * 4096);
#pragma unroll
    for (int k = 0; k < 4; ++k) g.rowg[k] = ((k + w) & 3) * 16 * 4096;
    return g;
}
DI void gelu16(f32x2 (&g)[8], const f32x16& acc, const f32x4 (&cs)[4], f32x2 nmu2, f32x2 A1v, f32x2 A3v, f32x2 sdv) {
    f32x2 u[8], e[8];
#pragma unroll
    for (int j = 0; j < 8; ++j) u[j] = fma2(nmu2, f32x2{cs[j >> 1][2 * (j & 1)], cs[j >> 1][2 * (j & 1) + 1]}, f32x2{acc[2 * j], acc[2 * j + 1]});
    SB();
#pragma unroll
    for (int j = 0; j < 8; ++j) e[j] = u[j] * u[j];
    SB();
#pragma unroll
    for (int j = 0; j < 8; ++j) e[j] = fma2(e[j], A3v, A1v);
    SB();
#pragma unroll
    for (int j = 0; j < 8; ++j) e[j] = e[j] * u[j];
    SB();
#pragma unroll
    for (int j = 0; j < 8; ++j) e[j] = f32x2{EX2(e[j][0]), EX2(e[j][1])};
    SB();
#pragma unroll
    for (int j = 0; j < 8; ++j) e[j] = fma2(e[j], sdv, sdv);
    SB();
#pragma unroll
    for (int j = 0; j < 8; ++j) e[j] = f32x2{__builtin_amdgcn_rcpf(e[j][0]), __builtin_amdgcn_rcpf(e[j][1])};
    SB();
#pragma unroll
    for (int j = 0; j < 8; ++j) g[j] = e[j] * u[j];
    SB();
}

struct P1Lane { int lane, r, h, m, q, p, offA[4], offW[4][3]; bf16x8 sel; AS3 float* cp; AS3 float* pv; };
DI void p1_lane(P1Lane& c, lptr L, int wid, int t0) {
    c.lane = otid() & 63; c.r = c.lane & 31; c.h = c.lane >> 5; c.m = wid & 3; c.q = (c.lane & 15) >> 2; c.p = c.lane & 3;
#pragma unroll
    for (int i = 0; i < 4; ++i) {
        const int ks = (i + wid + (wid >> 2) * 2) & 3;
        c.offA[i] = (c.m * 32 + c.r) * 128 + (((2 * ks + c.h) ^ ((c.r >> 1) & 7)) << 4);
#pragma unroll
        for (int t = 0; t < 3; ++t) c.offW[i][t] = ((t0 + t) >> 1) * 8192 + (16 * ks + 8 * c.h + c.q) * 128 + ((((t0 + t) ^ (c.q >> 1)) & 1) << 6) + 32 * ((c.lane >> 4) & 1) + 8 * c.p;
    }
    const unsigned selw = (c.lane & 15) == ((c.lane >> 4) & 1) ? 0x3f803f80u : 0u;
    c.sel = __builtin_bit_cast(bf16x8, u32x4{selw, selw, selw, selw});
    c.cp = (AS3 float*)(L + P1_CP);
    c.pv = (AS3 float*)(L + P1_PRIV + wid * 512);
}
DI bf16x8 wfrag(const P1Lane& c, lptr base, int t, int i) { return cat8(trread(base + c.offW[i][t]), trread(base + c.offW[i][t] + 512)); }
template <int NT> DI void cs_put(const P1Lane& c, int tb, const f32x4 (&cs)[NT]) {
    if ((c.lane & 15) < 2) {
#pragma unroll
        for (int t = 0; t < NT; ++t) *(AS3 f32x4*)(c.cp + c.m * 256 + (tb + t) * 32 + (c.lane & 15) * 16 + (c.lane >> 4) * 4) = cs[t];
    }
}
template <int ROLE, int NT, int NX>
DI void p1_stream(const P1Src& src, const P1Lane& c, lptr L, int wid, bf16x8 (&xf)[NX], f32x16 (&acc)[NT], float& mu, float& var) {
    constexpr int WB = ROLE ? P1_WZ : P1_WQ, TB = ROLE ? 0 : 4;
    f32x4 cs[NT]; bf16x8 w[2][NT];
#pragma unroll
    for (int t = 0; t < NT; ++t) { cs[t] = f32x4{0.f, 0.f, 0.f, 0.f};
#pragma unroll
        for (int i = 0; i < 16; ++i) acc[t][i] = 0.f; }
    float sx[2] = {0.f, 0.f}, sq[2] = {0.f, 0.f};
    auto stat = [&](unsigned u) { const float lo = bflo(u), hi = bfhi(u);
        sx[0] += lo; sx[1] += hi; sq[0] = __builtin_fmaf(lo, lo, sq[0]); sq[1] = __builtin_fmaf(hi, hi, sq[1]);
        PIN(sx[0]); PIN(sx[1]); PIN(sq[0]); PIN(sq[1]); };
    AS3 f32x2* sp = (AS3 f32x2*)(L + P1_SP);
    f32x2 cpart[4], st[4];
    SB();
    p1_stage<1>(src);
    waitbar<5>();
    xf[0] = *(const AS3 bf16x8*)(L + c.offA[0]);
#pragma unroll
    for (int t = 0; t < NT; ++t) w[0][t] = wfrag(c, L + WB, t, 0);
    unroll<28>([&](auto S) { constexpr int s = S, kt = s >> 2, i = s & 3, n = s + 1, nb = ((n >> 2) % 3) * P1_SLOT;
        SB();
        if constexpr (i == 3) { if constexpr (kt < 5) waitbar<5>(); else if constexpr (kt == 5) waitbar<4>(); else lbar(); }
        if constexpr (n < 28) xf[n % NX] = *(const AS3 bf16x8*)(L + nb + c.offA[n & 3]);
        else {
#pragma unroll
            for (int j = 0; j < 4; ++j) cpart[j] = *(const AS3 f32x2*)(c.cp + 256 * j + TB * 32 + 2 * c.lane);
            st[0] = sp[c.m * 64 + c.r]; st[1] = sp[c.m * 64 + 32 + c.r]; st[2] = sp[(c.m + 4) * 64 + c.r]; st[3] = sp[(c.m + 4) * 64 + 32 + c.r];
        }
        const u32x4 ua = __builtin_bit_cast(u32x4, xf[s % NX]);
        constexpr int pk = i == 3 ? kt + 3 : kt + 2;
        constexpr bool pon = pk < 9 && (i != 2 || kt == 0);
        constexpr int pj = kt == 0 && i < 3 ? 2 * i : 2 * ((i + 1) & 3);
        unroll<NT>([&](auto T) { constexpr int t = T;
            SB();
            acc[t] = MFMA32(w[s & 1][t], xf[s % NX], acc[t]);
            if constexpr (i == 2) cs[t] = MFMA16(w[s & 1][t], c.sel, cs[t]);
            SB();
            if constexpr (n < 28) w[n & 1][t] = wfrag(c, L + nb + WB, t, n & 3);
            if constexpr (i < 2) { if constexpr (NT == 2) { stat(ua[2 * t]); stat(ua[2 * t + 1]); } else { stat(ua[t]); if constexpr (t == 2) stat(ua[3]); } }
            if constexpr (pon && t < 2 && pj + t < 5) p1_piece<pon ? pk : 0, pj + t>(src);
        });
        if constexpr (s == 25) sp[wid * 64 + c.lane] = f32x2{sx[0] + sx[1], sq[0] + sq[1]};
        if constexpr (s == 26) { SB(); cs_put<NT>(c, TB, cs); }
    });
    SB();
    *(AS3 f32x2*)(c.pv + 2 * c.lane) = (cpart[0] + cpart[1]) + (cpart[2] + cpart[3]);
    const f32x2 stt = (st[0] + st[1]) + (st[2] + st[3]);
    mu = stt[0] * (1.0f / 448.0f); var = fmaxf(stt[1] * (1.0f / 448.0f) - mu * mu, 0.f) + 1e-5f;
}

DI void phase1_qkv(const P1Src& src, char* ws, lptr L, int mt, int cg, int wid, u64 tag) {
    P1Lane c; p1_lane(c, L, wid, cg & 1);
    const int r = c.r, h = c.h, tt = mt * 4 + c.m;
    unsigned qo[3][2];
#pragma unroll
    for (int t = 0; t < 3; ++t) {
        const int T = 3 * cg + t;
        if (T >= 28) { const int cc = (T - 28) * 32 + r, head = cc / 28, d = cc - head * 28;
            qo[t][0] = WS_KVF + ((head * 64 + tt) * 4 + 2) * 1024 + (h * 32 + d) * 16; qo[t][1] = d;
        } else {
            const bool isq = T < 14; const int Tp = isq ? T : T - 14;
#pragma unroll
            for (int e = 0; e < 2; ++e) { const int U = 4 * Tp + 2 * e + h, hp = U / 7, u7 = U - 7 * hp, head = 2 * hp + (u7 > 3), uu = u7 & 3;
                qo[t][e] = (isq ? WS_QF + (head * 64 + tt) * 2048 : WS_KVF + (head * 64 + tt) * 4096) + uu * 512 + r * 16; }
        }
        PIN(qo[t][0]); PIN(qo[t][1]);
    }
    f32x16 acc[3]; bf16x8 xf[2]; float mu, var;
    p1_stream<0, 3, 2>(src, c, L, wid, xf, acc, mu, var);
    const float rs = __builtin_amdgcn_rsqf(var);
    const f32x2 nmu2 = {-mu, -mu};
    const rsrc_t rw = mkrsrc(ws, WS_BYTES);
    const lptr TV0 = L + c.m * 6144;
    f32x4 cq[3][4];
#pragma unroll
    for (int t = 0; t < 3; ++t)
#pragma unroll
        for (int gq = 0; gq < 4; ++gq) cq[t][gq] = *(const AS3 f32x4*)(c.pv + 32 * t + 8 * gq + 4 * h);
    SB();
#pragma unroll
    for (int t = 0; t < 3; ++t) {
        const int T = 3 * cg + t;
        const float a0 = rs * (T < 14 ? 0.18898223650461362f * 1.4426950408889634f : 1.0f);
        const f32x2 a2 = {a0, a0};
        unsigned o[8];
#pragma unroll
        for (int gq = 0; gq < 4; ++gq) {
            const f32x4 c4 = cq[t][gq];
            o[2 * gq] = pk2v(a2 * fma2(nmu2, f32x2{c4[0], c4[1]}, f32x2{acc[t][4 * gq], acc[t][4 * gq + 1]}));
            o[2 * gq + 1] = pk2v(a2 * fma2(nmu2, f32x2{c4[2], c4[3]}, f32x2{acc[t][4 * gq + 2], acc[t][4 * gq + 3]}));
        }
        if (T >= 28) {
#pragma unroll
            for (int gq = 0; gq < 4; ++gq) *(AS3 u32x2*)(TV0 + t * 2048 + r * 64 + ((gq ^ (r >> 1)) & 3) * 16 + 8 * h) = u32x2{o[2 * gq], o[2 * gq + 1]};
            if (qo[t][1] >= 24u) {
                const unsigned v = qo[t][1] == 24u ? 0x3f803f80u : 0u;
                st4(rw, qo[t][0] + 64, u32x4{v, v, v, v}); st4(rw, qo[t][0] + 1024 + 64, u32x4{v, v, v, v});
            }
        } else {
#pragma unroll
            for (int e = 0; e < 2; ++e) {
                const auto a = __builtin_amdgcn_permlane32_swap(o[4 * e], o[4 * e + 2], false, false), b = __builtin_amdgcn_permlane32_swap(o[4 * e + 1], o[4 * e + 3], false, false);
                const bool split = (qo[t][e] & 0x600u) == 0x600u;
                st4(rw, qo[t][e], u32x4{a[0], b[0], split ? 0x00003f80u : a[1], split ? 0u : b[1]});
                if (split) st4(rw, qo[t][e] + (T < 14 ? 64 * 2048 : 64 * 4096), u32x4{a[1], b[1], 0x00003f80u, 0u});
            }
        }
    }
    if (cg >= 9) {
        const int row = 4 * h + c.q, chunk = ((c.lane >> 4) & 1) * 2 + (c.p >> 1);
        const lptr p = TV0 + row * 64 + ((chunk ^ (row >> 1)) & 3) * 16 + 8 * (c.p & 1);
        bf16x8 v[3][2];
#pragma unroll
        for (int t = 0; t < 3; ++t)
#pragma unroll
            for (int sx = 0; sx < 2; ++sx) v[t][sx] = cat8(trread(p + t * 2048 + sx * 1024), trread(p + t * 2048 + sx * 1024 + 512));
#pragma unroll
        for (int t = 0; t < 3; ++t) if (3 * cg + t >= 28) { st4(rw, qo[t][0], __builtin_bit_cast(u32x4, v[t][0])); st4(rw, qo[t][0] + 1024, __builtin_bit_cast(u32x4, v[t][1])); }
    }
    publish(lcnt(L, C_PUB1_A), 4, F1P(cg * 16 + mt), 8, 224 * 4, tag);
}

DI void phase1_z(const P1Src& src, const char* wsp, int mi, char* ws, lptr L, int mt, int cg, int wid, u64 tag) {
    P1Lane c; p1_lane(c, L, wid, 0);
    const int lane = c.lane, r = c.r, h = c.h, tt = mt * 4 + c.m;
    bf16x8 xf[28]; f32x16 acc[2]; f32x2 mv;
    { float mu, var; p1_stream<1, 2, 28>(src, c, L, wid, xf, acc, mu, var); mv = f32x2{mu, var}; }
    const float rs = __builtin_amdgcn_rsqf(mv[1]);
    const f32x2 nmu2 = {-mv[0], -mv[0]};
    const float A1 = -2.3022081985f * rs, A3 = (-2.3022081985f * 0.044715f) * rs * rs * rs, sd = mv[1] * rs;
    const f32x2 A1v = {A1, A1}, A3v = {A3, A3}, sdv = {sd, sd};
    const rsrc_t rw = mkrsrc(ws, WS_BYTES);
    f32x2 gz[2][8], zs = {0.f, 0.f}, zq = {0.f, 0.f};
#pragma unroll
    for (int t = 0; t < 2; ++t) {
        f32x4 cz[4];
#pragma unroll
        for (int gq = 0; gq < 4; ++gq) cz[gq] = *(const AS3 f32x4*)(c.pv + 32 * t + 8 * gq + 4 * h);
        gelu16(gz[t], acc[t], cz, nmu2, A1v, A3v, sdv);
        f32x2 a = gz[t][0] + gz[t][1], b = gz[t][0] * gz[t][0], d = gz[t][1] * gz[t][1];
#pragma unroll
        for (int j = 2; j < 8; j += 2) { zs += gz[t][j]; a += gz[t][j + 1]; b = fma2(gz[t][j], gz[t][j], b); d = fma2(gz[t][j + 1], gz[t][j + 1], d); }
        zs += a; zq += b + d;
        SB();
    }
    const unsigned tagA = (unsigned)tag, tagB = (unsigned)(tag >> 32);
    const float ps = halfsum(zs[0] + zs[1]), pq = halfsum(zq[0] + zq[1]);
    const unsigned go = WS_ST + tt * 14 * 512;
    vm_wait<0>(); larrive(lcnt(L, C_Z1));
    if (h == 0) st4(rw, go + cg * 512 + r * 16, u32x4{__float_as_uint(ps), tagA, __float_as_uint(pq), tagB});
    SB();
    u32x4 gr[7];
    lwait4(lcnt(L, C_Z1)); lwait4(lcnt(L, C_PUB1_A));
    {
        f32x4 cs[2]; bf16x8 w[3][2];
#pragma unroll
        for (int t = 0; t < 2; ++t) { cs[t] = f32x4{0.f, 0.f, 0.f, 0.f}; w[0][t] = wfrag(c, L + P1_WZ1, t, 0); w[1][t] = wfrag(c, L + P1_WZ1, t, 1);
#pragma unroll
            for (int i = 0; i < 16; ++i) acc[t][i] = 0.f; }
        unroll<28>([&](auto S) { constexpr int s = S, n = s + 2;
            unroll<2>([&](auto T) { constexpr int t = T;
                SB();
                acc[t] = MFMA32(w[s % 3][t], xf[s], acc[t]);
                if constexpr ((s & 3) == 0) cs[t] = MFMA16(w[s % 3][t], c.sel, cs[t]);
                SB();
                if constexpr (n < 28) w[n % 3][t] = wfrag(c, L + P1_WZ1 + (n >> 2) * 8192, t, n & 3);
            });
            if constexpr (s >= 14 && s < 14 + 7) gr[s - 14] = ld4(rw, lane * 16 + ((s - 14) & 3) * 1024, go + ((s - 14) >> 2) * 4096);
            if constexpr (s == 25) { SB(); cs_put<2>(c, 2, cs); asm volatile("s_waitcnt lgkmcnt(0)" ::: "memory"); larrive(lcnt(L, C_CS_B)); }
        });
        PIN(acc[0]); PIN(acc[1]);
        SB();
    }
    __builtin_amdgcn_s_setprio(2);
    float t1, t2;
    for (int spin = 0; spin < SPIN_CAP; ++spin) {
        bool ok = true; t1 = 0.f; t2 = 0.f;
#pragma unroll
        for (int i = 0; i < 7; ++i) { ok &= gr[i][1] == tagA && gr[i][3] == tagB; t1 += __uint_as_float(gr[i][0]); t2 += __uint_as_float(gr[i][2]); }
        if (__all(ok)) break;
        asm volatile("" ::: "memory");
#pragma unroll
        for (int i = 0; i < 7; ++i) gr[i] = ld4(rw, lane * 16 + (i & 3) * 1024, go + (i >> 2) * 4096);
    }
    t1 = halfsum(t1); t2 = halfsum(t2);
    const float zm = t1 * (1.0f / 896.0f), zv = fmaxf(t2 * (1.0f / 896.0f) - zm * zm, 0.f);
    const float zr = __builtin_amdgcn_rsqf(zv + 1e-5f);
    const f32x2 zr2 = {zr, zr}, zb2 = {-zm * zr, -zm * zr};
    const lptr T = L + L_TRZ + c.m * 4096;
    const int kq = lane >> 4;
#pragma unroll
    for (int t = 0; t < 2; ++t)
#pragma unroll
        for (int gq = 0; gq < 4; ++gq)
            *(AS3 u32x2*)(T + (2 * t + (gq >> 1)) * 1024 + r * 32 + (gq & 1) * 16 + 8 * h) = u32x2{pk2v(fma2(gz[t][2 * gq], zr2, zb2)), pk2v(fma2(gz[t][2 * gq + 1], zr2, zb2))};
#pragma unroll
    for (int i = 0; i < 4; ++i) {
        const bf16x8 v = cat8(trread(T + i * 1024 + (8 * kq + c.q) * 32 + 8 * c.p), trread(T + i * 1024 + (8 * kq + 4 + c.q) * 32 + 8 * c.p));
        const int F = 4 * cg + i, j = F / 7, f = F - 7 * j;
        st4(rw, WS_Z2N + ((j * 64 + tt) * 7 + f) * 1024 + lane * 16, __builtin_bit_cast(u32x4, v));
    }
    SB();
    lwait4(lcnt(L, C_CS_B));
    {   f32x2 cpart[4];
#pragma unroll
        for (int j = 0; j < 4; ++j) cpart[j] = *(const AS3 f32x2*)(c.cp + 256 * j + 64 + 2 * r);
        *(AS3 f32x2*)(c.pv + 2 * r) = (cpart[0] + cpart[1]) + (cpart[2] + cpart[3]); }
    u32x4 z1p[4];
#pragma unroll
    for (int t = 0; t < 2; ++t) {
        f32x4 cz[4];
#pragma unroll
        for (int gq = 0; gq < 4; ++gq) cz[gq] = *(const AS3 f32x4*)(c.pv + 32 * t + 8 * gq + 4 * h);
        f32x2 g1[8];
        gelu16(g1, acc[t], cz, nmu2, A1v, A3v, sdv);
        z1p[2 * t] = u32x4{pk2v(g1[0]), pk2v(g1[1]), pk2v(g1[2]), pk2v(g1[3])}; z1p[2 * t + 1] = u32x4{pk2v(g1[4]), pk2v(g1[5]), pk2v(g1[6]), pk2v(g1[7])};
        PIN(z1p[2 * t]); PIN(z1p[2 * t + 1]);
        SB();
        if (t == 0) publish(lcnt(L, C_PUB1_B), 4, F1P(2048 + cg * 16 + mt), 8, 224 * 4, tag);
    }
#pragma unroll
    for (int i = 0; i < 4; ++i) st4(rw, WS_ZF + (tt * 56 + cg * 4 + i) * 1024 + lane * 16, z1p[i]);
    { const GW gw = make_gw(wsp, L, mi, c.m); gw.tile<0>(); gw.tile<1>(); }
    SB();
}

DI void helper(const unsigned short* wo, const unsigned short* wout, const char* wsp, char* ws, lptr L, int sp, int wid, u64 tag) {
    const int lane = otid() & 63, gw = sp * 8 + wid, h = lane >> 5;
    const rsrc_t rw = mkrsrc(ws, WS_BYTES);
    for (int b = 0; b < 2; ++b) {
        u32x4 t[16];
#pragma unroll
        for (int i = 0; i < 16; ++i) t[i] = *(const u32x4*)(wsp + (size_t)((b * 16 + i) * 256 + gw) * 1024 + lane * 16);
#pragma unroll
        for (int i = 0; i < 16; ++i) asm volatile("" :: "v"(t[i]));
    }
    unsigned e[5][8];
#pragma unroll
    for (int i = 0; i < 5; ++i) {
        const int f = min(gw + 256 * i, 14 * 88 - 1), nt = f / 88, ks = f - nt * 88, col = nt * 32 + (lane & 31);
        const unsigned short* src = ks < 32 ? wo + (size_t)((ks >> 1) * 28 + (ks & 1) * 16) * 448 : wout + (size_t)(ks - 32) * 16 * 448;
        const int hi = (ks < 32 && (ks & 1)) ? 0 : 4 * h;
#pragma unroll
        for (int j = 0; j < 8; ++j) e[i][j] = src[(8 * (j >> 2) + (j < 4 ? 4 * h : hi) + (j & 3)) * 448 + col];
    }
#pragma unroll
    for (int i = 0; i < 5; ++i) {
        const int f = min(gw + 256 * i, 14 * 88 - 1), ks = f % 88;
        const bool pad = ks < 32 && (ks & 1) && h;
        st4(rw, WS_BF + f * 1024 + lane * 16, u32x4{e[i][0] | (e[i][1] << 16), e[i][2] | (e[i][3] << 16), pad ? 0u : e[i][4] | (e[i][5] << 16), pad ? 0u : e[i][6] | (e[i][7] << 16)});
    }
    publish(lcnt(L, C_PUBH), 8, (gu64*)(ws + WS_FH) + sp, 8, 32, tag);
}

DI void attn_store(char* ws, const f32x16& o, int head, int tt) {
    const int lane = otid() & 63;
    const float inv = 1.0f / hihalf(o[12]);
    const rsrc_t rw = mkrsrc(ws, WS_BYTES);
#pragma unroll
    for (int s = 0; s < 2; ++s)
        st4(rw, WS_AF + (tt * 88 + 2 * head + s) * 1024 + lane * 16,
            u32x4{pk2(smul(o[8 * s], inv), smul(o[8 * s + 1], inv)), pk2(smul(o[8 * s + 2], inv), smul(o[8 * s + 3], inv)), pk2(smul(o[8 * s + 4], inv), smul(o[8 * s + 5], inv)), pk2(smul(o[8 * s + 6], inv), smul(o[8 * s + 7], inv))});
}
DI void attn_safe(char* ws, f32x16& o, int head, int qt) {
    const int lane = otid() & 63, h = lane >> 5;
    const rsrc_t rw = mkrsrc(ws, WS_BYTES);
    const unsigned lo = lane * 16, qp = WS_QF + (head * 64 + qt) * 2048;
    const bf16x8 q0 = ld8(rw, lo, qp); u32x4 q1u = ld4(rw, lo + 1024, qp);
    if (h) { q1u[2] = 0u; q1u[3] = 0u; }
    const bf16x8 q1 = __builtin_bit_cast(bf16x8, q1u);
    f32x16 zero;
#pragma unroll
    for (int i = 0; i < 16; ++i) { o[i] = 0.f; zero[i] = 0.f; }
    float m = -3.0e38f;
#pragma unroll 1
    for (int kt = 0; kt < 64; ++kt) {
        const unsigned kp = WS_KVF + (head * 64 + kt) * 4096;
        const bf16x8 k0 = ld8(rw, lo, kp), k1 = ld8(rw, lo + 1024, kp), v0 = ld8(rw, lo + 2048, kp), v1 = ld8(rw, lo + 3072, kp);
        f32x16 s = MFMA32(k0, q0, zero); s = MFMA32(k1, q1, s);
        float mx = s[0];
#pragma unroll
        for (int i = 1; i < 16; ++i) mx = fmaxf(mx, s[i]);
        mx = fmaxf(halfmax(mx), m);
        const float f = EX2(m - mx); m = mx;
        u32x4 p0, p1;
#pragma unroll
        for (int i = 0; i < 16; ++i) { o[i] *= f; s[i] = EX2(s[i] - m); }
#pragma unroll
        for (int i = 0; i < 4; ++i) { p0[i] = pk2(s[2 * i], s[2 * i + 1]); p1[i] = pk2(s[8 + 2 * i], s[9 + 2 * i]); }
        o = MFMA32(v0, __builtin_bit_cast(bf16x8, p0), o); o = MFMA32(v1, __builtin_bit_cast(bf16x8, p1), o);
    }
}

DI void attention(char* ws, lptr L, int head, int qb, int w, int rep, u64 tag) {
    const int lane = otid() & 63, h = lane >> 5;
    {
        const int l = lane & 31, blk = l < 16 ? 1 + (l >> 3) : l < 24 ? 1 : 0, mtp = l < 16 ? 4 * w + (l & 3) : l < 24 ? 0 : qb;
        poll1(F1P(rep * 224 + ((448 * blk + 28 * head + 27 * ((l >> 2) & 1)) / 96) * 16 + mtp), tag);
    }
    const rsrc_t rw = mkrsrc(ws, WS_BYTES);
    unsigned kv = WS_KVF + head * 64 * 4096;
    const unsigned lo = lane * 16;
    bf16x8 q0[4]; u32x4 q1[4];
    bf16x8 r0 = ld8(rw, lo, kv), r1 = ld8(rw, lo + 1024, kv);
#pragma unroll
    for (int t = 0; t < 4; ++t) { const unsigned qp = WS_QF + (head * 64 + qb * 4 + ((w + t) & 3)) * 2048;
        q0[t] = ld8(rw, lo, qp); q1[t] = ld4(rw, lo + 1024, qp); }
    kv += w * 16 * 4096;
    bf16x8 ka0 = ld8(rw, lo, kv), ka1 = ld8(rw, lo + 1024, kv), va0 = ld8(rw, lo + 2048, kv), va1 = ld8(rw, lo + 3072, kv);
    bf16x8 kb0 = ld8(rw, lo, kv + 4096), kb1 = ld8(rw, lo + 1024, kv + 4096), vb0, vb1;
    f32x16 zero, o[4], sA, sB;
    u32x4 p0, p1 = {0u, 0u, 0u, 0u};
#pragma unroll
    for (int i = 0; i < 16; ++i) zero[i] = 0.f;
#pragma unroll
    for (int i = 0; i < 8; ++i) vb1[i] = 0;
#pragma unroll
    for (int t = 0; t < 4; ++t) {
        q1[t][2] = h ? 0u : q1[t][2]; q1[t][3] = h ? 0u : q1[t][3];
        o[t] = MFMA32(r0, q0[t], zero); o[t] = MFMA32(r1, __builtin_bit_cast(bf16x8, q1[t]), o[t]);
    }
#pragma unroll
    for (int t = 0; t < 4; ++t) {
        float mx = fmaxf(o[t][0], o[t][1]);
#pragma unroll
        for (int i = 2; i < 16; i += 2) mx = fmaxf(fmaxf(mx, o[t][i]), o[t][i + 1]);
        mx = halfmax(mx);
        q1[t][2] = h ? pk2(-(mx + 88.0f), 0.f) : q1[t][2];
#pragma unroll
        for (int i = 0; i < 16; ++i) o[t][i] = 0.f;
    }
#define Q1(t) __builtin_bit_cast(bf16x8, q1[t])
#define PB(x) __builtin_bit_cast(bf16x8, x)
#define ASTEP(A, B, O, V0, OP, VP, K0, K1, QN) do { \
    B = MFMA32(K0, q0[QN], zero); SB(); \
    A[0] = EX2(A[0]); A[1] = EX2(A[1]); A[2] = EX2(A[2]); A[3] = EX2(A[3]); PIN(A); SB(); \
    OP = MFMA32(VP, PB(p1), OP); SB(); \
    p0[0] = pk2(A[0], A[1]); p0[1] = pk2(A[2], A[3]); SB(); A[4] = EX2(A[4]); A[5] = EX2(A[5]); A[6] = EX2(A[6]); A[7] = EX2(A[7]); PIN(A); PIN(p0); SB(); \
    B = MFMA32(K1, Q1(QN), B); SB(); \
    p0[2] = pk2(A[4], A[5]); p0[3] = pk2(A[6], A[7]); SB(); A[8] = EX2(A[8]); A[9] = EX2(A[9]); A[10] = EX2(A[10]); A[11] = EX2(A[11]); PIN(A); PIN(p0); SB(); \
    O = MFMA32(V0, PB(p0), O); SB(); \
    A[12] = EX2(A[12]); A[13] = EX2(A[13]); A[14] = EX2(A[14]); A[15] = EX2(A[15]); SB(); \
    p1[0] = pk2(A[8], A[9]); p1[1] = pk2(A[10], A[11]); SB(); p1[2] = pk2(A[12], A[13]); SB(); p1[3] = pk2(A[14], A[15]); PIN(p1); SB(); } while (0)
#define LDK(K0, K1, off) do { K0 = ld8(rw, lo, kv + (off)); K1 = ld8(rw, lo + 1024, kv + (off)); SB(); } while (0)
#define LDV(V0, V1, off) do { V0 = ld8(rw, lo + 2048, kv + (off)); V1 = ld8(rw, lo + 3072, kv + (off)); SB(); } while (0)
#define APAIR(MORE) do { \
    ASTEP(sA, sB, o[0], va0, o[3], vb1, ka0, ka1, 1); LDV(vb0, vb1, 4096); \
    ASTEP(sB, sA, o[1], va0, o[0], va1, ka0, ka1, 2); \
    ASTEP(sA, sB, o[2], va0, o[1], va1, ka0, ka1, 3); if (MORE) LDK(ka0, ka1, 8192); \
    ASTEP(sB, sA, o[3], va0, o[2], va1, kb0, kb1, 0); \
    ASTEP(sA, sB, o[0], vb0, o[3], va1, kb0, kb1, 1); if (MORE) LDV(va0, va1, 8192); \
    ASTEP(sB, sA, o[1], vb0, o[0], vb1, kb0, kb1, 2); \
    ASTEP(sA, sB, o[2], vb0, o[1], vb1, kb0, kb1, 3); if (MORE) LDK(kb0, kb1, 12288); \
    ASTEP(sB, sA, o[3], vb0, o[2], vb1, ka0, ka1, 0); kv += 8192; } while (0)
    sA = MFMA32(ka0, q0[0], zero); sA = MFMA32(ka1, Q1(0), sA);
    for (int it = 0; it < 7; ++it) APAIR(true);
    APAIR(false);
    o[3] = MFMA32(vb1, PB(p1), o[3]);
    lwait4(lcnt(L, C_PUB1_B));
    const lptr P = L + P2_AT;
#pragma unroll
    for (int t = 1; t < 4; ++t)
#pragma unroll
        for (int g = 0; g < 4; ++g) *(AS3 f32x4*)(P + (w * 3 + t - 1) * 4096 + g * 1024 + lo) = f32x4{o[t][4 * g], o[t][4 * g + 1], o[t][4 * g + 2], o[t][4 * g + 3]};
    role_sync(lcnt(L, C_SYNC_A));
#pragma unroll
    for (int t = 1; t < 4; ++t)
#pragma unroll
        for (int g = 0; g < 4; ++g) { f32x4 v = *(const AS3 f32x4*)(P + (((w - t) & 3) * 3 + t - 1) * 4096 + g * 1024 + lo);
#pragma unroll
            for (int c = 0; c < 4; ++c) o[0][4 * g + c] = sadd(o[0][4 * g + c], v[c]); }
    float chk = 0.f;
#pragma unroll
    for (int i = 0; i < 16; ++i) chk = __builtin_fmaf(o[0][i], 0.f, chk);
    float big = fmaxf(fabsf(o[0][0]), fabsf(o[0][1]));
#pragma unroll
    for (int i = 2; i < 12; i += 2) big = fmaxf(fmaxf(big, fabsf(o[0][i])), fabsf(o[0][i + 1]));
    const float l = hihalf(o[0][12]);
    const bool bad = !(chk == 0.f) || !(l > 0x1p-96f) || !(l < 0x1p120f) || !(halfmax(big) > 0x1p-104f);
    if (__builtin_expect(__any(bad), 0)) { attn_safe(ws, o[0], head, qb * 4 + w); SB(); asm volatile("s_nop 15" : "+v"(o[0])); }
    attn_store(ws, o[0], head, qb * 4 + w);
    publish(lcnt(L, C_PUB2_A), 4, (gu64*)(ws + WS_F2) + qb * 384 + head * 4, 2, 192, tag);
}

constexpr int g_extra(int s) { return s == 7 ? 1 : s == 10 ? 7 : 0; }
constexpr int g_issued_before(int s, int f) {
    int n = 14;
    for (int t = 0; t <= s; ++t) {
        n += g_extra(t);
        for (int g = 0; g < 7; ++g) {
            if (t == s && g == f) return n;
            if (t + 2 < 16) ++n;
            if (!(t & 1) && (t >> 1) + 2 < 8) n += g == 6 ? 2 : 1;
        }
    }
    return n;
}
constexpr int g_pos_z(int s, int f) { return s < 2 ? 7 * s + f + 1 : g_issued_before(s - 2, f) + 1; }
constexpr int g_pos_w(int ds) { return g_issued_before(2 * (ds - 2) + 1, 0) - g_extra(2 * (ds - 2) + 1); }
template <int N> DI void vm_wait_for(bf16x8& v) { asm volatile("s_waitcnt vmcnt(%1)" : "+v"(v) : "i"(N)); }
template <int OFF> DI void abld(bf16x8& d, unsigned voff, const u32x4& rs, unsigned soff) {
    asm volatile("buffer_load_dwordx4 %0, %1, %2, %3 offen offset:%4 sc1" : "=v"(d) : "v"(voff), "s"(rs), "s"(soff), "i"(OFF));
}
template <int OFF> DI void abld_rw(bf16x8& d, unsigned voff, const u32x4& rs, unsigned soff) {
    asm volatile("buffer_load_dwordx4 %0, %1, %2, %3 offen offset:%4 sc1" : "+v"(d) : "v"(voff), "s"(rs), "s"(soff), "i"(OFF));
}

template <int S> struct GStep {
    template <class C> static DI void run(C& c) {
        constexpr int ks = S & 1;
        if constexpr (S + 1 < 16) {
            constexpr int nds = (S + 1) >> 1;
            if constexpr (ks == 1 && nds >= 2) vm_wait<g_issued_before(S, 0) - g_extra(S) - g_pos_w(nds)>();
#pragma unroll
            for (int t = 0; t < 4; ++t) c.wf[(S + 1) & 1][t] = *(const AS3 bf16x8*)(c.W + (nds % 3) * 8192 + t * 2048 + (ks ? c.rd0 : c.rd1));
        }
        if constexpr (S == 7) asm volatile("buffer_load_dwordx2 %0, %1, %2, 0 offen sc1" : "=v"(c.flag) : "v"(c.fo), "s"(c.rw));
        if constexpr (S == 10) c.epi_loads();
        SB();
        group<0>(c); group<1>(c); group<2>(c); group<3>(c); group<4>(c); group<5>(c); group<6>(c);
        if constexpr (S + 1 < 16 && S != 9) GStep<S + 1>::run(c);
    }
    template <int F, class C> static DI void group(C& c) {
        constexpr int ds = S >> 1, ks = S & 1;
        vm_wait_for<g_issued_before(S, F) - g_pos_z(S, F)>(c.z[S & 1][F]);
#pragma unroll
        for (int t = 0; t < 4; ++t) c.acc[F][t] = MFMA16(c.z[S & 1][F], c.wf[S & 1][t], c.acc[F][t]);
        SB();
        if constexpr (S + 2 < 16) { constexpr int u = (S + 2) * 7 + F; abld_rw<(u & 3) * 1024>(c.z[S & 1][F], c.lo, c.rw, c.zsrc + (u >> 2) * 4096); }
        if constexpr (ks == 0 && ds + 2 < 8) { c.gw.template piece<ds + 2, F>(); if constexpr (F == 6) c.gw.template piece<ds + 2, 7>(); }
        if constexpr (S == 15 && F > 0) {
#pragma unroll
            for (int t = 1; t < 4; ++t) *(AS3 f32x4*)(c.W + ((t - 1) * 7 + F - 1) * 1024 + c.lo) = c.acc[F > 0 ? F - 1 : 0][t];
        }
        SB();
    }
};
struct GCtx {
    lptr W; unsigned lo, zsrc, zo, fo; int rd0, rd1; GW gw; u32x4 rw;
    f32x4 acc[7][4]; bf16x8 z[2][7], wf[2][4]; u32x2 z1[7], flag;
    DI void epi_loads() {
        asm volatile("buffer_load_dwordx2 %0, %4, %5, 0 offen sc1\n\tbuffer_load_dwordx2 %1, %4, %5, 0 offen offset:1024 sc1\n\tbuffer_load_dwordx2 %2, %4, %5, 0 offen offset:2048 sc1\n\tbuffer_load_dwordx2 %3, %4, %5, 0 offen offset:3072 sc1"
                     : "=&v"(z1[0]), "=&v"(z1[1]), "=&v"(z1[2]), "=&v"(z1[3]) : "v"(zo), "s"(rw));
        asm volatile("buffer_load_dwordx2 %0, %3, %4, %5 offen sc1\n\tbuffer_load_dwordx2 %1, %3, %4, %5 offen offset:1024 sc1\n\tbuffer_load_dwordx2 %2, %3, %4, %5 offen offset:2048 sc1"
                     : "=&v"(z1[4]), "=&v"(z1[5]), "=&v"(z1[6]) : "v"(zo), "s"(rw), "s"(4096u));
    }
};

DI void spatial(const char* wsp, const unsigned short* bsp, char* ws, lptr L, int mi, int j, int w, int rep, int z1flag, u64 tag) {
    const int lane = otid() & 63, r = lane & 15, kq = lane >> 4;
    GCtx c;
    c.W = L + w * P2_GP; c.gw = make_gw(wsp, L, mi, w); c.lo = lane * 16;
    c.rw = rawrsrc(ws, WS_BYTES);
    c.zsrc = WS_Z2N + (j * 64 + w * 16) * 7 * 1024;
    c.rd0 = r * 128 + ((kq ^ (r >> 1)) << 4); c.rd1 = c.rd0 ^ 64;
    const int tok = mi * 64 + w * 16 + r, tt = mi * 2 + (w >> 1), cgp = min((7 * j) / 4 + kq, (7 * j + 6) / 4);
    c.zo = WS_ZF + (tt * 56 + 7 * j) * 1024 + ((kq & 1) * 32 + (tok & 31)) * 16 + (kq >> 1) * 8;
    float bias = bflo(bsp[tok]); PIN(bias);
    c.fo = lane < 32 ? WS_F1 + (4096 + min((7 * j) / 4 + (lane & 3), (7 * j + 6) / 4) * 16 + (mi >> 1)) * 8 * 4 : WS_FH + (rep * 32 + lane - 32) * 8;
#pragma unroll
    for (int a = 0; a < 7; ++a)
#pragma unroll
        for (int b = 0; b < 4; ++b) c.acc[a][b] = f32x4{0.f, 0.f, 0.f, 0.f};
    if (z1flag >= 0) publish(lcnt(L, C_PUB1_Z), 4, F1P(4096 + z1flag), 1, 0, tag); else vm_wait<0>();
#pragma unroll
    for (int t = 0; t < 4; ++t) c.wf[0][t] = *(const AS3 bf16x8*)(c.W + t * 2048 + c.rd0);
    poll1(F1P(2048 + rep * 224 + cgp * 16 + 4 * w + (r & 3)), tag);
    vm_wait<0>();
#define ZP1(s, f) abld<(((s) * 7 + (f)) & 3) * 1024>(c.z[s][f], c.lo, c.rw, c.zsrc + (((s) * 7 + (f)) >> 2) * 4096)
#define ZPRO(s) ZP1(s, 0); ZP1(s, 1); ZP1(s, 2); ZP1(s, 3); ZP1(s, 4); ZP1(s, 5); ZP1(s, 6)
    ZPRO(0); ZPRO(1);
    SB();
    __builtin_amdgcn_s_setprio(0);
    GStep<0>::run(c);
    asm volatile("" : "+v"(c.flag));
    if (__builtin_expect(!__all(c.flag[0] == (unsigned)tag && c.flag[1] == (unsigned)(tag >> 32)), 0)) { gu64* f = (gu64*)(ws + c.fo); poll2(f, f, tag); }
    if (lane == 0) __hip_atomic_store(lcnt(L, C_FH), 1u, RLX_WG);
    SB();
    GStep<10>::run(c);
    __builtin_amdgcn_s_setprio(2);
#pragma unroll
    for (int t = 1; t < 4; ++t) *(AS3 f32x4*)(c.W + ((t - 1) * 7 + 6) * 1024 + c.lo) = c.acc[6][t];
    role_sync(lcnt(L, C_SYNC_B));
    asm volatile("s_waitcnt vmcnt(0)" : "+v"(c.z1[0]), "+v"(c.z1[1]), "+v"(c.z1[2]), "+v"(c.z1[3]), "+v"(c.z1[4]), "+v"(c.z1[5]), "+v"(c.z1[6]));
    u32x2 o[8];
#pragma unroll
    for (int f = 0; f < 7; ++f) {
        f32x4 g = c.acc[f][0];
#pragma unroll
        for (int t = 1; t < 4; ++t) { const f32x4 v = *(const AS3 f32x4*)(L + ((w - t) & 3) * P2_GP + ((t - 1) * 7 + f) * 1024 + c.lo);
#pragma unroll
            for (int e = 0; e < 4; ++e) g[e] = sadd(g[e], v[e]); }
        o[f] = u32x2{pk2(smul(sadd(g[0], bias), bflo(c.z1[f][0])), smul(sadd(g[1], bias), bfhi(c.z1[f][0]))), pk2(smul(sadd(g[2], bias), bflo(c.z1[f][1])), smul(sadd(g[3], bias), bfhi(c.z1[f][1])))};
    }
    o[7] = o[6];
    const rsrc_t rws = mkrsrc(ws, WS_BYTES);
    const unsigned ho = WS_AF + (tt * 88 + 32 + 7 * j) * 1024 + ((kq & 1) * 32 + (tok & 31)) * 16 + (kq >> 1) * 1024;
#pragma unroll
    for (int f = 0; f < 8; f += 2) {
        const auto a = __builtin_amdgcn_permlane32_swap(o[f][0], o[f + 1][0], false, false), b = __builtin_amdgcn_permlane32_swap(o[f][1], o[f + 1][1], false, false);
        if (f < 6 || lane < 32) st4(rws, ho + f * 1024, u32x4{a[0], b[0], a[1], b[1]});
    }
    publish(lcnt(L, C_PUB2_B), 4, (gu64*)(ws + WS_F2) + mi * 192 + 64 + j * 16, 1, 0, tag);
}

constexpr int P3_ROW = 272, P3_TILE = 64 * P3_ROW;
DI void outproj(const char* x, char* out, char* ws, lptr L, int mi, int nj, int w, u64 tag) {
    const int tid = otid(), lane = tid & 63, r = lane & 31, h = lane >> 5;
    const rsrc_t rw = mkrsrc(ws, WS_BYTES);
    const unsigned lo = lane * 16;
    f32x16 acc[2][2];
#pragma unroll
    for (int i = 0; i < 16; ++i) { acc[0][0][i] = 0.f; acc[0][1][i] = 0.f; acc[1][0][i] = 0.f; acc[1][1][i] = 0.f; }
    auto part = [&](auto NN, int ks0, int f0, int mask) { constexpr int N = NN;
        const unsigned bo = WS_BF + (nj * 2 * 88 + ks0) * 1024, ao = WS_AF + (mi * 2 * 88 + ks0) * 1024;
        bf16x8 a[N][2], b[N][2];
#pragma unroll
        for (int s = 0; s < N; ++s) { b[s][0] = ld8(rw, lo + (s & 3) * 1024, bo + (s >> 2) * 4096); b[s][1] = ld8(rw, lo + (s & 3) * 1024, bo + 88 * 1024 + (s >> 2) * 4096); }
        SB();
        gu64* f = (gu64*)(ws + WS_F2) + mi * 192 + f0 + (lane & mask) * 4;
        for (int spin = 0; spin < SPIN_CAP; ++spin) { if (__all(__hip_atomic_load(f, RLX_AGENT) == tag)) break; __builtin_amdgcn_s_sleep(4); }
        asm volatile("" ::: "memory");
#pragma unroll
        for (int s = 0; s < N; ++s) { a[s][0] = ld8(rw, lo + (s & 3) * 1024, ao + (s >> 2) * 4096); a[s][1] = ld8(rw, lo + (s & 3) * 1024, ao + 88 * 1024 + (s >> 2) * 4096); }
        SB();
#pragma unroll
        for (int s = 0; s < N; ++s) {
            acc[0][0] = MFMA32(b[s][0], a[s][0], acc[0][0]); acc[0][1] = MFMA32(b[s][1], a[s][0], acc[0][1]);
            acc[1][0] = MFMA32(b[s][0], a[s][1], acc[1][0]); acc[1][1] = MFMA32(b[s][1], a[s][1], acc[1][1]);
        }
        PIN(acc[0][0]); PIN(acc[0][1]); PIN(acc[1][0]); PIN(acc[1][1]);
        SB();
    };
    wait_ready(lcnt(L, C_FH));
    const u32x4 xr = *(const u32x4*)(x + (unsigned)((mi * 64 + (tid >> 3)) * 896 + nj * 128 + (tid & 7) * 16));
    if (w < 4) part(std::integral_constant<int, 8>{}, 8 * w, 16 * w, 3);
    part(std::integral_constant<int, 7>{}, 32 + 7 * w, 64 + 16 * w, 0);
    lwait4(lcnt(L, C_PUB2_A)); lwait4(lcnt(L, C_PUB2_B));
#pragma unroll
    for (int tm = 0; tm < 2; ++tm)
#pragma unroll
        for (int tn = 0; tn < 2; ++tn)
#pragma unroll
            for (int g = 0; g < 4; ++g)
                *(AS3 f32x4*)(L + w * P3_TILE + (tm * 32 + r) * P3_ROW + (tn * 32 + 8 * g + 4 * h) * 4) =
                    f32x4{acc[tm][tn][4 * g], acc[tm][tn][4 * g + 1], acc[tm][tn][4 * g + 2], acc[tm][tn][4 * g + 3]};
    __syncthreads();
    const int row = tid >> 3, c8 = tid & 7;
    f32x4 s0 = {0.f, 0.f, 0.f, 0.f}, s1 = s0;
#pragma unroll
    for (int p = 0; p < 8; ++p) { s0 += *(const AS3 f32x4*)(L + p * P3_TILE + row * P3_ROW + c8 * 32); s1 += *(const AS3 f32x4*)(L + p * P3_TILE + row * P3_ROW + c8 * 32 + 16); }
    const u32x4 o = {pk2(s0[0] + bflo(xr[0]), s0[1] + bfhi(xr[0])), pk2(s0[2] + bflo(xr[1]), s0[3] + bfhi(xr[1])),
                     pk2(s1[0] + bflo(xr[2]), s1[1] + bfhi(xr[2])), pk2(s1[2] + bflo(xr[3]), s1[3] + bfhi(xr[3]))};
    st4(mkrsrc(out, 2048 * 896), (mi * 64 + row) * 896 + nj * 128 + c8 * 16, o);
}

__global__ __launch_bounds__(512, 2) void fused_kernel(const char* x, const char* wqkv, const char* win, const unsigned short* wo, const unsigned short* wout,
                                                       const char* wsp, const unsigned short* bsp, char* out, char* ws) {
    extern __shared__ __attribute__((aligned(16))) char smem[];
    u64 pc0; unsigned clen;
    asm volatile("s_getpc_b64 %0\nLcode_begin_%=:\n\ts_mov_b32 %1, Lcode_end_mark - Lcode_begin_%=" : "=s"(pc0), "=s"(clen));
    const lptr L = (lptr)smem;
    const int bid = blockIdx.x, xcd = bid & 7, idx = bid >> 3, lane = threadIdx.x & 63;
    const int wid = __builtin_amdgcn_readfirstlane(threadIdx.x >> 6);
    const unsigned lds0 = (unsigned)(uintptr_t)L;
    const int i7 = (idx * 37) >> 8, m7 = idx - 7 * i7;
    const int mt = (xcd >> 1) * 4 + i7, cg = (xcd & 1) * 7 + m7;
    P1Src src;
    {   const unsigned rowo = (mt * 128 + 16 * wid + (lane >> 3)) * 896;
        src.voA0 = rowo + (((lane & 7) ^ (lane >> 4)) << 4); src.voA1 = rowo + 8 * 896 + (((lane & 7) ^ (4 + (lane >> 4))) << 4);
        const int kr8 = 8 * wid + (lane >> 3), c8 = ((lane & 7) ^ (((lane >> 4) & 1) << 2)) << 4;
        src.voZ = kr8 * 3584 + cg * 128 + c8; src.voQ = kr8 * 2688 + ((cg * 192) & ~127) + c8;
        src.ldsx = lds0 + wid * 2048; src.ldsw = lds0 + wid * 1024;
        src.rx = rawrsrc(x, 2048 * 896); src.rq = rawrsrc(wqkv, 448 * 2688); src.rz = rawrsrc(win, 448 * 3584);
        if (idx < 28) p1_stage<0>(src);
        SB();
    }
    unsigned epoch;
    asm volatile("s_load_dword %0, %1, 0x0\n\ts_waitcnt lgkmcnt(0)" : "=s"(epoch) : "s"(ws + WS_EP) : "memory");
    const u64 tag = (u64)(epoch * 0x9E3779B1u + 0x7F4A7C15u) | ((u64)(epoch * 0x85EBCA6Bu + 0x1B873593u) << 32);
    if (threadIdx.x < 32) *lcnt(L, threadIdx.x) = 0u;
    const int mi = (xcd >> 1) * 8 + (idx >> 2), j = (xcd & 1) * 4 + (idx & 3);
    if (idx < 28) {
        if (wid >= 4) phase1_z(src, wsp, mi, ws, L, mt, cg, wid, tag);
        else phase1_qkv(src, ws, L, mt, cg, wid, tag);
    } else {
        {
            const rsrc_t rc = mkrsrc((const void*)pc0, clen);
            u32x4 t[3];
#pragma unroll
            for (int i = 0; i < 3; ++i) t[i] = __builtin_amdgcn_raw_buffer_load_b128(rc, (i * 32 + (idx - 28) * 8 + wid) * 1024 + lane * 16, 0, 0);
#pragma unroll
            for (int i = 0; i < 3; ++i) asm volatile("" :: "v"(t[i]));
        }
        __syncthreads();
        helper(wo, wout, wsp, ws, L, xcd * 4 + idx - 28, wid, tag);
        if (wid < 4) larrive(lcnt(L, C_PUB1_B));
        else {
            const GW gw = make_gw(wsp, L, mi, wid - 4); gw.tile<0>(); gw.tile<1>();
        }
    }
    if (wid < 4) attention(ws, L, 2 * xcd + (idx >> 4), idx & 15, wid, xcd, tag);
    else spatial(wsp, bsp, ws, L, mi, j, wid - 4, xcd, idx < 28 ? cg * 16 + mt : -1, tag);
    if (idx < 28) outproj(x, out, ws, L, xcd * 4 + i7, m7, wid, tag);
    else if (bid == 255 && wid == 0) {
        const int ln = otid();
        gu64* f = (gu64*)(ws + WS_F2) + (ln >> 4) * 384 + (ln & 15) * 4;
        poll2(f, f + 1536, tag); poll2(f + 3072, f + 4608, tag);
        if (ln == 0) __hip_atomic_store((gu32*)(ws + WS_EP), epoch + 1u, RLX_AGENT);
    }
    asm volatile("Lcode_end_mark:");
}

extern "C" void kernel_launch(void* const* d_in, const int* in_sizes, int n_in, void* d_out, int out_size, void* d_ws, size_t ws_size, hipStream_t stream) {
    (void)hipFuncSetAttribute((const void*)fused_kernel, hipFuncAttributeMaxDynamicSharedMemorySize, LDS_BYTES);
    fused_kernel<<<256, 512, LDS_BYTES, stream>>>((const char*)d_in[0], (const char*)d_in[1], (const char*)d_in[4], (const unsigned short*)d_in[2], (const unsigned short*)d_in[7],
                                                  (const char*)d_in[5], (const unsigned short*)d_in[6], (char*)d_out, (char*)d_ws);
}
```

```cpp
#include <hip/hip_runtime.h>
#include <hip/hip_bf16.h>
#include <stdint.h>
#include <type_traits>

typedef short bf16x8 __attribute__((ext_vector_type(8)));
typedef short s16x4 __attribute__((ext_vector_type(4)));
typedef float f32x16 __attribute__((ext_vector_type(16)));
typedef float f32x4 __attribute__((ext_vector_type(4)));
typedef float f32x2 __attribute__((ext_vector_type(2)));
typedef unsigned u32x4 __attribute__((ext_vector_type(4)));
typedef unsigned u32x2 __attribute__((ext_vector_type(2)));
typedef __bf16 bf2 __attribute__((ext_vector_type(2)));
typedef unsigned long long u64;
#define AS3 __attribute__((address_space(3)))
typedef AS3 char* lptr;
typedef __attribute__((address_space(1))) unsigned gu32;
typedef __attribute__((address_space(1))) u64 gu64;
typedef __amdgpu_buffer_rsrc_t rsrc_t;
#define DI __device__ __forceinline__
#define SB() __builtin_amdgcn_sched_barrier(0)
#define PIN(x) asm volatile("" : "+v"(x))
#define MFMA32(a, b, c) __builtin_amdgcn_mfma_f32_32x32x16_bf16((a), (b), (c), 0, 0, 0)
#define MFMA16(a, b, c) __builtin_amdgcn_mfma_f32_16x16x32_bf16((a), (b), (c), 0, 0, 0)
#define EX2(x) __builtin_amdgcn_exp2f(x)
#define RLX_AGENT __ATOMIC_RELAXED, __HIP_MEMORY_SCOPE_AGENT
#define RLX_WG __ATOMIC_RELAXED, __HIP_MEMORY_SCOPE_WORKGROUP

constexpr unsigned WS_QF = 0;
constexpr unsigned WS_KVF = 2u << 20;
constexpr unsigned WS_ZF = 6u << 20;
constexpr unsigned WS_Z2N = 10u << 20;
constexpr unsigned WS_AF = 14u << 20;
constexpr unsigned WS_BF = 20u << 20;
constexpr unsigned WS_ST = WS_BF + (3u << 19);
constexpr unsigned WS_EP = 22u << 20;
constexpr unsigned WS_F1 = WS_EP + 4096;
constexpr unsigned WS_F2 = WS_F1 + 49152 * 4;
constexpr unsigned WS_FH = WS_F2 + 65536;
constexpr unsigned WS_BYTES = 23u << 20;
#define F1P(i) ((gu64*)(ws + WS_F1) + (i) * 4)


DI int otid() { int t = threadIdx.x; asm volatile("" : "+v"(t)); return t; }
DI unsigned pk2(float a, float b) { f32x2 v = {a, b}; return __builtin_bit_cast(unsigned, __builtin_convertvector(v, bf2)); }
DI unsigned pk2v(f32x2 v) { return __builtin_bit_cast(unsigned, __builtin_convertvector(v, bf2)); }
DI float bflo(unsigned u) { return __uint_as_float(u << 16); }
DI float bfhi(unsigned u) { return __uint_as_float(u & 0xffff0000u); }
DI f32x2 fma2(f32x2 a, f32x2 b, f32x2 c) { return __builtin_elementwise_fma(a, b, c); }
DI float sadd(float a, float b) { float d; asm("v_add_f32 %0, %1, %2" : "=v"(d) : "v"(a), "v"(b)); return d; }
DI float smul(float a, float b) { float d; asm("v_mul_f32 %0, %1, %2" : "=v"(d) : "v"(a), "v"(b)); return d; }
DI float halfsum(float v) { auto r = __builtin_amdgcn_permlane32_swap(__float_as_uint(v), __float_as_uint(v), false, false); return __uint_as_float(r[0]) + __uint_as_float(r[1]); }
DI float halfmax(float v) { auto r = __builtin_amdgcn_permlane32_swap(__float_as_uint(v), __float_as_uint(v), false, false); return fmaxf(__uint_as_float(r[0]), __uint_as_float(r[1])); }
DI float hihalf(float v) { auto r = __builtin_amdgcn_permlane32_swap(__float_as_uint(v), __float_as_uint(v), false, false); return __uint_as_float(r[1]); }
DI s16x4 trread(lptr p) { return __builtin_bit_cast(s16x4, __builtin_amdgcn_ds_read_tr16_b64_v4i16((AS3 s16x4*)p)); }
DI bf16x8 cat8(s16x4 a, s16x4 b) { return __builtin_shufflevector(a, b, 0, 1, 2, 3, 4, 5, 6, 7); }
template <int GO, int LO> DI void dma16(unsigned voff, const u32x4& rs, unsigned soff, unsigned lds) {
    unsigned tmp;
    asm volatile("s_add_u32 m0, %4, %6\n\ts_add_u32 %0, %3, %5\n\tbuffer_load_dwordx4 %1, %2, %0 offen lds"
                 : "=&s"(tmp) : "v"(voff), "s"(rs), "s"(soff), "s"(lds), "i"(GO), "i"(LO) : "memory", "scc");
}
DI u32x4 rawrsrc(const void* p, unsigned bytes) { const u64 a = (u64)p; return u32x4{(unsigned)a, (unsigned)(a >> 32) & 0xffffu, bytes, 0x00020000u}; }
template <int N, class F> DI void unroll(F f) { if constexpr (N > 0) { unroll<N - 1>(f); f(std::integral_constant<int, N - 1>{}); } }
DI rsrc_t mkrsrc(const void* p, unsigned bytes) { return __builtin_amdgcn_make_buffer_rsrc((void*)p, 0, bytes, 0x00020000); }
DI u32x4 ld4(rsrc_t r, unsigned voff, unsigned soff) { return __builtin_amdgcn_raw_buffer_load_b128(r, voff, soff, 16); }
DI bf16x8 ld8(rsrc_t r, unsigned voff, unsigned soff) { return __builtin_bit_cast(bf16x8, __builtin_amdgcn_raw_buffer_load_b128(r, voff, soff, 16)); }
DI void st4(rsrc_t r, unsigned voff, u32x4 v) { __builtin_amdgcn_raw_buffer_store_b128(v, r, voff, 0, 16); }

constexpr int P1_SLOT = 40960, P1_WZ = 16384, P1_WQ = 24576, P1_WZ1 = P1_SLOT + 8192;
constexpr int P2_GP = 24576, P2_AT = 4 * P2_GP;
constexpr int P1_CP = P2_AT + 4 * 12288, P1_SP = P1_CP + 4096, P1_PRIV = P1_SP + 4096;
constexpr int L_CNT = P1_PRIV + 8 * 512, LDS_BYTES = L_CNT + 128;
constexpr int L_TRZ = 3 * P1_SLOT;
static_assert(LDS_BYTES <= 163840 && L_TRZ + 16384 <= P1_CP, "LDS map");
enum { C_SYNC_A, C_SYNC_B, C_PUB2_A, C_PUB2_B, C_PUB1_A, C_PUB1_B, C_PUBH, C_PUB1_Z, C_CS_B, C_Z1, C_FH };
DI AS3 unsigned* lcnt(lptr L, int i) { return (AS3 unsigned*)(L + L_CNT) + i; }
DI void larrive(AS3 unsigned* c) { if ((otid() & 63) == 0) __hip_atomic_fetch_add(c, 1u, RLX_WG); }
DI void lwait4(AS3 unsigned* c, unsigned n = 4u) { while (__hip_atomic_load(c, RLX_WG) < n) __builtin_amdgcn_s_sleep(0); asm volatile("" ::: "memory"); }
constexpr int SPIN_CAP = 100000;

DI void role_sync(AS3 unsigned* cnt) {
    asm volatile("s_waitcnt lgkmcnt(0)" ::: "memory");
    if ((otid() & 63) == 0) __hip_atomic_fetch_add(cnt, 1u, RLX_WG);
    while (__hip_atomic_load(cnt, RLX_WG) < 4u) __builtin_amdgcn_s_sleep(1);
    asm volatile("" ::: "memory");
}
DI void publish(AS3 unsigned* cnt, unsigned n, gu64* flag, int copies, int stride, u64 tag) {
    asm volatile("s_waitcnt vmcnt(0)" ::: "memory");
    const int lane = otid() & 63;
    unsigned old = 0;
    if (lane == 0) old = __hip_atomic_fetch_add(cnt, 1u, RLX_WG);
    if (__builtin_amdgcn_readfirstlane(old) == n - 1 && lane < copies) __hip_atomic_store(flag + lane * stride, tag, RLX_AGENT);
}
DI void wait_ready(AS3 unsigned* w) { while (__hip_atomic_load(w, RLX_WG) == 0u) __builtin_amdgcn_s_sleep(1); asm volatile("" ::: "memory"); }
DI void poll1(gu64* a, u64 tag) {
    for (int spin = 0; spin < SPIN_CAP; ++spin) {
        if (__all(__hip_atomic_load(a, RLX_AGENT) == tag)) break;
        __builtin_amdgcn_s_sleep(3);
    }
    asm volatile("" ::: "memory");
}
DI void poll2(gu64* a, gu64* b, u64 tag) {
    for (int spin = 0; spin < SPIN_CAP; ++spin) {
        const u64 va = __hip_atomic_load(a, RLX_AGENT), vb = __hip_atomic_load(b, RLX_AGENT);
        if (__all(va == tag && vb == tag)) break;
        __builtin_amdgcn_s_sleep(2);
    }
    asm volatile("" ::: "memory");
}

template <int N> DI void vm_wait() { asm volatile("s_waitcnt vmcnt(%0)" :: "i"(N) : "memory"); }
template <int N> DI void waitbar() { asm volatile("s_waitcnt vmcnt(%0) lgkmcnt(0)\n\ts_barrier" :: "i"(N) : "memory"); }
DI void lbar() { asm volatile("s_waitcnt lgkmcnt(0)\n\ts_barrier" ::: "memory"); }
struct P1Src { unsigned voA0, voA1, voZ, voQ, ldsx, ldsw; u32x4 rx, rz, rq; };
template <int KT, int J> DI void p1_piece(const P1Src& s) {
    constexpr int so = (KT % 3) * P1_SLOT;
    if constexpr (KT < 7) {
        if (J == 0) dma16<KT * 128, so>(s.voA0, s.rx, 0, s.ldsx);
        if (J == 1) dma16<KT * 128, so + 1024>(s.voA1, s.rx, 0, s.ldsx);
        if (J == 2) dma16<KT * 64 * 3584 + 1792, so + P1_WZ>(s.voZ, s.rz, 0, s.ldsw);
        if (J == 3) dma16<KT * 64 * 2688, so + P1_WQ>(s.voQ, s.rq, 0, s.ldsw);
        if (J == 4) dma16<KT * 64 * 2688 + 128, so + P1_WQ + 8192>(s.voQ, s.rq, 0, s.ldsw);
    } else if constexpr ((KT - 7) * 4 + J < 7 && J < 4) {
        constexpr int k = (KT - 7) * 4 + J;
        dma16<k * 64 * 3584, P1_WZ1 + k * 8192>(s.voZ, s.rz, 0, s.ldsw);
    }
}
template <int KT> DI void p1_stage(const P1Src& s) { p1_piece<KT, 0>(s); p1_piece<KT, 1>(s); p1_piece<KT, 2>(s); p1_piece<KT, 3>(s); p1_piece<KT, 4>(s); }
struct GW { unsigned wv0, wv1, wdst, rowg[4]; u32x4 rs;
    template <int DS, int I> DI void piece() const { dma16<(I & 1) * 8 * 4096 + DS * 128, (DS % 3) * 8192 + I * 1024>((I & 1) ? wv1 : wv0, rs, rowg[I >> 1], wdst); }
    template <int DS> DI void tile() const { piece<DS, 0>(); piece<DS, 1>(); piece<DS, 2>(); piece<DS, 3>(); piece<DS, 4>(); piece<DS, 5>(); piece<DS, 6>(); piece<DS, 7>(); } };

DI GW make_gw(const char* wsp, lptr L, int mi, int w) {
    GW g; const int lane = otid() & 63;
    const unsigned wvo = (mi * 64 + (lane >> 3)) * 4096 + w * 1024;
    g.wv0 = wvo + (((lane & 7) ^ (lane >> 4)) << 4); g.wv1 = wvo + (((lane & 7) ^ (4 + (lane >> 4))) << 4);
    g.wdst = (unsigned)(uintptr_t)L + w * P2_GP; g.rs = rawrsrc(wsp, 2048 * 4096);
#pragma unroll
    for (int k = 0; k < 4; ++k) g.rowg[k] = ((k + w) & 3) * 16 * 4096;
    return g;
}
DI void gelu16(f32x2 (&g)[8], const f32x16& acc, const f32x4 (&cs)[4], f32x2 nmu2, f32x2 A1v, f32x2 A3v, f32x2 sdv) {
    f32x2 u[8], e[8];
#pragma unroll
    for (int j = 0; j < 8; ++j) u[j] = fma2(nmu2, f32x2{cs[j >> 1][2 * (j & 1)], cs[j >> 1][2 * (j & 1) + 1]}, f32x2{acc[2 * j], acc[2 * j + 1]});
    SB();
#pragma unroll
    for (int j = 0; j < 8; ++j) e[j] = u[j] * u[j];
    SB();
#pragma unroll
    for (int j = 0; j < 8; ++j) e[j] = fma2(e[j], A3v, A1v);
    SB();
#pragma unroll
    for (int j = 0; j < 8; ++j) e[j] = e[j] * u[j];
    SB();
#pragma unroll
    for (int j = 0; j < 8; ++j) e[j] = f32x2{EX2(e[j][0]), EX2(e[j][1])};
    SB();
#pragma unroll
    for (int j = 0; j < 8; ++j) e[j] = fma2(e[j], sdv, sdv);
    SB();
#pragma unroll
    for (int j = 0; j < 8; ++j) e[j] = f32x2{__builtin_amdgcn_rcpf(e[j][0]), __builtin_amdgcn_rcpf(e[j][1])};
    SB();
#pragma unroll
    for (int j = 0; j < 8; ++j) g[j] = e[j] * u[j];
    SB();
}

struct P1Lane { int lane, r, h, m, q, p, offA[4], offW[4][3]; bf16x8 sel; AS3 float* cp; AS3 float* pv; };
DI void p1_lane(P1Lane& c, lptr L, int wid, int t0) {
    c.lane = otid() & 63; c.r = c.lane & 31; c.h = c.lane >> 5; c.m = wid & 3; c.q = (c.lane & 15) >> 2; c.p = c.lane & 3;
#pragma unroll
    for (int i = 0; i < 4; ++i) {
        const int ks = (i + wid + (wid >> 2) * 2) & 3;
        c.offA[i] = (c.m * 32 + c.r) * 128 + (((2 * ks + c.h) ^ ((c.r >> 1) & 7)) << 4);
#pragma unroll
        for (int t = 0; t < 3; ++t) c.offW[i][t] = ((t0 + t) >> 1) * 8192 + (16 * ks + 8 * c.h + c.q) * 128 + ((((t0 + t) ^ (c.q >> 1)) & 1) << 6) + 32 * ((c.lane >> 4) & 1) + 8 * c.p;
    }
    const unsigned selw = (c.lane & 15) == ((c.lane >> 4) & 1) ? 0x3f803f80u : 0u;
    c.sel = __builtin_bit_cast(bf16x8, u32x4{selw, selw, selw, selw});
    c.cp = (AS3 float*)(L + P1_CP);
    c.pv = (AS3 float*)(L + P1_PRIV + wid * 512);
}
DI bf16x8 wfrag(const P1Lane& c, lptr base, int t, int i) { return cat8(trread(base + c.offW[i][t]), trread(base + c.offW[i][t] + 512)); }
template <int NT> DI void cs_put(const P1Lane& c, int tb, const f32x4 (&cs)[NT]) {
    if ((c.lane & 15) < 2) {
#pragma unroll
        for (int t = 0; t < NT; ++t) *(AS3 f32x4*)(c.cp + c.m * 256 + (tb + t) * 32 + (c.lane & 15) * 16 + (c.lane >> 4) * 4) = cs[t];
    }
}
template <int ROLE, int NT, int NX>
DI void p1_stream(const P1Src& src, const P1Lane& c, lptr L, int wid, bf16x8 (&xf)[NX], f32x16 (&acc)[NT], float& mu, float& var) {
    constexpr int WB = ROLE ? P1_WZ : P1_WQ, TB = ROLE ? 0 : 4;
    f32x4 cs[NT]; bf16x8 w[2][NT];
#pragma unroll
    for (int t = 0; t < NT; ++t) { cs[t] = f32x4{0.f, 0.f, 0.f, 0.f};
#pragma unroll
        for (int i = 0; i < 16; ++i) acc[t][i] = 0.f; }
    float sx[2] = {0.f, 0.f}, sq[2] = {0.f, 0.f};
    auto stat = [&](unsigned u) { const float lo = bflo(u), hi = bfhi(u);
        sx[0] += lo; sx[1] += hi; sq[0] = __builtin_fmaf(lo, lo, sq[0]); sq[1] = __builtin_fmaf(hi, hi, sq[1]);
        PIN(sx[0]); PIN(sx[1]); PIN(sq[0]); PIN(sq[1]); };
    AS3 f32x2* sp = (AS3 f32x2*)(L + P1_SP);
    f32x2 cpart[4], st[4];
    SB();
    p1_stage<1>(src);
    waitbar<5>();
    xf[0] = *(const AS3 bf16x8*)(L + c.offA[0]);
#pragma unroll
    for (int t = 0; t < NT; ++t) w[0][t] = wfrag(c, L + WB, t, 0);
    unroll<28>([&](auto S) { constexpr int s = S, kt = s >> 2, i = s & 3, n = s + 1, nb = ((n >> 2) % 3) * P1_SLOT;
        SB();
        if constexpr (i == 3) { if constexpr (kt < 5) waitbar<5>(); else if constexpr (kt == 5) waitbar<4>(); else lbar(); }
        if constexpr (n < 28) xf[n % NX] = *(const AS3 bf16x8*)(L + nb + c.offA[n & 3]);
        else {
#pragma unroll
            for (int j = 0; j < 4; ++j) cpart[j] = *(const AS3 f32x2*)(c.cp + 256 * j + TB * 32 + 2 * c.lane);
            st[0] = sp[c.m * 64 + c.r]; st[1] = sp[c.m * 64 + 32 + c.r]; st[2] = sp[(c.m + 4) * 64 + c.r]; st[3] = sp[(c.m + 4) * 64 + 32 + c.r];
        }
        const u32x4 ua = __builtin_bit_cast(u32x4, xf[s % NX]);
        constexpr int pk = i == 3 ? kt + 3 : kt + 2;
        constexpr bool pon = pk < 9 && (i != 2 || kt == 0);
        constexpr int pj = kt == 0 && i < 3 ? 2 * i : 2 * ((i + 1) & 3);
        unroll<NT>([&](auto T) { constexpr int t = T;
            SB();
            acc[t] = MFMA32(w[s & 1][t], xf[s % NX], acc[t]);
            if constexpr (i == 2) cs[t] = MFMA16(w[s & 1][t], c.sel, cs[t]);
            SB();
            if constexpr (n < 28) w[n & 1][t] = wfrag(c, L + nb + WB, t, n & 3);
            if constexpr (i < 2) { if constexpr (NT == 2) { stat(ua[2 * t]); stat(ua[2 * t + 1]); } else { stat(ua[t]); if constexpr (t == 2) stat(ua[3]); } }
            if constexpr (pon && t < 2 && pj + t < 5) p1_piece<pon ? pk : 0, pj + t>(src);
        });
        if constexpr (s == 25) sp[wid * 64 + c.lane] = f32x2{sx[0] + sx[1], sq[0] + sq[1]};
        if constexpr (s == 26) { SB(); cs_put<NT>(c, TB, cs); }
    });
    SB();
    *(AS3 f32x2*)(c.pv + 2 * c.lane) = (cpart[0] + cpart[1]) + (cpart[2] + cpart[3]);
    const f32x2 stt = (st[0] + st[1]) + (st[2] + st[3]);
    mu = stt[0] * (1.0f / 448.0f); var = fmaxf(stt[1] * (1.0f / 448.0f) - mu * mu, 0.f) + 1e-5f;
}

DI void phase1_qkv(const P1Src& src, char* ws, lptr L, int mt, int cg, int wid, u64 tag) {
    P1Lane c; p1_lane(c, L, wid, cg & 1);
    const int r = c.r, h = c.h, tt = mt * 4 + c.m;
    unsigned qo[3][2];
#pragma unroll
    for (int t = 0; t < 3; ++t) {
        const int T = 3 * cg + t;
        if (T >= 28) { const int cc = (T - 28) * 32 + r, head = cc / 28, d = cc - head * 28;
            qo[t][0] = WS_KVF + ((head * 64 + tt) * 4 + 2) * 1024 + (h * 32 + d) * 16; qo[t][1] = d;
        } else {
            const bool isq = T < 14; const int Tp = isq ? T : T - 14;
#pragma unroll
            for (int e = 0; e < 2; ++e) { const int U = 4 * Tp + 2 * e + h, hp = U / 7, u7 = U - 7 * hp, head = 2 * hp + (u7 > 3), uu = u7 & 3;
                qo[t][e] = (isq ? WS_QF + (head * 64 + tt) * 2048 : WS_KVF + (head * 64 + tt) * 4096) + uu * 512 + r * 16; }
        }
        PIN(qo[t][0]); PIN(qo[t][1]);
    }
    f32x16 acc[3]; bf16x8 xf[2]; float mu, var;
    p1_stream<0, 3, 2>(src, c, L, wid, xf, acc, mu, var);
    const float rs = __builtin_amdgcn_rsqf(var);
    const f32x2 nmu2 = {-mu, -mu};
    const rsrc_t rw = mkrsrc(ws, WS_BYTES);
    const lptr TV0 = L + c.m * 6144;
    f32x4 cq[3][4];
#pragma unroll
    for (int t = 0; t < 3; ++t)
#pragma unroll
        for (int gq = 0; gq < 4; ++gq) cq[t][gq] = *(const AS3 f32x4*)(c.pv + 32 * t + 8 * gq + 4 * h);
    SB();
#pragma unroll
    for (int t = 0; t < 3; ++t) {
        const int T = 3 * cg + t;
        const float a0 = rs * (T < 14 ? 0.18898223650461362f * 1.4426950408889634f : 1.0f);
        const f32x2 a2 = {a0, a0};
        unsigned o[8];
#pragma unroll
        for (int gq = 0; gq < 4; ++gq) {
            const f32x4 c4 = cq[t][gq];
            o[2 * gq] = pk2v(a2 * fma2(nmu2, f32x2{c4[0], c4[1]}, f32x2{acc[t][4 * gq], acc[t][4 * gq + 1]}));
            o[2 * gq + 1] = pk2v(a2 * fma2(nmu2, f32x2{c4[2], c4[3]}, f32x2{acc[t][4 * gq + 2], acc[t][4 * gq + 3]}));
        }
        if (T >= 28) {
#pragma unroll
            for (int gq = 0; gq < 4; ++gq) *(AS3 u32x2*)(TV0 + t * 2048 + r * 64 + ((gq ^ (r >> 1)) & 3) * 16 + 8 * h) = u32x2{o[2 * gq], o[2 * gq + 1]};
            if (qo[t][1] >= 24u) {
                const unsigned v = qo[t][1] == 24u ? 0x3f803f80u : 0u;
                st4(rw, qo[t][0] + 64, u32x4{v, v, v, v}); st4(rw, qo[t][0] + 1024 + 64, u32x4{v, v, v, v});
            }
        } else {
#pragma unroll
            for (int e = 0; e < 2; ++e) {
                const auto a = __builtin_amdgcn_permlane32_swap(o[4 * e], o[4 * e + 2], false, false), b = __builtin_amdgcn_permlane32_swap(o[4 * e + 1], o[4 * e + 3], false, false);
                const bool split = (qo[t][e] & 0x600u) == 0x600u;
                st4(rw, qo[t][e], u32x4{a[0], b[0], split ? 0x00003f80u : a[1], split ? 0u : b[1]});
                if (split) st4(rw, qo[t][e] + (T < 14 ? 64 * 2048 : 64 * 4096), u32x4{a[1], b[1], 0x00003f80u, 0u});
            }
        }
    }
    if (cg >= 9) {
        const int row = 4 * h + c.q, chunk = ((c.lane >> 4) & 1) * 2 + (c.p >> 1);
        const lptr p = TV0 + row * 64 + ((chunk ^ (row >> 1)) & 3) * 16 + 8 * (c.p & 1);
        bf16x8 v[3][2];
#pragma unroll
        for (int t = 0; t < 3; ++t)
#pragma unroll
            for (int sx = 0; sx < 2; ++sx) v[t][sx] = cat8(trread(p + t * 2048 + sx * 1024), trread(p + t * 2048 + sx * 1024 + 512));
#pragma unroll
        for (int t = 0; t < 3; ++t) if (3 * cg + t >= 28) { st4(rw, qo[t][0], __builtin_bit_cast(u32x4, v[t][0])); st4(rw, qo[t][0] + 1024, __builtin_bit_cast(u32x4, v[t][1])); }
    }
    publish(lcnt(L, C_PUB1_A), 4, F1P(cg * 16 + mt), 8, 224 * 4, tag);
}

DI void phase1_z(const P1Src& src, const char* wsp, int mi, char* ws, lptr L, int mt, int cg, int wid, u64 tag) {
    P1Lane c; p1_lane(c, L, wid, 0);
    const int lane = c.lane, r = c.r, h = c.h, tt = mt * 4 + c.m;
    bf16x8 xf[28]; f32x16 acc[2]; f32x2 mv;
    { float mu, var; p1_stream<1, 2, 28>(src, c, L, wid, xf, acc, mu, var); mv = f32x2{mu, var}; }
    const float rs = __builtin_amdgcn_rsqf(mv[1]);
    const f32x2 nmu2 = {-mv[0], -mv[0]};
    const float A1 = -2.3022081985f * rs, A3 = (-2.3022081985f * 0.044715f) * rs * rs * rs, sd = mv[1] * rs;
    const f32x2 A1v = {A1, A1}, A3v = {A3, A3}, sdv = {sd, sd};
    const rsrc_t rw = mkrsrc(ws, WS_BYTES);
    f32x2 gz[2][8], zs = {0.f, 0.f}, zq = {0.f, 0.f};
#pragma unroll
    for (int t = 0; t < 2; ++t) {
        f32x4 cz[4];
#pragma unroll
        for (int gq = 0; gq < 4; ++gq) cz[gq] = *(const AS3 f32x4*)(c.pv + 32 * t + 8 * gq + 4 * h);
        gelu16(gz[t], acc[t], cz, nmu2, A1v, A3v, sdv);
        f32x2 a = gz[t][0] + gz[t][1], b = gz[t][0] * gz[t][0], d = gz[t][1] * gz[t][1];
#pragma unroll
        for (int j = 2; j < 8; j += 2) { zs += gz[t][j]; a += gz[t][j + 1]; b = fma2(gz[t][j], gz[t][j], b); d = fma2(gz[t][j + 1], gz[t][j + 1], d); }
        zs += a; zq += b + d;
        SB();
    }
    const unsigned tagA = (unsigned)tag, tagB = (unsigned)(tag >> 32);
    const float ps = halfsum(zs[0] + zs[1]), pq = halfsum(zq[0] + zq[1]);
    const unsigned go = WS_ST + tt * 14 * 512;
    vm_wait<0>(); larrive(lcnt(L, C_Z1));
    if (h == 0) st4(rw, go + cg * 512 + r * 16, u32x4{__float_as_uint(ps), tagA, __float_as_uint(pq), tagB});
    SB();
    u32x4 gr[7];
    lwait4(lcnt(L, C_Z1)); lwait4(lcnt(L, C_PUB1_A));
    {
        f32x4 cs[2]; bf16x8 w[3][2];
#pragma unroll
        for (int t = 0; t < 2; ++t) { cs[t] = f32x4{0.f, 0.f, 0.f, 0.f}; w[0][t] = wfrag(c, L + P1_WZ1, t, 0); w[1][t] = wfrag(c, L + P1_WZ1, t, 1);
#pragma unroll
            for (int i = 0; i < 16; ++i) acc[t][i] = 0.f; }
        unroll<28>([&](auto S) { constexpr int s = S, n = s + 2;
            unroll<2>([&](auto T) { constexpr int t = T;
                SB();
                acc[t] = MFMA32(w[s % 3][t], xf[s], acc[t]);
                if constexpr ((s & 3) == 0) cs[t] = MFMA16(w[s % 3][t], c.sel, cs[t]);
                SB();
                if constexpr (n < 28) w[n % 3][t] = wfrag(c, L + P1_WZ1 + (n >> 2) * 8192, t, n & 3);
            });
            if constexpr (s >= 14 && s < 14 + 7) gr[s - 14] = ld4(rw, lane * 16 + ((s - 14) & 3) * 1024, go + ((s - 14) >> 2) * 4096);
            if constexpr (s == 25) { SB(); cs_put<2>(c, 2, cs); asm volatile("s_waitcnt lgkmcnt(0)" ::: "memory"); larrive(lcnt(L, C_CS_B)); }
        });
        PIN(acc[0]); PIN(acc[1]);
        SB();
    }
    __builtin_amdgcn_s_setprio(2);
    float t1, t2;
    for (int spin = 0; spin < SPIN_CAP; ++spin) {
        bool ok = true; t1 = 0.f; t2 = 0.f;
#pragma unroll
        for (int i = 0; i < 7; ++i) { ok &= gr[i][1] == tagA && gr[i][3] == tagB; t1 += __uint_as_float(gr[i][0]); t2 += __uint_as_float(gr[i][2]); }
        if (__all(ok)) break;
        asm volatile("" ::: "memory");
#pragma unroll
        for (int i = 0; i < 7; ++i) gr[i] = ld4(rw, lane * 16 + (i & 3) * 1024, go + (i >> 2) * 4096);
    }
    t1 = halfsum(t1); t2 = halfsum(t2);
    const float zm = t1 * (1.0f / 896.0f), zv = fmaxf(t2 * (1.0f / 896.0f) - zm * zm, 0.f);
    const float zr = __builtin_amdgcn_rsqf(zv + 1e-5f);
    const f32x2 zr2 = {zr, zr}, zb2 = {-zm * zr, -zm * zr};
    const lptr T = L + L_TRZ + c.m * 4096;
    const int kq = lane >> 4;
#pragma unroll
    for (int t = 0; t < 2; ++t)
#pragma unroll
        for (int gq = 0; gq < 4; ++gq)
            *(AS3 u32x2*)(T + (2 * t + (gq >> 1)) * 1024 + r * 32 + (gq & 1) * 16 + 8 * h) = u32x2{pk2v(fma2(gz[t][2 * gq], zr2, zb2)), pk2v(fma2(gz[t][2 * gq + 1], zr2, zb2))};
#pragma unroll
    for (int i = 0; i < 4; ++i) {
        const bf16x8 v = cat8(trread(T + i * 1024 + (8 * kq + c.q) * 32 + 8 * c.p), trread(T + i * 1024 + (8 * kq + 4 + c.q) * 32 + 8 * c.p));
        const int F = 4 * cg + i, j = F / 7, f = F - 7 * j;
        st4(rw, WS_Z2N + ((j * 64 + tt) * 7 + f) * 1024 + lane * 16, __builtin_bit_cast(u32x4, v));
    }
    SB();
    lwait4(lcnt(L, C_CS_B));
    {   f32x2 cpart[4];
#pragma unroll
        for (int j = 0; j < 4; ++j) cpart[j] = *(const AS3 f32x2*)(c.cp + 256 * j + 64 + 2 * r);
        *(AS3 f32x2*)(c.pv + 2 * r) = (cpart[0] + cpart[1]) + (cpart[2] + cpart[3]); }
    u32x4 z1p[4];
#pragma unroll
    for (int t = 0; t < 2; ++t) {
        f32x4 cz[4];
#pragma unroll
        for (int gq = 0; gq < 4; ++gq) cz[gq] = *(const AS3 f32x4*)(c.pv + 32 * t + 8 * gq + 4 * h);
        f32x2 g1[8];
        gelu16(g1, acc[t], cz, nmu2, A1v, A3v, sdv);
        z1p[2 * t] = u32x4{pk2v(g1[0]), pk2v(g1[1]), pk2v(g1[2]), pk2v(g1[3])}; z1p[2 * t + 1] = u32x4{pk2v(g1[4]), pk2v(g1[5]), pk2v(g1[6]), pk2v(g1[7])};
        PIN(z1p[2 * t]); PIN(z1p[2 * t + 1]);
        SB();
        if (t == 0) publish(lcnt(L, C_PUB1_B), 4, F1P(2048 + cg * 16 + mt), 8, 224 * 4, tag);
    }
#pragma unroll
    for (int i = 0; i < 4; ++i) st4(rw, WS_ZF + (tt * 56 + cg * 4 + i) * 1024 + lane * 16, z1p[i]);
    { const GW gw = make_gw(wsp, L, mi, c.m); gw.tile<0>(); gw.tile<1>(); }
    SB();
}

DI void helper(const unsigned short* wo, const unsigned short* wout, const char* wsp, char* ws, lptr L, int sp, int wid, u64 tag) {
    const int lane = otid() & 63, gw = sp * 8 + wid, h = lane >> 5;
    const rsrc_t rw = mkrsrc(ws, WS_BYTES);
    for (int b = 0; b < 2; ++b) {
        u32x4 t[16];
#pragma unroll
        for (int i = 0; i < 16; ++i) t[i] = *(const u32x4*)(wsp + (size_t)((b * 16 + i) * 256 + gw) * 1024 + lane * 16);
#pragma unroll
        for (int i = 0; i < 16; ++i) asm volatile("" :: "v"(t[i]));
    }
    unsigned e[5][8];
#pragma unroll
    for (int i = 0; i < 5; ++i) {
        const int f = min(gw + 256 * i, 14 * 88 - 1), nt = f / 88, ks = f - nt * 88, col = nt * 32 + (lane & 31);
        const unsigned short* src = ks < 32 ? wo + (size_t)((ks >> 1) * 28 + (ks & 1) * 16) * 448 : wout + (size_t)(ks - 32) * 16 * 448;
        const int hi = (ks < 32 && (ks & 1)) ? 0 : 4 * h;
#pragma unroll
        for (int j = 0; j < 8; ++j) e[i][j] = src[(8 * (j >> 2) + (j < 4 ? 4 * h : hi) + (j & 3)) * 448 + col];
    }
#pragma unroll
    for (int i = 0; i < 5; ++i) {
        const int f = min(gw + 256 * i, 14 * 88 - 1), ks = f % 88;
        const bool pad = ks < 32 && (ks & 1) && h;
        st4(rw, WS_BF + f * 1024 + lane * 16, u32x4{e[i][0] | (e[i][1] << 16), e[i][2] | (e[i][3] << 16), pad ? 0u : e[i][4] | (e[i][5] << 16), pad ? 0u : e[i][6] | (e[i][7] << 16)});
    }
    publish(lcnt(L, C_PUBH), 8, (gu64*)(ws + WS_FH) + sp, 8, 32, tag);
}

DI void attn_store(char* ws, const f32x16& o, int head, int tt) {
    const int lane = otid() & 63;
    const float inv = 1.0f / hihalf(o[12]);
    const rsrc_t rw = mkrsrc(ws, WS_BYTES);
#pragma unroll
    for (int s = 0; s < 2; ++s)
        st4(rw, WS_AF + (tt * 88 + 2 * head + s) * 1024 + lane * 16,
            u32x4{pk2(smul(o[8 * s], inv), smul(o[8 * s + 1], inv)), pk2(smul(o[8 * s + 2], inv), smul(o[8 * s + 3], inv)), pk2(smul(o[8 * s + 4], inv), smul(o[8 * s + 5], inv)), pk2(smul(o[8 * s + 6], inv), smul(o[8 * s + 7], inv))});
}
DI void attn_safe(char* ws, f32x16& o, int head, int qt) {
    const int lane = otid() & 63, h = lane >> 5;
    const rsrc_t rw = mkrsrc(ws, WS_BYTES);
    const unsigned lo = lane * 16, qp = WS_QF + (head * 64 + qt) * 2048;
    const bf16x8 q0 = ld8(rw, lo, qp); u32x4 q1u = ld4(rw, lo + 1024, qp);
    if (h) { q1u[2] = 0u; q1u[3] = 0u; }
    const bf16x8 q1 = __builtin_bit_cast(bf16x8, q1u);
    f32x16 zero;
#pragma unroll
    for (int i = 0; i < 16; ++i) { o[i] = 0.f; zero[i] = 0.f; }
    float m = -3.0e38f;
    const unsigned kb = WS_KVF + head * 64 * 4096;
    bf16x8 n0 = ld8(rw, lo, kb), n1 = ld8(rw, lo + 1024, kb), n2 = ld8(rw, lo + 2048, kb), n3 = ld8(rw, lo + 3072, kb);
#pragma unroll 1
    for (int kt = 0; kt < 64; ++kt) {
        const bf16x8 k0 = n0, k1 = n1, v0 = n2, v1 = n3;
        const unsigned kp = kb + min(kt + 1, 63) * 4096;
        n0 = ld8(rw, lo, kp); n1 = ld8(rw, lo + 1024, kp); n2 = ld8(rw, lo + 2048, kp); n3 = ld8(rw, lo + 3072, kp);
        SB();
        f32x16 s = MFMA32(k0, q0, zero); s = MFMA32(k1, q1, s);
        float mx = s[0];
#pragma unroll
        for (int i = 1; i < 16; ++i) mx = fmaxf(mx, s[i]);
        mx = fmaxf(halfmax(mx), m);
        const float f = EX2(m - mx); m = mx;
        u32x4 p0, p1;
#pragma unroll
        for (int i = 0; i < 16; ++i) { o[i] *= f; s[i] = EX2(s[i] - m); }
#pragma unroll
        for (int i = 0; i < 4; ++i) { p0[i] = pk2(s[2 * i], s[2 * i + 1]); p1[i] = pk2(s[8 + 2 * i], s[9 + 2 * i]); }
        o = MFMA32(v0, __builtin_bit_cast(bf16x8, p0), o); o = MFMA32(v1, __builtin_bit_cast(bf16x8, p1), o);
    }
}

DI void attention(char* ws, lptr L, int head, int qb, int w, int rep, u64 tag) {
    const int lane = otid() & 63, h = lane >> 5;
    {
        const int l = lane & 31, blk = l < 16 ? 1 + (l >> 3) : l < 24 ? 1 : 0, mtp = l < 16 ? 4 * w + (l & 3) : l < 24 ? 0 : qb;
        poll1(F1P(rep * 224 + ((448 * blk + 28 * head + 27 * ((l >> 2) & 1)) / 96) * 16 + mtp), tag);
    }
    const rsrc_t rw = mkrsrc(ws, WS_BYTES);
    unsigned kv = WS_KVF + head * 64 * 4096;
    const unsigned lo = lane * 16;
    bf16x8 q0[4]; u32x4 q1[4];
    bf16x8 r0 = ld8(rw, lo, kv), r1 = ld8(rw, lo + 1024, kv);
#pragma unroll
    for (int t = 0; t < 4; ++t) { const unsigned qp = WS_QF + (head * 64 + qb * 4 + ((w + t) & 3)) * 2048;
        q0[t] = ld8(rw, lo, qp); q1[t] = ld4(rw, lo + 1024, qp); }
    kv += w * 16 * 4096;
    bf16x8 ka0 = ld8(rw, lo, kv), ka1 = ld8(rw, lo + 1024, kv), va0 = ld8(rw, lo + 2048, kv), va1 = ld8(rw, lo + 3072, kv);
    bf16x8 kb0 = ld8(rw, lo, kv + 4096), kb1 = ld8(rw, lo + 1024, kv + 4096), vb0, vb1;
    f32x16 zero, o[4], sA, sB;
    u32x4 p0, p1 = {0u, 0u, 0u, 0u};
#pragma unroll
    for (int i = 0; i < 16; ++i) zero[i] = 0.f;
#pragma unroll
    for (int i = 0; i < 8; ++i) vb1[i] = 0;
#pragma unroll
    for (int t = 0; t < 4; ++t) {
        q1[t][2] = h ? 0u : q1[t][2]; q1[t][3] = h ? 0u : q1[t][3];
        o[t] = MFMA32(r0, q0[t], zero); o[t] = MFMA32(r1, __builtin_bit_cast(bf16x8, q1[t]), o[t]);
    }
#pragma unroll
    for (int t = 0; t < 4; ++t) {
        float mx = fmaxf(o[t][0], o[t][1]);
#pragma unroll
        for (int i = 2; i < 16; i += 2) mx = fmaxf(fmaxf(mx, o[t][i]), o[t][i + 1]);
        mx = halfmax(mx);
        q1[t][2] = h ? pk2(-(mx + 88.0f), 0.f) : q1[t][2];
#pragma unroll
        for (int i = 0; i < 16; ++i) o[t][i] = 0.f;
    }
#define Q1(t) __builtin_bit_cast(bf16x8, q1[t])
#define PB(x) __builtin_bit_cast(bf16x8, x)
#define ASTEP(A, B, O, V0, OP, VP, K0, K1, QN) do { \
    B = MFMA32(K0, q0[QN], zero); SB(); \
    A[0] = EX2(A[0]); A[1] = EX2(A[1]); A[2] = EX2(A[2]); A[3] = EX2(A[3]); PIN(A); SB(); \
    OP = MFMA32(VP, PB(p1), OP); SB(); \
    p0[0] = pk2(A[0], A[1]); p0[1] = pk2(A[2], A[3]); SB(); A[4] = EX2(A[4]); A[5] = EX2(A[5]); A[6] = EX2(A[6]); A[7] = EX2(A[7]); PIN(A); PIN(p0); SB(); \
    B = MFMA32(K1, Q1(QN), B); SB(); \
    p0[2] = pk2(A[4], A[5]); p0[3] = pk2(A[6], A[7]); SB(); A[8] = EX2(A[8]); A[9] = EX2(A[9]); A[10] = EX2(A[10]); A[11] = EX2(A[11]); PIN(A); PIN(p0); SB(); \
    O = MFMA32(V0, PB(p0), O); SB(); \
    A[12] = EX2(A[12]); A[13] = EX2(A[13]); A[14] = EX2(A[14]); A[15] = EX2(A[15]); SB(); \
    p1[0] = pk2(A[8], A[9]); p1[1] = pk2(A[10], A[11]); SB(); p1[2] = pk2(A[12], A[13]); SB(); p1[3] = pk2(A[14], A[15]); PIN(p1); SB(); } while (0)
#define LDK(K0, K1, off) do { K0 = ld8(rw, lo, kv + (off)); K1 = ld8(rw, lo + 1024, kv + (off)); SB(); } while (0)
#define LDV(V0, V1, off) do { V0 = ld8(rw, lo + 2048, kv + (off)); V1 = ld8(rw, lo + 3072, kv + (off)); SB(); } while (0)
#define APAIR(MORE) do { \
    ASTEP(sA, sB, o[0], va0, o[3], vb1, ka0, ka1, 1); LDV(vb0, vb1, 4096); \
    ASTEP(sB, sA, o[1], va0, o[0], va1, ka0, ka1, 2); \
    ASTEP(sA, sB, o[2], va0, o[1], va1, ka0, ka1, 3); if (MORE) LDK(ka0, ka1, 8192); \
    ASTEP(sB, sA, o[3], va0, o[2], va1, kb0, kb1, 0); \
    ASTEP(sA, sB, o[0], vb0, o[3], va1, kb0, kb1, 1); if (MORE) LDV(va0, va1, 8192); \
    ASTEP(sB, sA, o[1], vb0, o[0], vb1, kb0, kb1, 2); \
    ASTEP(sA, sB, o[2], vb0, o[1], vb1, kb0, kb1, 3); if (MORE) LDK(kb0, kb1, 12288); \
    ASTEP(sB, sA, o[3], vb0, o[2], vb1, ka0, ka1, 0); kv += 8192; } while (0)
    sA = MFMA32(ka0, q0[0], zero); sA = MFMA32(ka1, Q1(0), sA);
    for (int it = 0; it < 7; ++it) APAIR(true);
    APAIR(false);
    o[3] = MFMA32(vb1, PB(p1), o[3]);
    lwait4(lcnt(L, C_PUB1_B));
    const lptr P = L + P2_AT;
#pragma unroll
    for (int t = 1; t < 4; ++t)
#pragma unroll
        for (int g = 0; g < 4; ++g) *(AS3 f32x4*)(P + (w * 3 + t - 1) * 4096 + g * 1024 + lo) = f32x4{o[t][4 * g], o[t][4 * g + 1], o[t][4 * g + 2], o[t][4 * g + 3]};
    role_sync(lcnt(L, C_SYNC_A));
#pragma unroll
    for (int t = 1; t < 4; ++t)
#pragma unroll
        for (int g = 0; g < 4; ++g) { f32x4 v = *(const AS3 f32x4*)(P + (((w - t) & 3) * 3 + t - 1) * 4096 + g * 1024 + lo);
#pragma unroll
            for (int c = 0; c < 4; ++c) o[0][4 * g + c] = sadd(o[0][4 * g + c], v[c]); }
    float chk = 0.f;
#pragma unroll
    for (int i = 0; i < 16; ++i) chk = __builtin_fmaf(o[0][i], 0.f, chk);
    float big = fmaxf(fabsf(o[0][0]), fabsf(o[0][1]));
#pragma unroll
    for (int i = 2; i < 12; i += 2) big = fmaxf(fmaxf(big, fabsf(o[0][i])), fabsf(o[0][i + 1]));
    const float l = hihalf(o[0][12]);
    const bool bad = !(chk == 0.f) || !(l > 0x1p-96f) || !(l < 0x1p120f) || !(halfmax(big) > 0x1p-104f);
    if (__builtin_expect(__any(bad), 0)) { attn_safe(ws, o[0], head, qb * 4 + w); SB(); asm volatile("s_nop 15" : "+v"(o[0])); }
    attn_store(ws, o[0], head, qb * 4 + w);
    publish(lcnt(L, C_PUB2_A), 4, (gu64*)(ws + WS_F2) + qb * 384 + head * 4, 2, 192, tag);
}

constexpr int g_extra(int s) { return s == 7 ? 1 : s == 10 ? 7 : 0; }
constexpr int g_issued_before(int s, int f) {
    int n = 14;
    for (int t = 0; t <= s; ++t) {
        n += g_extra(t);
        for (int g = 0; g < 7; ++g) {
            if (t == s && g == f) return n;
            if (t + 2 < 16) ++n;
            if (!(t & 1) && (t >> 1) + 2 < 8) n += g == 6 ? 2 : 1;
        }
    }
    return n;
}
constexpr int g_pos_z(int s, int f) { return s < 2 ? 7 * s + f + 1 : g_issued_before(s - 2, f) + 1; }
constexpr int g_pos_w(int ds) { return g_issued_before(2 * (ds - 2) + 1, 0) - g_extra(2 * (ds - 2) + 1); }
template <int N> DI void vm_wait_for(bf16x8& v) { asm volatile("s_waitcnt vmcnt(%1)" : "+v"(v) : "i"(N)); }
template <int OFF> DI void abld(bf16x8& d, unsigned voff, const u32x4& rs, unsigned soff) {
    asm volatile("buffer_load_dwordx4 %0, %1, %2, %3 offen offset:%4 sc1" : "=v"(d) : "v"(voff), "s"(rs), "s"(soff), "i"(OFF));
}
template <int OFF> DI void abld_rw(bf16x8& d, unsigned voff, const u32x4& rs, unsigned soff) {
    asm volatile("buffer_load_dwordx4 %0, %1, %2, %3 offen offset:%4 sc1" : "+v"(d) : "v"(voff), "s"(rs), "s"(soff), "i"(OFF));
}

template <int S> struct GStep {
    template <class C> static DI void run(C& c) {
        constexpr int ks = S & 1;
        if constexpr (S + 1 < 16) {
            constexpr int nds = (S + 1) >> 1;
            if constexpr (ks == 1 && nds >= 2) vm_wait<g_issued_before(S, 0) - g_extra(S) - g_pos_w(nds)>();
#pragma unroll
            for (int t = 0; t < 4; ++t) c.wf[(S + 1) & 1][t] = *(const AS3 bf16x8*)(c.W + (nds % 3) * 8192 + t * 2048 + (ks ? c.rd0 : c.rd1));
        }
        if constexpr (S == 7) asm volatile("buffer_load_dwordx2 %0, %1, %2, 0 offen sc1" : "=v"(c.flag) : "v"(c.fo), "s"(c.rw));
        if constexpr (S == 10) c.epi_loads();
        SB();
        group<0>(c); group<1>(c); group<2>(c); group<3>(c); group<4>(c); group<5>(c); group<6>(c);
        if constexpr (S + 1 < 16 && S != 9) GStep<S + 1>::run(c);
    }
    template <int F, class C> static DI void group(C& c) {
        constexpr int ds = S >> 1, ks = S & 1;
        vm_wait_for<g_issued_before(S, F) - g_pos_z(S, F)>(c.z[S & 1][F]);
#pragma unroll
        for (int t = 0; t < 4; ++t) c.acc[F][t] = MFMA16(c.z[S & 1][F], c.wf[S & 1][t], c.acc[F][t]);
        SB();
        if constexpr (S + 2 < 16) { constexpr int u = (S + 2) * 7 + F; abld_rw<(u & 3) * 1024>(c.z[S & 1][F], c.lo, c.rw, c.zsrc + (u >> 2) * 4096); }
        if constexpr (ks == 0 && ds + 2 < 8) { c.gw.template piece<ds + 2, F>(); if constexpr (F == 6) c.gw.template piece<ds + 2, 7>(); }
        if constexpr (S == 15 && F > 0) {
#pragma unroll
            for (int t = 1; t < 4; ++t) *(AS3 f32x4*)(c.W + ((t - 1) * 7 + F - 1) * 1024 + c.lo) = c.acc[F > 0 ? F - 1 : 0][t];
        }
        SB();
    }
};
struct GCtx {
    lptr W; unsigned lo, zsrc, zo, fo; int rd0, rd1; GW gw; u32x4 rw;
    f32x4 acc[7][4]; bf16x8 z[2][7], wf[2][4]; u32x2 z1[7], flag;
    DI void epi_loads() {
        asm volatile("buffer_load_dwordx2 %0, %4, %5, 0 offen sc1\n\tbuffer_load_dwordx2 %1, %4, %5, 0 offen offset:1024 sc1\n\tbuffer_load_dwordx2 %2, %4, %5, 0 offen offset:2048 sc1\n\tbuffer_load_dwordx2 %3, %4, %5, 0 offen offset:3072 sc1"
                     : "=&v"(z1[0]), "=&v"(z1[1]), "=&v"(z1[2]), "=&v"(z1[3]) : "v"(zo), "s"(rw));
        asm volatile("buffer_load_dwordx2 %0, %3, %4, %5 offen sc1\n\tbuffer_load_dwordx2 %1, %3, %4, %5 offen offset:1024 sc1\n\tbuffer_load_dwordx2 %2, %3, %4, %5 offen offset:2048 sc1"
                     : "=&v"(z1[4]), "=&v"(z1[5]), "=&v"(z1[6]) : "v"(zo), "s"(rw), "s"(4096u));
    }
};

DI void spatial(const char* wsp, const unsigned short* bsp, char* ws, lptr L, int mi, int j, int w, int rep, int z1flag, u64 tag) {
    const int lane = otid() & 63, r = lane & 15, kq = lane >> 4;
    GCtx c;
    c.W = L + w * P2_GP; c.gw = make_gw(wsp, L, mi, w); c.lo = lane * 16;
    c.rw = rawrsrc(ws, WS_BYTES);
    c.zsrc = WS_Z2N + (j * 64 + w * 16) * 7 * 1024;
    c.rd0 = r * 128 + ((kq ^ (r >> 1)) << 4); c.rd1 = c.rd0 ^ 64;
    const int tok = mi * 64 + w * 16 + r, tt = mi * 2 + (w >> 1), cgp = min((7 * j) / 4 + kq, (7 * j + 6) / 4);
    c.zo = WS_ZF + (tt * 56 + 7 * j) * 1024 + ((kq & 1) * 32 + (tok & 31)) * 16 + (kq >> 1) * 8;
    float bias = bflo(bsp[tok]); PIN(bias);
    c.fo = lane < 32 ? WS_F1 + (4096 + min((7 * j) / 4 + (lane & 3), (7 * j + 6) / 4) * 16 + (mi >> 1)) * 8 * 4 : WS_FH + (rep * 32 + lane - 32) * 8;
#pragma unroll
    for (int a = 0; a < 7; ++a)
#pragma unroll
        for (int b = 0; b < 4; ++b) c.acc[a][b] = f32x4{0.f, 0.f, 0.f, 0.f};
    if (z1flag >= 0) publish(lcnt(L, C_PUB1_Z), 4, F1P(4096 + z1flag), 1, 0, tag); else vm_wait<0>();
#pragma unroll
    for (int t = 0; t < 4; ++t) c.wf[0][t] = *(const AS3 bf16x8*)(c.W + t * 2048 + c.rd0);
    poll1(F1P(2048 + rep * 224 + cgp * 16 + 4 * w + (r & 3)), tag);
    vm_wait<0>();
#define ZP1(s, f) abld<(((s) * 7 + (f)) & 3) * 1024>(c.z[s][f], c.lo, c.rw, c.zsrc + (((s) * 7 + (f)) >> 2) * 4096)
#define ZPRO(s) ZP1(s, 0); ZP1(s, 1); ZP1(s, 2); ZP1(s, 3); ZP1(s, 4); ZP1(s, 5); ZP1(s, 6)
    ZPRO(0); ZPRO(1);
    SB();
    __builtin_amdgcn_s_setprio(0);
    GStep<0>::run(c);
    asm volatile("" : "+v"(c.flag));
    if (__builtin_expect(!__all(c.flag[0] == (unsigned)tag && c.flag[1] == (unsigned)(tag >> 32)), 0)) { gu64* f = (gu64*)(ws + c.fo); poll2(f, f, tag); }
    if (lane == 0) __hip_atomic_store(lcnt(L, C_FH), 1u, RLX_WG);
    SB();
    GStep<10>::run(c);
    __builtin_amdgcn_s_setprio(2);
#pragma unroll
    for (int t = 1; t < 4; ++t) *(AS3 f32x4*)(c.W + ((t - 1) * 7 + 6) * 1024 + c.lo) = c.acc[6][t];
    role_sync(lcnt(L, C_SYNC_B));
    asm volatile("s_waitcnt vmcnt(0)" : "+v"(c.z1[0]), "+v"(c.z1[1]), "+v"(c.z1[2]), "+v"(c.z1[3]), "+v"(c.z1[4]), "+v"(c.z1[5]), "+v"(c.z1[6]));
    u32x2 o[8];
#pragma unroll
    for (int f = 0; f < 7; ++f) {
        f32x4 g = c.acc[f][0];
#pragma unroll
        for (int t = 1; t < 4; ++t) { const f32x4 v = *(const AS3 f32x4*)(L + ((w - t) & 3) * P2_GP + ((t - 1) * 7 + f) * 1024 + c.lo);
#pragma unroll
            for (int e = 0; e < 4; ++e) g[e] = sadd(g[e], v[e]); }
        o[f] = u32x2{pk2(smul(sadd(g[0], bias), bflo(c.z1[f][0])), smul(sadd(g[1], bias), bfhi(c.z1[f][0]))), pk2(smul(sadd(g[2], bias), bflo(c.z1[f][1])), smul(sadd(g[3], bias), bfhi(c.z1[f][1])))};
    }
    o[7] = o[6];
    const rsrc_t rws = mkrsrc(ws, WS_BYTES);
    const unsigned ho = WS_AF + (tt * 88 + 32 + 7 * j) * 1024 + ((kq & 1) * 32 + (tok & 31)) * 16 + (kq >> 1) * 1024;
#pragma unroll
    for (int f = 0; f < 8; f += 2) {
        const auto a = __builtin_amdgcn_permlane32_swap(o[f][0], o[f + 1][0], false, false), b = __builtin_amdgcn_permlane32_swap(o[f][1], o[f + 1][1], false, false);
        if (f < 6 || lane < 32) st4(rws, ho + f * 1024, u32x4{a[0], b[0], a[1], b[1]});
    }
    publish(lcnt(L, C_PUB2_B), 4, (gu64*)(ws + WS_F2) + mi * 192 + 64 + j * 16, 1, 0, tag);
}

constexpr int P3_ROW = 272, P3_TILE = 64 * P3_ROW;
DI void outproj(const char* x, char* out, char* ws, lptr L, int mi, int nj, int w, u64 tag) {
    const int tid = otid(), lane = tid & 63, r = lane & 31, h = lane >> 5;
    const rsrc_t rw = mkrsrc(ws, WS_BYTES);
    const unsigned lo = lane * 16;
    f32x16 acc[2][2];
#pragma unroll
    for (int i = 0; i < 16; ++i) { acc[0][0][i] = 0.f; acc[0][1][i] = 0.f; acc[1][0][i] = 0.f; acc[1][1][i] = 0.f; }
    auto part = [&](auto NN, int ks0, int f0, int mask) { constexpr int N = NN;
        const unsigned bo = WS_BF + (nj * 2 * 88 + ks0) * 1024, ao = WS_AF + (mi * 2 * 88 + ks0) * 1024;
        bf16x8 a[N][2], b[N][2];
#pragma unroll
        for (int s = 0; s < N; ++s) { b[s][0] = ld8(rw, lo + (s & 3) * 1024, bo + (s >> 2) * 4096); b[s][1] = ld8(rw, lo + (s & 3) * 1024, bo + 88 * 1024 + (s >> 2) * 4096); }
        SB();
        gu64* f = (gu64*)(ws + WS_F2) + mi * 192 + f0 + (lane & mask) * 4;
        for (int spin = 0; spin < SPIN_CAP; ++spin) { if (__all(__hip_atomic_load(f, RLX_AGENT) == tag)) break; __builtin_amdgcn_s_sleep(4); }
        asm volatile("" ::: "memory");
#pragma unroll
        for (int s = 0; s < N; ++s) { a[s][0] = ld8(rw, lo + (s & 3) * 1024, ao + (s >> 2) * 4096); a[s][1] = ld8(rw, lo + (s & 3) * 1024, ao + 88 * 1024 + (s >> 2) * 4096); }
        SB();
#pragma unroll
        for (int s = 0; s < N; ++s) {
            acc[0][0] = MFMA32(b[s][0], a[s][0], acc[0][0]); acc[0][1] = MFMA32(b[s][1], a[s][0], acc[0][1]);
            acc[1][0] = MFMA32(b[s][0], a[s][1], acc[1][0]); acc[1][1] = MFMA32(b[s][1], a[s][1], acc[1][1]);
        }
        PIN(acc[0][0]); PIN(acc[0][1]); PIN(acc[1][0]); PIN(acc[1][1]);
        SB();
    };
    wait_ready(lcnt(L, C_FH));
    const u32x4 xr = *(const u32x4*)(x + (unsigned)((mi * 64 + (tid >> 3)) * 896 + nj * 128 + (tid & 7) * 16));
    if (w < 4) part(std::integral_constant<int, 8>{}, 8 * w, 16 * w, 3);
    part(std::integral_constant<int, 7>{}, 32 + 7 * w, 64 + 16 * w, 0);
    lwait4(lcnt(L, C_PUB2_A)); lwait4(lcnt(L, C_PUB2_B));
#pragma unroll
    for (int tm = 0; tm < 2; ++tm)
#pragma unroll
        for (int tn = 0; tn < 2; ++tn)
#pragma unroll
            for (int g = 0; g < 4; ++g)
                *(AS3 f32x4*)(L + w * P3_TILE + (tm * 32 + r) * P3_ROW + (tn * 32 + 8 * g + 4 * h) * 4) =
                    f32x4{acc[tm][tn][4 * g], acc[tm][tn][4 * g + 1], acc[tm][tn][4 * g + 2], acc[tm][tn][4 * g + 3]};
    __syncthreads();
    const int row = tid >> 3, c8 = tid & 7;
    f32x4 s0 = {0.f, 0.f, 0.f, 0.f}, s1 = s0;
#pragma unroll
    for (int p = 0; p < 8; ++p) { s0 += *(const AS3 f32x4*)(L + p * P3_TILE + row * P3_ROW + c8 * 32); s1 += *(const AS3 f32x4*)(L + p * P3_TILE + row * P3_ROW + c8 * 32 + 16); }
    const u32x4 o = {pk2(s0[0] + bflo(xr[0]), s0[1] + bfhi(xr[0])), pk2(s0[2] + bflo(xr[1]), s0[3] + bfhi(xr[1])),
                     pk2(s1[0] + bflo(xr[2]), s1[1] + bfhi(xr[2])), pk2(s1[2] + bflo(xr[3]), s1[3] + bfhi(xr[3]))};
    st4(mkrsrc(out, 2048 * 896), (mi * 64 + row) * 896 + nj * 128 + c8 * 16, o);
}

__global__ __launch_bounds__(512, 2) void fused_kernel(const char* x, const char* wqkv, const char* win, const unsigned short* wo, const unsigned short* wout,
                                                       const char* wsp, const unsigned short* bsp, char* out, char* ws) {
    extern __shared__ __attribute__((aligned(16))) char smem[];
    u64 pc0; unsigned clen;
    asm volatile("s_getpc_b64 %0\nLcode_begin_%=:\n\ts_mov_b32 %1, Lcode_end_mark - Lcode_begin_%=" : "=s"(pc0), "=s"(clen));
    const lptr L = (lptr)smem;
    const int bid = blockIdx.x, xcd = bid & 7, idx = bid >> 3, lane = threadIdx.x & 63;
    const int wid = __builtin_amdgcn_readfirstlane(threadIdx.x >> 6);
    const unsigned lds0 = (unsigned)(uintptr_t)L;
    const int i7 = (idx * 37) >> 8, m7 = idx - 7 * i7;
    const int mt = (xcd >> 1) * 4 + i7, cg = (xcd & 1) * 7 + m7;
    P1Src src;
    {   const unsigned rowo = (mt * 128 + 16 * wid + (lane >> 3)) * 896;
        src.voA0 = rowo + (((lane & 7) ^ (lane >> 4)) << 4); src.voA1 = rowo + 8 * 896 + (((lane & 7) ^ (4 + (lane >> 4))) << 4);
        const int kr8 = 8 * wid + (lane >> 3), c8 = ((lane & 7) ^ (((lane >> 4) & 1) << 2)) << 4;
        src.voZ = kr8 * 3584 + cg * 128 + c8; src.voQ = kr8 * 2688 + ((cg * 192) & ~127) + c8;
        src.ldsx = lds0 + wid * 2048; src.ldsw = lds0 + wid * 1024;
        src.rx = rawrsrc(x, 2048 * 896); src.rq = rawrsrc(wqkv, 448 * 2688); src.rz = rawrsrc(win, 448 * 3584);
        if (idx < 28) p1_stage<0>(src);
        SB();
    }
    unsigned epoch;
    asm volatile("s_load_dword %0, %1, 0x0\n\ts_waitcnt lgkmcnt(0)" : "=s"(epoch) : "s"(ws + WS_EP) : "memory");
    const u64 tag = (u64)(epoch * 0x9E3779B1u + 0x7F4A7C15u) | ((u64)(epoch * 0x85EBCA6Bu + 0x1B873593u) << 32);
    if (threadIdx.x < 32) *lcnt(L, threadIdx.x) = 0u;
    const int mi = (xcd >> 1) * 8 + (idx >> 2), j = (xcd & 1) * 4 + (idx & 3);
    if (idx < 28) {
        if (wid >= 4) phase1_z(src, wsp, mi, ws, L, mt, cg, wid, tag);
        else phase1_qkv(src, ws, L, mt, cg, wid, tag);
    } else {
        {
            const rsrc_t rc = mkrsrc((const void*)pc0, clen);
            u32x4 t[3];
#pragma unroll
            for (int i = 0; i < 3; ++i) t[i] = __builtin_amdgcn_raw_buffer_load_b128(rc, (i * 32 + (idx - 28) * 8 + wid) * 1024 + lane * 16, 0, 0);
#pragma unroll
            for (int i = 0; i < 3; ++i) asm volatile("" :: "v"(t[i]));
        }
        __syncthreads();
        helper(wo, wout, wsp, ws, L, xcd * 4 + idx - 28, wid, tag);
        if (wid < 4) larrive(lcnt(L, C_PUB1_B));
        else {
            const GW gw = make_gw(wsp, L, mi, wid - 4); gw.tile<0>(); gw.tile<1>();
        }
    }
    if (wid < 4) attention(ws, L, 2 * xcd + (idx >> 4), idx & 15, wid, xcd, tag);
    else spatial(wsp, bsp, ws, L, mi, j, wid - 4, xcd, idx < 28 ? cg * 16 + mt : -1, tag);
    if (idx < 28) outproj(x, out, ws, L, xcd * 4 + i7, m7, wid, tag);
    else if (bid == 255 && wid == 0) {
        const int ln = otid();
        gu64* f = (gu64*)(ws + WS_F2) + (ln >> 4) * 384 + (ln & 15) * 4;
        poll2(f, f + 1536, tag); poll2(f + 3072, f + 4608, tag);
        if (ln == 0) __hip_atomic_store((gu32*)(ws + WS_EP), epoch + 1u, RLX_AGENT);
    }
    asm volatile("Lcode_end_mark:");
}

extern "C" void kernel_launch(void* const* d_in, const int* in_sizes, int n_in, void* d_out, int out_size, void* d_ws, size_t ws_size, hipStream_t stream) {
    (void)hipFuncSetAttribute((const void*)fused_kernel, hipFuncAttributeMaxDynamicSharedMemorySize, LDS_BYTES);
    fused_kernel<<<256, 512, LDS_BYTES, stream>>>((const char*)d_in[0], (const char*)d_in[1], (const char*)d_in[4], (const unsigned short*)d_in[2], (const unsigned short*)d_in[7],
                                                  (const char*)d_in[5], (const unsigned short*)d_in[6], (char*)d_out, (char*)d_ws);
}
```
